```python
import torch

def _softmax(x, axis=-1):
    return torch.softmax(x, dim=axis)

def _gelu(x):
    return torch.nn.functional.gelu(x, approximate='tanh')

def _silu(x):
    return torch.nn.functional.silu(x)

def _sigmoid(x):
    return torch.sigmoid(x)

def _layernorm(x, axis=-1, eps=1e-05):
    m = x.mean(dim=axis, keepdim=True)
    v = x.var(dim=axis, keepdim=True, unbiased=False)
    return (x - m) / torch.sqrt(v + eps)

def _scatter_add(tgt, idx, src, axis=0):
    return tgt.clone().index_add_(axis, idx, src)

def _segment_sum(src, seg, n):
    out = torch.zeros((n,) + src.shape[1:], dtype=src.dtype)
    return out.index_add_(0, seg, src)

def _pad(x, pw):
    flat = tuple((v for pair in reversed(pw) for v in pair))
    return torch.nn.functional.pad(x, flat)
kernel_name = 'standalone__dilated_atrous__vision'

def setup_inputs():

    _w = lambda a: (lambda t: t.to(torch.bfloat16) if t.is_floating_point() else t)(torch.as_tensor(a))
    H = W = 224
    C = 96
    k = 7
    return {'x': _w((lambda *s: torch.randn(*s, dtype=torch.bfloat16) * 0.05)(1, H, W, C)), 'w': _w((lambda *s: torch.randn(*s, dtype=torch.bfloat16) * 0.05)(k, k, C, C) * (1.0 / (k * C ** 0.5)))}

def reference(x, w):
    B, H, W, C = x.shape
    k = 7
    rate = 2
    pad = (k - 1) * rate // 2
    xp = _pad(x, ((0, 0), (pad, pad), (pad, pad), (0, 0)))
    out = torch.zeros_like(x)
    for i in range(k):
        for j in range(k):
            tap = xp[:, i * rate:i * rate + H, j * rate:j * rate + W, :].clone()
            out = out + torch.einsum('bhwc,co->bhwo', tap, w[i, j])
    return out
```

```cpp
#include <hip/hip_runtime.h>
#include <hip/hip_bf16.h>
typedef __attribute__((ext_vector_type(8))) short bf16x8;
typedef __attribute__((ext_vector_type(4))) float f32x4;
typedef __attribute__((ext_vector_type(2))) float f32x2;
typedef __attribute__((ext_vector_type(2))) __bf16 bf16x2_t;
constexpr int H = 224, W = 224, C = 96, KS = 7;

__global__ void wt_kernel(const unsigned short* __restrict__ w, unsigned short* __restrict__ wt) {
  int i = blockIdx.x * 256 + threadIdx.x;
  if (i >= 49 * C * C) return;
  int c = i % C, o = (i / C) % C, t = i / (C * C);
  wt[i] = w[(t * C + c) * C + o];
}

__global__ __launch_bounds__(256) void conv_kernel(const unsigned short* __restrict__ x, const unsigned short* __restrict__ wt,
                                                   unsigned short* __restrict__ out) {
  int wave = (blockIdx.x * 256 + threadIdx.x) >> 6;
  int lane = threadIdx.x & 63;
  int row = wave / 14, seg = wave % 14;
  int px = seg * 16 + (lane & 15);
  int q = lane >> 4;
  f32x4 acc[6];
  for (int n = 0; n < 6; ++n) acc[n] = (f32x4){0, 0, 0, 0};
  for (int i = 0; i < KS; ++i) {
    int y = row + 2 * i - 6;
    if (y < 0 || y >= H) continue;
    for (int j = 0; j < KS; ++j) {
      int xx = px + 2 * j - 6;
      bool ok = xx >= 0 && xx < W;
      const unsigned short* xp = x + ((long)y * W + (ok ? xx : 0)) * C + q * 8;
      const unsigned short* wp = wt + ((long)(i * KS + j) * C + (lane & 15)) * C + q * 8;
#pragma unroll
      for (int kc = 0; kc < 3; ++kc) {
        bf16x8 b = *(const bf16x8*)(xp + kc * 32);
        if (!ok) b = (bf16x8){0, 0, 0, 0, 0, 0, 0, 0};
#pragma unroll
        for (int n = 0; n < 6; ++n) {
          bf16x8 a = *(const bf16x8*)(wp + n * 16 * C + kc * 32);
          acc[n] = __builtin_amdgcn_mfma_f32_16x16x32_bf16(a, b, acc[n], 0, 0, 0);
        }
      }
    }
  }
  unsigned short* op = out + ((long)row * W + px) * C + q * 4;
#pragma unroll
  for (int n = 0; n < 6; ++n) {
    f32x2 lo = {acc[n][0], acc[n][1]}, hi = {acc[n][2], acc[n][3]};
    bf16x2_t l = __builtin_convertvector(lo, bf16x2_t), h = __builtin_convertvector(hi, bf16x2_t);
    uint2 v = {__builtin_bit_cast(unsigned, l), __builtin_bit_cast(unsigned, h)};
    *(uint2*)(op + n * 16) = v;
  }
}

extern "C" void kernel_launch(void* const* d_in, const int* in_sizes, int n_in, void* d_out, int out_size, void* d_ws,
                              size_t ws_size, hipStream_t stream) {
  const unsigned short* x = (const unsigned short*)d_in[0];
  const unsigned short* w = (const unsigned short*)d_in[1];
  unsigned short* wt = (unsigned short*)d_ws;
  wt_kernel<<<(49 * C * C + 255) / 256, 256, 0, stream>>>(w, wt);
  conv_kernel<<<H * 14 / 4, 256, 0, stream>>>(x, wt, (unsigned short*)d_out);
}
```

```cpp
#include <hip/hip_runtime.h>
#include <hip/hip_bf16.h>
constexpr int LDS_TOTAL = 163328;
static __device__ const __attribute__((aligned(1024))) unsigned lane_tab[14 * 256] = {
  0xbf700000,0x40073f70,0xbef0be70,0x0,0x0,0x0,0xbf700000,0x40073f70,0x0,0x0,0x0,0x0,0x0,0x0,0x0,0x0,
  0x0,0x0,0x0,0x0,0xbea00000,0xbf0c3f70,0x3e20be70,0x0,0x0,0x0,0xbea00000,0xbf0c3f70,0x0,0x0,0x0,0x0,
  0x0,0x0,0x0,0x0,0x0,0x0,0x0,0x0,0xc0343fb4,0x4061bfe1,0xbf343eb4,0x0,0x0,0x0,0xc0343fb4,0x4061bfe1,
  0x0,0x0,0x0,0x0,0x0,0x0,0x0,0x0,0x0,0x0,0x0,0x0,0x3d200000,0xbda0bd70,0x3d203d70,0x0,
  0x0,0x0,0x3d200000,0xbda0bd70,0x0,0x0,0x0,0x0,0x0,0x0,0x0,0x0,0x0,0x0,0x0,0x0,
  0x3cc00000,0xbd70,0xbcc03d70,0x0,0x0,0x0,0x3cc00000,0xbd70,0x0,0x0,0x0,0x0,0x0,0x0,0x0,0x0,
  0x0,0x0,0x0,0x0,0x3f000000,0xbf200000,0x3e000000,0x0,0x0,0x0,0x3f000000,0xbf200000,0x0,0x0,0x0,0x0,
  0x0,0x0,0x0,0x0,0x0,0x0,0x0,0x0,0xc0340000,0x406140b4,0xbf34c0e1,0x3fb4,0x0,0x0,0xc0340000,0x406140b4,
  0x0,0x0,0x0,0x0,0xbef0be70,0x0,0x0,0x0,0xbf700000,0x40073f70,0xbef0be70,0x0,0x0,0x0,0xbf700000,0x40073f70,
  0x0,0x0,0x0,0x0,0x0,0x0,0x0,0x0,0x3e20be70,0x0,0x0,0x0,0xbea00000,0xbf0c3f70,0x3e20be70,0x0,
  0x0,0x0,0xbea00000,0xbf0c3f70,0x0,0x0,0xc0340000,0x406140b4,0x0,0x0,0x0,0x0,0xbf343eb4,0x0,0x0,0x0,
  0xc0343fb4,0x4061bfe1,0xbf343eb4,0x0,0x0,0x0,0xc0343fb4,0x4061bfe1,0x0,0x0,0x0,0x0,0x0,0x0,0x0,0x0,
  0x3d203d70,0x0,0x0,0x0,0x3d200000,0xbda0bd70,0x3d203d70,0x0,0x0,0x0,0x3d200000,0xbda0bd70,0x0,0x0,0x0,0x0,
  0x0,0x0,0x0,0x0,0xbcc03d70,0x0,0x0,0x0,0x3cc00000,0xbd70,0xbcc03d70,0x0,0x0,0x0,0x3cc00000,0xbd70,
  0xc0340000,0x406140b4,0xbf34c0e1,0x3fb4,0x0,0x0,0x0,0x0,0x3e000000,0x0,0x0,0x0,0x3f000000,0xbf200000,0x3e000000,0x0,
  0x0,0x0,0x3f000000,0xbf200000,0x0,0x0,0x0,0x0,0x0,0x0,0x0,0x0,0xbf34c0e1,0x3fb4,0x0,0x0,
  0x0,0x0,0x0,0x0,0x0,0x0,0x0,0x0,0x0,0x0,0x0,0x0,0x0,0x0,0x0,0x0,
  0x0,0x0,0x0,0x0,0x0,0x0,0x0,0x0,0x0,0x0,0x0,0x0,0x0,0x0,0x0,0x0,
  0x0,0x0,0x0,0x0,0x0,0x0,0x0,0x0,0x0,0x0,0x0,0x0,0x0,0x0,0x0,0x0,
  0x0,0x0,0x0,0x0,0x0,0x0,0x0,0x0,0x0,0x0,0x0,0x0,0x0,0x0,0x0,0x0,
  0x0,0x0,0x0,0x0,0x0,0x0,0x0,0x0,0x0,0x0,0x0,0x0,0x0,0x0,0x0,0x0,
  0x0,0x0,0x0,0x0,0x0,0x0,0x0,0x0,0x0,0x0,0x0,0x0,0x0,0x0,0x0,0x0,
  0x0,0x0,0x0,0x0,0x0,0x0,0x0,0x0,0x0,0x0,0x0,0x0,0x0,0x0,0x0,0x0,
  0x0,0x0,0x0,0x0,0x0,0x0,0x0,0x0,0x0,0x0,0x0,0x0,0x0,0x0,0x0,0x0,
  0x0,0x0,0x0,0x0,0x0,0x0,0x0,0x0,0x0,0x0,0x0,0x0,0x0,0xbef0be70,0x0,0x0,
  0x0,0xbf700000,0x40073f70,0xbef0be70,0x0,0x0,0x0,0x0,0x0,0x0,0x0,0x0,0x0,0x0,0x0,0x0,
  0x0,0x3e20be70,0x0,0x0,0x0,0xbf34c0e1,0x3fb4,0x0,0x0,0x0,0x0,0x0,0x0,0x0,0x0,0x0,
  0x0,0x0,0x0,0x0,0x0,0xbf343eb4,0x0,0x0,0x0,0xc0343fb4,0x4061bfe1,0xbf343eb4,0x0,0x0,0x0,0x0,
  0x0,0x0,0x0,0x0,0x0,0x0,0x0,0x0,0x0,0x3d203d70,0x0,0x0,0x0,0x3d200000,0xbda0bd70,0x3d203d70,
  0x0,0x0,0x0,0x0,0x0,0x0,0x0,0x0,0x0,0x0,0x0,0x0,0x0,0xbcc03d70,0x0,0x0,
  0x0,0x0,0x0,0x0,0x0,0x0,0x0,0x0,0x0,0x0,0x0,0x0,0x0,0x0,0x0,0x0,
  0x0,0x3e000000,0x0,0x0,0x0,0x3f000000,0xbf200000,0x3e000000,0x0,0x0,0x0,0x0,0x0,0x0,0x0,0x0,
  0x3f70bea0,0xbe70bf0c,0x3e20,0x0,0xbd703cc0,0x3d700000,0xbcc0,0x0,0x0,0x0,0xbea00000,0xbf0c3f70,0x0,0x0,0x3cc00000,0xbd70,
  0x0,0x0,0x0,0x0,0x0,0x0,0x0,0x0,0x0,0x0,0x0,0x0,0x0,0x0,0x0,0x0,
  0x0,0x0,0x0,0x0,0x0,0x0,0x0,0x0,0x0,0x0,0x0,0x0,0x0,0x0,0x0,0x0,
  0x0,0x0,0x0,0x0,0x0,0x0,0x0,0x0,0x0,0x0,0x0,0x0,0x0,0x0,0x0,0x0,
  0x0,0x0,0x0,0x0,0x0,0x0,0x0,0x0,0x0,0x0,0x0,0x0,0x0,0x0,0x0,0x0,
  0x0,0x0,0x0,0x0,0x0,0x0,0x0,0x0,0x0,0x0,0x0,0x0,0x0,0x0,0x0,0x0,
  0x0,0x0,0x0,0x0,0x0,0x0,0x0,0x0,0x0,0x0,0x0,0x0,0x0,0x0,0x0,0x0,
  0x0,0x0,0x0,0x0,0x0,0x0,0x0,0x0,0x0,0x0,0x0,0x0,0x0,0x0,0x0,0x0,
  0x0,0x0,0x0,0x0,0x0,0x0,0x0,0x0,0x3e20be70,0x0,0x0,0x0,0xbcc03d70,0x0,0x0,0x0,
  0x0,0x0,0x0,0x0,0x0,0x0,0x0,0x0,0x0,0x0,0x0,0x0,0x0,0x0,0x0,0x0,
  0x0,0x3f70bea0,0xbe70bf0c,0x3e20,0x0,0xbd703cc0,0x3d700000,0xbcc0,0x0,0x0,0x0,0x0,0x0,0x0,0x0,0x0,
  0x0,0x0,0x0,0x0,0x0,0x0,0x0,0x0,0x0,0x0,0x0,0x0,0x0,0x0,0x0,0x0,
  0x0,0x0,0x0,0x0,0x0,0x0,0x0,0x0,0x0,0x0,0x0,0x0,0x0,0x0,0x0,0x0,
  0x0,0x0,0x0,0x0,0x0,0x0,0x0,0x0,0x0,0x0,0x0,0x0,0x0,0x0,0x0,0x0,
  0x0,0x0,0x0,0x0,0x0,0x0,0x0,0x0,0x0,0x0,0x0,0x0,0x0,0x0,0x0,0x0,
  0x0,0x0,0x0,0x0,0x0,0x0,0x0,0x0,0x0,0x0,0x0,0x0,0x0,0x0,0x0,0x0,
  0x0,0x0,0x0,0x0,0x0,0x0,0x0,0x0,0x0,0x0,0x0,0x0,0x0,0x0,0x0,0x0,
  0x0,0x0,0x0,0x0,0x0,0x0,0x0,0x0,0x0,0x0,0x0,0x0,0x0,0x0,0x0,0x0,
  0x0,0x0,0x0,0x0,0x0,0x0,0x0,0x0,0x3f70bea0,0xbe70bf0c,0x3e20,0x0,0xbd703cc0,0x3d700000,0xbcc0,0x0,
  0x0,0x0,0x0,0x0,0x0,0x0,0x0,0x0,0x0,0x0,0x0,0x0,0x0,0x0,0x0,0x0,
  0x0,0x0,0xbea00000,0xbf0c3f70,0x0,0x0,0x3cc00000,0xbd70,0x0,0x0,0x0,0x0,0x0,0x0,0x0,0x0,
  0x0,0x0,0x0,0x0,0x0,0x0,0x0,0x0,0x0,0x0,0x0,0x0,0x0,0x0,0x0,0x0,
  0x0,0x0,0x0,0x0,0x0,0x0,0x0,0x0,0x0,0x0,0x0,0x0,0x0,0x0,0x0,0x0,
  0x0,0x0,0x0,0x0,0x0,0x0,0x0,0x0,0x0,0x0,0x0,0x0,0x0,0x0,0x0,0x0,
  0x0,0x0,0x0,0x0,0x0,0x0,0x0,0x0,0x0,0x0,0x0,0x0,0x0,0x0,0x0,0x0,
  0x0,0x0,0x0,0x0,0x0,0x0,0x0,0x0,0x0,0x0,0x0,0x0,0x0,0x0,0x0,0x0,
  0x0,0x0,0x0,0x0,0x0,0x0,0x0,0x0,0x0,0x0,0x0,0x0,0x0,0x0,0x0,0x0,
  0x0,0x0,0x0,0x0,0x0,0x0,0x0,0x0,0x0,0x0,0x0,0x0,0x0,0x0,0x0,0x0,
  0x3e20be70,0x0,0x0,0x0,0xbcc03d70,0x0,0x0,0x0,0x0,0x3f70bea0,0xbe70bf0c,0x3e20,0x0,0xbd703cc0,0x3d700000,0xbcc0,
  0x0,0x0,0x0,0x0,0x0,0x0,0x0,0x0,0x0,0x0,0x0,0x0,0x0,0x0,0x0,0x0,
  0x0,0x0,0x0,0x0,0x0,0x0,0x0,0x0,0x0,0x0,0x0,0x0,0x0,0x0,0x0,0x0,
  0x0,0x0,0x0,0x0,0x0,0x0,0x0,0x0,0x0,0x0,0x0,0x0,0x0,0x0,0x0,0x0,
  0x0,0x440,0x540,0x0,0x8,0x448,0x548,0x10,0x10,0x450,0x550,0x20,0x18,0x458,0x558,0x30,
  0x40,0x480,0xf40,0x40,0x48,0x488,0xf48,0x50,0x50,0x490,0xf50,0x60,0x58,0x498,0xf58,0x70,
  0x80,0x4c0,0xf80,0x80,0x88,0x4c8,0xf88,0x210,0x90,0x4d0,0xf90,0xa0,0x98,0x4d8,0xf98,0xb0,
  0xc0,0x500,0xfc0,0xc0,0xc8,0x508,0xfc8,0xd0,0xd0,0x510,0xfd0,0xe0,0xd8,0x518,0xfd8,0xf0,
  0x5a0,0x9e0,0xae0,0x100,0x5a8,0x9e8,0xae8,0x110,0x5b0,0x9f0,0xaf0,0x120,0x5b8,0x9f8,0xaf8,0x130,
  0x5e0,0xa20,0x14e0,0x140,0x5e8,0xa28,0x14e8,0x150,0x5f0,0xa30,0x14f0,0x160,0x5f8,0xa38,0x14f8,0x170,
  0x620,0xa60,0x1520,0x200,0x628,0xa68,0x1528,0x190,0x630,0xa70,0x1530,0x1a0,0x638,0xa78,0x1538,0x1b0,
  0x660,0xaa0,0x1560,0x1c0,0x668,0xaa8,0x1568,0x1d0,0x670,0xab0,0x1570,0x1e0,0x678,0xab8,0x1578,0x1f0,
  0x200,0x1000,0x1ac0,0x6200,0x208,0x1008,0x1ac8,0x6210,0x210,0x1010,0x1ad0,0x6220,0x218,0x1018,0x1ad8,0x6230,
  0x240,0x1040,0x1b00,0x6240,0x248,0x1048,0x1b08,0x6250,0x250,0x1050,0x1b10,0x6260,0x258,0x1058,0x1b18,0x6270,
  0x280,0x1a40,0x1b40,0x6280,0x288,0x1a48,0x1b48,0x6410,0x290,0x1a50,0x1b50,0x62a0,0x298,0x1a58,0x1b58,0x62b0,
  0x2c0,0x1a80,0x1b40,0x62c0,0x2c8,0x1a88,0x1b48,0x62d0,0x2d0,0x1a90,0x1b50,0x62e0,0x2d8,0x1a98,0x1b58,0x62f0,
  0x7a0,0x15a0,0x2060,0x6300,0x7a8,0x15a8,0x2068,0x6310,0x7b0,0x15b0,0x2070,0x6320,0x7b8,0x15b8,0x2078,0x6330,
  0x7e0,0x15e0,0x20a0,0x6340,0x7e8,0x15e8,0x20a8,0x6350,0x7f0,0x15f0,0x20b0,0x6360,0x7f8,0x15f8,0x20b8,0x6370,
  0x820,0x1fe0,0x20e0,0x6400,0x828,0x1fe8,0x20e8,0x6390,0x830,0x1ff0,0x20f0,0x63a0,0x838,0x1ff8,0x20f8,0x63b0,
  0x860,0x2020,0x20e0,0x63c0,0x868,0x2028,0x20e8,0x63d0,0x870,0x2030,0x20f0,0x63e0,0x878,0x2038,0x20f8,0x63f0,
  0x90,0x90,0x0,0x1e0,0x180,0x180,0x10,0x1f0,0x510,0x510,0x20,0x200,0x600,0x600,0x30,0x210,
  0x28040,0x28040,0x1440,0x1620,0x28050,0x28050,0x1450,0x1630,0x28060,0x28060,0x1460,0x1640,0x28070,0x28070,0x1470,0x1650,
  0x990,0x990,0x2880,0x2a60,0xa80,0xa80,0x2890,0x2a70,0xe10,0x280a0,0x28a0,0x2a80,0xf00,0x280b0,0x28b0,0x2a90,
  0x280c0,0x280c0,0x3cc0,0x3ea0,0x280d0,0x280d0,0x3cd0,0x3eb0,0x280e0,0x280e0,0x3ce0,0x3ec0,0x280f0,0x280f0,0x3cf0,0x3ed0,
  0x1290,0xe10,0x6200,0x63e0,0x1380,0xf00,0x6210,0x63f0,0x1710,0x1290,0x6220,0x6400,0x1800,0x1380,0x6230,0x6410,
  0x28140,0x28140,0x7640,0x7820,0x28150,0x28150,0x7650,0x7830,0x28160,0x28160,0x7660,0x7840,0x28170,0x28170,0x7670,0x7850,
  0x28180,0x28180,0x8a80,0x8c60,0x28190,0x28190,0x8a90,0x8c70,0x281a0,0x281a0,0x8aa0,0x8c80,0x281b0,0x281b0,0x8ab0,0x8c90,
  0x281c0,0x281c0,0x9ec0,0xa0a0,0x281d0,0x281d0,0x9ed0,0xa0b0,0x281e0,0x281e0,0x9ee0,0xa0c0,0x281f0,0x281f0,0x9ef0,0xa0d0,
  0x6290,0x6290,0x10,0x420,0x6380,0x6380,0x20,0x430,0x6710,0x6710,0x30,0x440,0x6800,0x6800,0x40,0x450,
  0x28040,0x28040,0x1450,0x1860,0x28050,0x28050,0x1460,0x1870,0x28060,0x28060,0x1470,0x1880,0x28070,0x28070,0x1480,0x1890,
  0x6b90,0x6b90,0x2890,0x2ca0,0x6c80,0x6c80,0x28a0,0x2cb0,0x7010,0x280a0,0x28b0,0x2cc0,0x7100,0x280b0,0x28c0,0x2cd0,
  0x280c0,0x280c0,0x3cd0,0x40e0,0x280d0,0x280d0,0x3ce0,0x40f0,0x280e0,0x280e0,0x3cf0,0x4100,0x280f0,0x280f0,0x3d00,0x4110,
  0x7490,0x7010,0x6210,0x6620,0x7580,0x7100,0x6220,0x6630,0x7910,0x7490,0x6230,0x6640,0x7a00,0x7580,0x6240,0x6650,
  0x28140,0x28140,0x7650,0x7a60,0x28150,0x28150,0x7660,0x7a70,0x28160,0x28160,0x7670,0x7a80,0x28170,0x28170,0x7680,0x7a90,
  0x28180,0x28180,0x8a90,0x8ea0,0x28190,0x28190,0x8aa0,0x8eb0,0x281a0,0x281a0,0x8ab0,0x8ec0,0x281b0,0x281b0,0x8ac0,0x8ed0,
  0x281c0,0x281c0,0x9ed0,0xa2e0,0x281d0,0x281d0,0x9ee0,0xa2f0,0x281e0,0x281e0,0x9ef0,0xa300,0x281f0,0x281f0,0x9f00,0xa310,
  0x3f803f80,0x3f803f80,0x40003f80,0x41004080,0xbf803f80,0xbf803f80,0xc0003f80,0xc1004080,0x3f80,0x0,0x40804100,0x3f804000,0x0,0x0,0x0,0x0,
  0x3f803f80,0x3f803f80,0x40003f80,0x41004080,0xbf803f80,0xbf803f80,0xc0003f80,0xc1004080,0x3f80,0x0,0x40804100,0x3f804000,0x0,0x0,0x0,0x0,
  0x3f803f80,0x3f803f80,0x40003f80,0x41004080,0xbf803f80,0xbf803f80,0xc0003f80,0xc1004080,0x3f80,0x0,0x40804100,0x3f804000,0x0,0x0,0x0,0x0,
  0x3f803f80,0x3f803f80,0x40003f80,0x41004080,0xbf803f80,0xbf803f80,0xc0003f80,0xc1004080,0x3f80,0x0,0x40804100,0x3f804000,0x0,0x0,0x0,0x0,
  0x3f803f80,0x3f803f80,0x40003f80,0x41004080,0xbf803f80,0xbf803f80,0xc0003f80,0xc1004080,0x3f80,0x0,0x40804100,0x3f804000,0x0,0x0,0x0,0x0,
  0x3f803f80,0x3f803f80,0x40003f80,0x41004080,0xbf803f80,0xbf803f80,0xc0003f80,0xc1004080,0x3f80,0x0,0x40804100,0x3f804000,0x0,0x0,0x0,0x0,
  0x3f803f80,0x3f803f80,0x40003f80,0x41004080,0xbf803f80,0xbf803f80,0xc0003f80,0xc1004080,0x3f80,0x0,0x40804100,0x3f804000,0x0,0x0,0x0,0x0,
  0x3f803f80,0x3f803f80,0x40003f80,0x41004080,0xbf803f80,0xbf803f80,0xc0003f80,0xc1004080,0x3f80,0x0,0x40804100,0x3f804000,0x0,0x0,0x0,0x0,
  0x3f803f80,0x3f80,0x40003f80,0x4080,0xbf803f80,0x3f80,0xc0003f80,0x4080,0x3f80,0x0,0x40804100,0x4000,0x0,0x0,0x0,0x0,
  0x3f803f80,0x3f80,0x40003f80,0x4080,0xbf803f80,0x3f80,0xc0003f80,0x4080,0x3f80,0x0,0x40804100,0x4000,0x0,0x0,0x0,0x0,
  0x3f803f80,0x3f80,0x40003f80,0x4080,0xbf803f80,0x3f80,0xc0003f80,0x4080,0x3f80,0x0,0x40804100,0x4000,0x0,0x0,0x0,0x0,
  0x3f803f80,0x3f80,0x40003f80,0x4080,0xbf803f80,0x3f80,0xc0003f80,0x4080,0x3f80,0x0,0x40804100,0x4000,0x0,0x0,0x0,0x0,
  0x3f803f80,0x3f80,0x40003f80,0x4080,0xbf803f80,0x3f80,0xc0003f80,0x4080,0x3f80,0x0,0x40804100,0x4000,0x0,0x0,0x0,0x0,
  0x3f803f80,0x3f80,0x40003f80,0x4080,0xbf803f80,0x3f80,0xc0003f80,0x4080,0x3f80,0x0,0x40804100,0x4000,0x0,0x0,0x0,0x0,
  0x3f803f80,0x3f80,0x40003f80,0x4080,0xbf803f80,0x3f80,0xc0003f80,0x4080,0x3f80,0x0,0x40804100,0x4000,0x0,0x0,0x0,0x0,
  0x3f803f80,0x3f80,0x40003f80,0x4080,0xbf803f80,0x3f80,0xc0003f80,0x4080,0x3f80,0x0,0x40804100,0x4000,0x0,0x0,0x0,0x0,
  0xc400,0xd0c0,0x178c0,0x0,0xc408,0xd0c8,0x178c8,0x0,0xc410,0xd0d0,0x178d0,0x0,0xc418,0xd0d8,0x178d8,0x0,
  0xc840,0xd080,0x17880,0x0,0xc848,0xd088,0x17888,0x0,0xc850,0xd090,0x17890,0x0,0xc858,0xd098,0x17898,0x0,
  0xcc80,0xd040,0x17840,0x0,0xcc88,0xd048,0x17848,0x0,0xcc90,0xd050,0x17850,0x0,0xcc98,0xd058,0x17858,0x0,
  0xd0c0,0xd000,0x17800,0x0,0xd0c8,0xd008,0x17808,0x0,0xd0d0,0xd010,0x17810,0x0,0xd0d8,0xd018,0x17818,0x0,
  0xc520,0xd1e0,0x179e0,0x0,0xc528,0xd1e8,0x179e8,0x0,0xc530,0xd1f0,0x179f0,0x0,0xc538,0xd1f8,0x179f8,0x0,
  0xc960,0xd1a0,0x179a0,0x0,0xc968,0xd1a8,0x179a8,0x0,0xc970,0xd1b0,0x179b0,0x0,0xc978,0xd1b8,0x179b8,0x0,
  0xcda0,0xd160,0x17960,0x0,0xcda8,0xd168,0x17968,0x0,0xcdb0,0xd170,0x17970,0x0,0xcdb8,0xd178,0x17978,0x0,
  0xd1e0,0xd120,0x17920,0x0,0xd1e8,0xd128,0x17928,0x0,0xd1f0,0xd130,0x17930,0x0,0xd1f8,0xd138,0x17938,0x0,
  0xd400,0xecc0,0x178c0,0x0,0xd408,0xecc8,0x178c8,0x0,0xd410,0xecd0,0x178d0,0x0,0xd418,0xecd8,0x178d8,0x0,
  0xd840,0xec80,0x17880,0x0,0xd848,0xec88,0x17888,0x0,0xd850,0xec90,0x17890,0x0,0xd858,0xec98,0x17898,0x0,
  0xdc80,0xec40,0x17840,0x0,0xdc88,0xec48,0x17848,0x0,0xdc90,0xec50,0x17850,0x0,0xdc98,0xec58,0x17858,0x0,
  0xdc80,0xec00,0x17800,0x0,0xdc88,0xec08,0x17808,0x0,0xdc90,0xec10,0x17810,0x0,0xdc98,0xec18,0x17818,0x0,
  0xd520,0xede0,0x179e0,0x0,0xd528,0xede8,0x179e8,0x0,0xd530,0xedf0,0x179f0,0x0,0xd538,0xedf8,0x179f8,0x0,
  0xd960,0xeda0,0x179a0,0x0,0xd968,0xeda8,0x179a8,0x0,0xd970,0xedb0,0x179b0,0x0,0xd978,0xedb8,0x179b8,0x0,
  0xdda0,0xed60,0x17960,0x0,0xdda8,0xed68,0x17968,0x0,0xddb0,0xed70,0x17970,0x0,0xddb8,0xed78,0x17978,0x0,
  0xdda0,0xed20,0x17920,0x0,0xdda8,0xed28,0x17928,0x0,0xddb0,0xed30,0x17930,0x0,0xddb8,0xed38,0x17938,0x0,
  0x0,0x0,0x0,0x0,0x40,0x10,0x10,0x0,0x90,0x20,0x20,0x0,0xd0,0x30,0x30,0x0,
  0x120,0x40,0x600,0x0,0x160,0x50,0x610,0x0,0x1b0,0x60,0x620,0x0,0x1f0,0x70,0x630,0x0,
  0x200,0x90,0xc00,0x0,0x240,0x80,0xc10,0x0,0x290,0xb0,0xc20,0x0,0x2d0,0xa0,0xc30,0x0,
  0x320,0xd0,0x1200,0x0,0x360,0xc0,0x1210,0x0,0x3b0,0xf0,0x1220,0x0,0x3f0,0xe0,0x1230,0x0,
  0x8,0x120,0xbd000,0x0,0x48,0x130,0xbd010,0x0,0x98,0x100,0xbd020,0x0,0xd8,0x110,0xbd030,0x0,
  0x128,0x160,0xbd600,0x0,0x168,0x170,0xbd610,0x0,0x1b8,0x140,0xbd620,0x0,0x1f8,0x150,0xbd630,0x0,
  0x208,0x1b0,0xbdc00,0x0,0x248,0x1a0,0xbdc10,0x0,0x298,0x190,0xbdc20,0x0,0x2d8,0x180,0xbdc30,0x0,
  0x328,0x1f0,0xbe200,0x0,0x368,0x1e0,0xbe210,0x0,0x3b8,0x1d0,0xbe220,0x0,0x3f8,0x1c0,0xbe230,0x0,
  0x10,0x200,0x17a000,0x0,0x50,0x210,0x17a010,0x0,0x80,0x220,0x17a020,0x0,0xc0,0x230,0x17a030,0x0,
  0x130,0x240,0x17a600,0x0,0x170,0x250,0x17a610,0x0,0x1a0,0x260,0x17a620,0x0,0x1e0,0x270,0x17a630,0x0,
  0x210,0x290,0x17ac00,0x0,0x250,0x280,0x17ac10,0x0,0x280,0x2b0,0x17ac20,0x0,0x2c0,0x2a0,0x17ac30,0x0,
  0x330,0x2d0,0x17b200,0x0,0x370,0x2c0,0x17b210,0x0,0x3a0,0x2f0,0x17b220,0x0,0x3e0,0x2e0,0x17b230,0x0,
  0x18,0x320,0x237000,0x0,0x58,0x330,0x237010,0x0,0x88,0x300,0x237020,0x0,0xc8,0x310,0x237030,0x0,
  0x138,0x360,0x237600,0x0,0x178,0x370,0x237610,0x0,0x1a8,0x340,0x237620,0x0,0x1e8,0x350,0x237630,0x0,
  0x218,0x3b0,0x237c00,0x0,0x258,0x3a0,0x237c10,0x0,0x288,0x390,0x237c20,0x0,0x2c8,0x380,0x237c30,0x0,
  0x338,0x3f0,0x238200,0x0,0x378,0x3e0,0x238210,0x0,0x3a8,0x3d0,0x238220,0x0,0x3e8,0x3c0,0x238230,0x0,
  0x0,0x440,0x540,0x0,0x8,0x448,0x548,0x10,0x10,0x450,0x550,0x20,0x18,0x458,0x558,0x30,
  0x40,0x480,0xf40,0x40,0x48,0x488,0xf48,0x50,0x50,0x490,0xf50,0x60,0x58,0x498,0xf58,0x70,
  0x80,0x4c0,0xf80,0x80,0x88,0x4c8,0xf88,0x210,0x90,0x4d0,0xf90,0xa0,0x98,0x4d8,0xf98,0xb0,
  0xc0,0x500,0xfc0,0xc0,0xc8,0x508,0xfc8,0xd0,0xd0,0x510,0xfd0,0xe0,0xd8,0x518,0xfd8,0xf0,
  0x5a0,0x9e0,0xae0,0x100,0x5a8,0x9e8,0xae8,0x110,0x5b0,0x9f0,0xaf0,0x120,0x5b8,0x9f8,0xaf8,0x130,
  0x5e0,0xa20,0x14e0,0x140,0x5e8,0xa28,0x14e8,0x150,0x5f0,0xa30,0x14f0,0x160,0x5f8,0xa38,0x14f8,0x170,
  0x620,0xa60,0x1520,0x200,0x628,0xa68,0x1528,0x190,0x630,0xa70,0x1530,0x1a0,0x638,0xa78,0x1538,0x1b0,
  0x660,0xaa0,0x1560,0x1c0,0x668,0xaa8,0x1568,0x1d0,0x670,0xab0,0x1570,0x1e0,0x678,0xab8,0x1578,0x1f0,
  0x200,0x1000,0x1ac0,0x6200,0x208,0x1008,0x1ac8,0x6210,0x210,0x1010,0x1ad0,0x6220,0x218,0x1018,0x1ad8,0x6230,
  0x240,0x1040,0x1b00,0x6240,0x248,0x1048,0x1b08,0x6250,0x250,0x1050,0x1b10,0x6260,0x258,0x1058,0x1b18,0x6270,
  0x280,0x1a40,0x1b40,0x6280,0x288,0x1a48,0x1b48,0x6410,0x290,0x1a50,0x1b50,0x62a0,0x298,0x1a58,0x1b58,0x62b0,
  0x2c0,0x1a80,0x1b40,0x62c0,0x2c8,0x1a88,0x1b48,0x62d0,0x2d0,0x1a90,0x1b50,0x62e0,0x2d8,0x1a98,0x1b58,0x62f0,
  0x7a0,0x15a0,0x2060,0x6300,0x7a8,0x15a8,0x2068,0x6310,0x7b0,0x15b0,0x2070,0x6320,0x7b8,0x15b8,0x2078,0x6330,
  0x7e0,0x15e0,0x20a0,0x6340,0x7e8,0x15e8,0x20a8,0x6350,0x7f0,0x15f0,0x20b0,0x6360,0x7f8,0x15f8,0x20b8,0x6370,
  0x820,0x1fe0,0x20e0,0x6400,0x828,0x1fe8,0x20e8,0x6390,0x830,0x1ff0,0x20f0,0x63a0,0x838,0x1ff8,0x20f8,0x63b0,
  0x860,0x2020,0x20e0,0x63c0,0x868,0x2028,0x20e8,0x63d0,0x870,0x2030,0x20f0,0x63e0,0x878,0x2038,0x20f8,0x63f0,
  0x90,0x90,0x0,0x1e0,0x180,0x180,0x10,0x1f0,0x4d0,0x4d0,0x20,0x200,0x5c0,0x5c0,0x30,0x210,
  0x28040,0x28040,0x1540,0x1720,0x28050,0x28050,0x1550,0x1730,0x28060,0x28060,0x1560,0x1740,0x28070,0x28070,0x1570,0x1750,
  0x910,0x910,0x2a80,0x2c60,0xa00,0xa00,0x2a90,0x2c70,0xd50,0x280a0,0x2aa0,0x2c80,0xe40,0x280b0,0x2ab0,0x2c90,
  0x280c0,0x280c0,0x3fc0,0x41a0,0x280d0,0x280d0,0x3fd0,0x41b0,0x280e0,0x280e0,0x3fe0,0x41c0,0x280f0,0x280f0,0x3ff0,0x41d0,
  0x1190,0xd50,0x6200,0x63e0,0x1280,0xe40,0x6210,0x63f0,0x15d0,0x1190,0x6220,0x6400,0x16c0,0x1280,0x6230,0x6410,
  0x28140,0x28140,0x7740,0x7920,0x28150,0x28150,0x7750,0x7930,0x28160,0x28160,0x7760,0x7940,0x28170,0x28170,0x7770,0x7950,
  0x28180,0x28180,0x8c80,0x8e60,0x28190,0x28190,0x8c90,0x8e70,0x281a0,0x281a0,0x8ca0,0x8e80,0x281b0,0x281b0,0x8cb0,0x8e90,
  0x281c0,0x281c0,0xa1c0,0xa3a0,0x281d0,0x281d0,0xa1d0,0xa3b0,0x281e0,0x281e0,0xa1e0,0xa3c0,0x281f0,0x281f0,0xa1f0,0xa3d0,
  0x6290,0x6290,0x10,0x400,0x6380,0x6380,0x20,0x410,0x66d0,0x66d0,0x30,0x420,0x67c0,0x67c0,0x40,0x430,
  0x28040,0x28040,0x1550,0x1940,0x28050,0x28050,0x1560,0x1950,0x28060,0x28060,0x1570,0x1960,0x28070,0x28070,0x1580,0x1970,
  0x6b10,0x6b10,0x2a90,0x2e80,0x6c00,0x6c00,0x2aa0,0x2e90,0x6f50,0x280a0,0x2ab0,0x2ea0,0x7040,0x280b0,0x2ac0,0x2eb0,
  0x280c0,0x280c0,0x3fd0,0x43c0,0x280d0,0x280d0,0x3fe0,0x43d0,0x280e0,0x280e0,0x3ff0,0x43e0,0x280f0,0x280f0,0x4000,0x43f0,
  0x7390,0x6f50,0x6210,0x6600,0x7480,0x7040,0x6220,0x6610,0x77d0,0x7390,0x6230,0x6620,0x78c0,0x7480,0x6240,0x6630,
  0x28140,0x28140,0x7750,0x7b40,0x28150,0x28150,0x7760,0x7b50,0x28160,0x28160,0x7770,0x7b60,0x28170,0x28170,0x7780,0x7b70,
  0x28180,0x28180,0x8c90,0x9080,0x28190,0x28190,0x8ca0,0x9090,0x281a0,0x281a0,0x8cb0,0x90a0,0x281b0,0x281b0,0x8cc0,0x90b0,
  0x281c0,0x281c0,0xa1d0,0xa5c0,0x281d0,0x281d0,0xa1e0,0xa5d0,0x281e0,0x281e0,0xa1f0,0xa5e0,0x281f0,0x281f0,0xa200,0xa5f0,
  0x3f803f80,0x3f803f80,0x40003f80,0x41004080,0xbf803f80,0xbf803f80,0xc0003f80,0xc1004080,0x3f80,0x0,0x40804100,0x3f804000,0x0,0x0,0x0,0x0,
  0x3f803f80,0x3f803f80,0x40003f80,0x41004080,0xbf803f80,0xbf803f80,0xc0003f80,0xc1004080,0x3f80,0x0,0x40804100,0x3f804000,0x0,0x0,0x0,0x0,
  0x3f803f80,0x3f803f80,0x40003f80,0x41004080,0xbf803f80,0xbf803f80,0xc0003f80,0xc1004080,0x3f80,0x0,0x40804100,0x3f804000,0x0,0x0,0x0,0x0,
  0x3f803f80,0x3f803f80,0x40003f80,0x41004080,0xbf803f80,0xbf803f80,0xc0003f80,0xc1004080,0x3f80,0x0,0x40804100,0x3f804000,0x0,0x0,0x0,0x0,
  0x3f803f80,0x3f803f80,0x40003f80,0x41004080,0xbf803f80,0xbf803f80,0xc0003f80,0xc1004080,0x3f80,0x0,0x40804100,0x3f804000,0x0,0x0,0x0,0x0,
  0x3f803f80,0x3f803f80,0x40003f80,0x41004080,0xbf803f80,0xbf803f80,0xc0003f80,0xc1004080,0x3f80,0x0,0x40804100,0x3f804000,0x0,0x0,0x0,0x0,
  0x3f803f80,0x3f803f80,0x40003f80,0x41004080,0xbf803f80,0xbf803f80,0xc0003f80,0xc1004080,0x3f80,0x0,0x40804100,0x3f804000,0x0,0x0,0x0,0x0,
  0x3f803f80,0x3f803f80,0x40003f80,0x41004080,0xbf803f80,0xbf803f80,0xc0003f80,0xc1004080,0x3f80,0x0,0x40804100,0x3f804000,0x0,0x0,0x0,0x0,
  0x3f803f80,0x3f80,0x40003f80,0x4080,0xbf803f80,0x3f80,0xc0003f80,0x4080,0x3f80,0x0,0x40804100,0x4000,0x0,0x0,0x0,0x0,
  0x3f803f80,0x3f80,0x40003f80,0x4080,0xbf803f80,0x3f80,0xc0003f80,0x4080,0x3f80,0x0,0x40804100,0x4000,0x0,0x0,0x0,0x0,
  0x3f803f80,0x3f80,0x40003f80,0x4080,0xbf803f80,0x3f80,0xc0003f80,0x4080,0x3f80,0x0,0x40804100,0x4000,0x0,0x0,0x0,0x0,
  0x3f803f80,0x3f80,0x40003f80,0x4080,0xbf803f80,0x3f80,0xc0003f80,0x4080,0x3f80,0x0,0x40804100,0x4000,0x0,0x0,0x0,0x0,
  0x3f803f80,0x3f80,0x40003f80,0x4080,0xbf803f80,0x3f80,0xc0003f80,0x4080,0x3f80,0x0,0x40804100,0x4000,0x0,0x0,0x0,0x0,
  0x3f803f80,0x3f80,0x40003f80,0x4080,0xbf803f80,0x3f80,0xc0003f80,0x4080,0x3f80,0x0,0x40804100,0x4000,0x0,0x0,0x0,0x0,
  0x3f803f80,0x3f80,0x40003f80,0x4080,0xbf803f80,0x3f80,0xc0003f80,0x4080,0x3f80,0x0,0x40804100,0x4000,0x0,0x0,0x0,0x0,
  0x3f803f80,0x3f80,0x40003f80,0x4080,0xbf803f80,0x3f80,0xc0003f80,0x4080,0x3f80,0x0,0x40804100,0x4000,0x0,0x0,0x0,0x0,
  0xc400,0xd0c0,0x178c0,0x0,0xc408,0xd0c8,0x178c8,0x0,0xc410,0xd0d0,0x178d0,0x0,0xc418,0xd0d8,0x178d8,0x0,
  0xc840,0xd080,0x17880,0x0,0xc848,0xd088,0x17888,0x0,0xc850,0xd090,0x17890,0x0,0xc858,0xd098,0x17898,0x0,
  0xcc80,0xd040,0x17840,0x0,0xcc88,0xd048,0x17848,0x0,0xcc90,0xd050,0x17850,0x0,0xcc98,0xd058,0x17858,0x0,
  0xd0c0,0xd000,0x17800,0x0,0xd0c8,0xd008,0x17808,0x0,0xd0d0,0xd010,0x17810,0x0,0xd0d8,0xd018,0x17818,0x0,
  0xc520,0xd1e0,0x179e0,0x0,0xc528,0xd1e8,0x179e8,0x0,0xc530,0xd1f0,0x179f0,0x0,0xc538,0xd1f8,0x179f8,0x0,
  0xc960,0xd1a0,0x179a0,0x0,0xc968,0xd1a8,0x179a8,0x0,0xc970,0xd1b0,0x179b0,0x0,0xc978,0xd1b8,0x179b8,0x0,
  0xcda0,0xd160,0x17960,0x0,0xcda8,0xd168,0x17968,0x0,0xcdb0,0xd170,0x17970,0x0,0xcdb8,0xd178,0x17978,0x0,
  0xd1e0,0xd120,0x17920,0x0,0xd1e8,0xd128,0x17928,0x0,0xd1f0,0xd130,0x17930,0x0,0xd1f8,0xd138,0x17938,0x0,
  0xd400,0xecc0,0x178c0,0x0,0xd408,0xecc8,0x178c8,0x0,0xd410,0xecd0,0x178d0,0x0,0xd418,0xecd8,0x178d8,0x0,
  0xd840,0xec80,0x17880,0x0,0xd848,0xec88,0x17888,0x0,0xd850,0xec90,0x17890,0x0,0xd858,0xec98,0x17898,0x0,
  0xdc80,0xec40,0x17840,0x0,0xdc88,0xec48,0x17848,0x0,0xdc90,0xec50,0x17850,0x0,0xdc98,0xec58,0x17858,0x0,
  0xdc80,0xec00,0x17800,0x0,0xdc88,0xec08,0x17808,0x0,0xdc90,0xec10,0x17810,0x0,0xdc98,0xec18,0x17818,0x0,
  0xd520,0xede0,0x179e0,0x0,0xd528,0xede8,0x179e8,0x0,0xd530,0xedf0,0x179f0,0x0,0xd538,0xedf8,0x179f8,0x0,
  0xd960,0xeda0,0x179a0,0x0,0xd968,0xeda8,0x179a8,0x0,0xd970,0xedb0,0x179b0,0x0,0xd978,0xedb8,0x179b8,0x0,
  0xdda0,0xed60,0x17960,0x0,0xdda8,0xed68,0x17968,0x0,0xddb0,0xed70,0x17970,0x0,0xddb8,0xed78,0x17978,0x0,
  0xdda0,0xed20,0x17920,0x0,0xdda8,0xed28,0x17928,0x0,0xddb0,0xed30,0x17930,0x0,0xddb8,0xed38,0x17938,0x0,
  0x0,0x0,0x0,0x0,0x40,0x10,0x10,0x0,0x90,0x20,0x20,0x0,0xd0,0x30,0x30,0x0,
  0x120,0x40,0x600,0x0,0x160,0x50,0x610,0x0,0x1b0,0x60,0x620,0x0,0x1f0,0x70,0x630,0x0,
  0x200,0x90,0xc00,0x0,0x240,0x80,0xc10,0x0,0x290,0xb0,0xc20,0x0,0x2d0,0xa0,0xc30,0x0,
  0x320,0xd0,0x1200,0x0,0x360,0xc0,0x1210,0x0,0x3b0,0xf0,0x1220,0x0,0x3f0,0xe0,0x1230,0x0,
  0x8,0x120,0xd2000,0x0,0x48,0x130,0xd2010,0x0,0x98,0x100,0xd2020,0x0,0xd8,0x110,0xd2030,0x0,
  0x128,0x160,0xd2600,0x0,0x168,0x170,0xd2610,0x0,0x1b8,0x140,0xd2620,0x0,0x1f8,0x150,0xd2630,0x0,
  0x208,0x1b0,0xd2c00,0x0,0x248,0x1a0,0xd2c10,0x0,0x298,0x190,0xd2c20,0x0,0x2d8,0x180,0xd2c30,0x0,
  0x328,0x1f0,0xd3200,0x0,0x368,0x1e0,0xd3210,0x0,0x3b8,0x1d0,0xd3220,0x0,0x3f8,0x1c0,0xd3230,0x0,
  0x10,0x200,0x1a4000,0x0,0x50,0x210,0x1a4010,0x0,0x80,0x220,0x1a4020,0x0,0xc0,0x230,0x1a4030,0x0,
  0x130,0x240,0x1a4600,0x0,0x170,0x250,0x1a4610,0x0,0x1a0,0x260,0x1a4620,0x0,0x1e0,0x270,0x1a4630,0x0,
  0x210,0x290,0x1a4c00,0x0,0x250,0x280,0x1a4c10,0x0,0x280,0x2b0,0x1a4c20,0x0,0x2c0,0x2a0,0x1a4c30,0x0,
  0x330,0x2d0,0x1a5200,0x0,0x370,0x2c0,0x1a5210,0x0,0x3a0,0x2f0,0x1a5220,0x0,0x3e0,0x2e0,0x1a5230,0x0,
  0x18,0x320,0x276000,0x0,0x58,0x330,0x276010,0x0,0x88,0x300,0x276020,0x0,0xc8,0x310,0x276030,0x0,
  0x138,0x360,0x276600,0x0,0x178,0x370,0x276610,0x0,0x1a8,0x340,0x276620,0x0,0x1e8,0x350,0x276630,0x0,
  0x218,0x3b0,0x276c00,0x0,0x258,0x3a0,0x276c10,0x0,0x288,0x390,0x276c20,0x0,0x2c8,0x380,0x276c30,0x0,
  0x338,0x3f0,0x277200,0x0,0x378,0x3e0,0x277210,0x0,0x3a8,0x3d0,0x277220,0x0,0x3e8,0x3c0,0x277230,0x0
};

__global__ __launch_bounds__(256, 1) void conv_kernel(const char* __restrict__ x, const char* __restrict__ w, char* __restrict__ out, char* __restrict__ ws) {
  __shared__ __attribute__((aligned(1024))) char lds[LDS_TOTAL];
  const unsigned bid = blockIdx.x;
  const unsigned t = (bid & 7) * 32 + (bid >> 3);
  if (t >= 252) {
    if (t == 252 && threadIdx.x < 64) {
      __amdgpu_buffer_rsrc_t srd = __builtin_amdgcn_make_buffer_rsrc((void*)(ws + 1695744), (short)0, 4096, 0x00020000);
      const unsigned l16 = threadIdx.x * 16;
      asm volatile(
        "s_nop 4\n"
        "v_mov_b32 v8, 0\n v_mov_b32 v9, 0\n v_mov_b32 v10, 0\n v_mov_b32 v11, 0\n"
        "buffer_store_dwordx4 v[8:11], %[o], %[srd], 0 offen sc0 sc1\n"
        "buffer_store_dwordx4 v[8:11], %[o], %[srd], 0 offen offset:1024 sc0 sc1\n"
        "buffer_load_dword v12, off, %[srd], 0 offset:2048 sc0 sc1\n"
        "s_mov_b32 s40, 0x100000\n"
        "s_waitcnt vmcnt(0)\n"
        "3:\n"
        "v_add_u32 v12, 1, v12\n"
        "s_mov_b64 exec, 1\n"
        "buffer_store_dword v12, off, %[srd], 0 offset:2048 sc0 sc1\n"
        "s_mov_b64 exec, -1\n"
        "buffer_load_dwordx4 v[8:11], %[o], %[srd], 0 offen offset:1024 sc0 sc1\n"
        "s_waitcnt vmcnt(0)\n"
        "v_min3_u32 v8, v8, v9, v10\n v_min_u32 v8, v8, v11\n"
        "v_cmp_eq_u32 vcc, 0, v8\n"
        "s_nop 3\n"
        "s_and_b32 vcc_hi, vcc_hi, 0x7fffffff\n s_or_b32 s42, vcc_lo, vcc_hi\n"
        "s_cbranch_scc0 4f\n"
        "s_sub_u32 s40, s40, 1\n s_cmp_lg_u32 s40, 0\n s_cbranch_scc1 3b\n"
        "4:\n"
        :: [o] "v"(l16), [srd] "s"(srd)
        : "v8","v9","v10","v11","v12","s40","s42","vcc","scc","memory");
    }
    return;
  }
  asm volatile(
    "s_load_dwordx2 s[16:17], %[ka], 0\n"
    "s_load_dwordx2 s[20:21], %[ka], 8\n"
    "s_load_dwordx2 s[24:25], %[ka], 16\n"
    "s_load_dwordx2 s[28:29], %[ka], 24\n"
    "v_lshrrev_b32 v10, 6, %[tid]\n"
    "v_and_b32 v4, 63, %[tid]\n"
    "s_getpc_b64 s[84:85]\n"
    "92:\n"
    "s_and_b32 s72, %[t], 15\n"
    "s_lshl_b32 s72, s72, 12\n"
    "v_lshl_add_u32 v11, %[tid], 4, s72\n"
    "v_min_u32 v11, 91f-92b, v11\n"
    "global_load_dword v37, v11, s[84:85]\n"
    "s_mul_i32 s72, %[t], 171\n"
    "v_readfirstlane_b32 s42, v10\n"
    "s_lshr_b32 s72, s72, 9\n"
    "s_and_b32 s38, s72, 1\n"
    "s_mul_i32 s37, s72, 3\n"
    "s_lshr_b32 s72, s72, 1\n"
    "s_mul_i32 s69, s72, 171\n"
    "s_lshr_b32 s69, s69, 9\n"
    "s_mul_i32 s74, s69, 3\n"
    "s_sub_u32 s39, s72, s74\n"
    "s_lshr_b32 s45, s39, 1\n"
    "v_lshlrev_b32 v5, 4, v4\n"
    "s_mov_b64 s[12:13], %[tab]\n"
    "s_movk_i32 s14, 14336\n"
    "s_mov_b32 s15, 0x20000\n"
    "s_mul_i32 s53, s45, 5120\n"
    "s_addk_i32 s53, 4096\n"
    "s_lshl_b32 s73, s42, 10\n"
    "s_add_u32 m0, s73, 78848\n"
    "s_add_u32 s53, s53, s73\n"
    "buffer_load_dwordx4 v5, s[12:15], s53 offen lds\n"
    "s_add_u32 m0, s73, 82944\n"
    "s_nop 0\n"
    "buffer_load_dwordx4 v5, s[12:15], s73 offen lds\n"
    "s_cmp_ge_u32 s69, 7\n"
    "s_cselect_b32 s41, 1, 0\n"
    "s_cselect_b32 s74, 7, 0\n"
    "s_sub_u32 s40, s69, s74\n"
    "v_lshrrev_b32 v55, 2, v4\n"
    "v_and_b32 v11, 3, v4\n"
    "s_lshl_b32 s72, s40, 5\n"
    "s_add_u32 s72, s72, s38\n"
    "s_sub_u32 s72, s72, 6\n"
    "s_movk_i32 s73, 224\n"
    "s_movk_i32 s74, 192\n"
    "v_lshl_add_u32 v48, v55, 1, s72\n"
    "v_lshlrev_b32 v49, 4, v11\n"
    "v_bfrev_b32 v51, 1\n"
    "v_cmp_gt_u32 vcc, s73, v48\n"
    "v_mad_u32_u24 v6, v48, s74, v49\n"
    "s_add_u32 s46, s45, 1\n"
    "s_min_u32 s53, s42, s46\n"
    "v_cndmask_b32 v6, v51, v6, vcc\n"
    "s_mul_i32 s47, s42, 5\n"
    "s_add_u32 s47, s47, s53\n"
    "s_mul_i32 s51, s39, 18\n"
    "s_add_u32 s51, s51, s47\n"
    "s_lshl_b32 s51, s51, 2\n"
    "s_add_u32 s51, s51, s41\n"
    "s_sub_u32 s51, s51, 6\n"
    "s_mul_i32 s51, s51, 43008\n"
    "s_mul_i32 s48, s47, 2816\n"
    "s_add_u32 s48, s48, 100352\n"
    "s_mov_b32 s18, 9633792\n"
    "s_mov_b32 s19, s15\n"
    "v_xor_b32 v50, 32, v49\n"
    "v_add_u32 v50, 86016, v50\n"
    "v_add_u32 v52, 20, v48\n"
    "v_add_u32 v53, 32, v48\n"
    "v_subrev_u32 v54, 12, v48\n"
    "v_cmp_lt_u32 vcc, 23, v4\n"
    "v_mad_u32_u24 v8, v52, s74, v50\n"
    "s_sub_u32 s37, %[t], s37\n"
    "v_cndmask_b32 v53, v53, v54, vcc\n"
    "v_cndmask_b32 v49, v49, v50, vcc\n"
    "v_cmp_gt_u32 vcc, s73, v53\n"
    "v_mad_u32_u24 v7, v53, s74, v49\n"
    "s_mov_b32 s36, %[t]\n"
    "v_cndmask_b32 v7, v51, v7, vcc\n"
    "v_cmp_gt_u32 vcc, s73, v52\n"
    "s_mov_b32 s22, 903168\n"
    "s_mov_b32 s23, s19\n"
    "v_cndmask_b32 v8, v51, v8, vcc\n"
    "s_waitcnt lgkmcnt(0)\n"
    "s_add_u32 s58, s51, 0\n"
    "s_add_u32 m0, s48, 0\n"
    "v_add_u32 v10, s58, v6\n"
    "buffer_load_dwordx4 v10, s[16:19], 0 offen lds\n"
    "s_add_u32 m0, s48, 1024\n"
    "v_add_u32 v10, s58, v7\n"
    "buffer_load_dwordx4 v10, s[16:19], 0 offen lds\n"
    "s_add_u32 m0, s48, 2048\n"
    "v_add_u32 v10, s58, v8\n"
    "s_mov_b32 exec_hi, 0xffff\n"
    "buffer_load_dwordx4 v10, s[16:19], 0 offen lds\n"
    "s_mov_b32 exec_hi, -1\n"
    "v_xor_b32 v9, s42, v55\n"
    "v_mul_u32_u24 v9, 192, v9\n"
    "v_lshrrev_b32 v48, 3, v4\n"
    "v_and_b32 v48, 2, v48\n"
    "v_xor_b32 v48, v48, v11\n"
    "v_lshl_add_u32 v9, v48, 4, v9\n"
    "s_lshl_b32 s60, s42, 10\n"
    "s_add_u32 s60, s60, 50176\n"
    "s_add_u32 s61, s60, 4096\n"
    "s_mul_i32 s63, s42, 18432\n"
    "s_add_u32 s64, s63, 73728\n"
    "s_cmp_eq_u32 s42, 3\n"
    "s_cselect_b32 s61, 162304, s61\n"
    "s_cselect_b32 s62, 0, 7168\n"
    "s_cselect_b32 s64, 1073741824, s64\n"
    "s_cselect_b32 s65, 0, 1073741824\n"
    "s_mul_i32 s69, s69, 6\n"
    "s_sub_u32 s78, s36, s69\n"
    "s_lshl_b32 s72, s45, 8\n"
    "s_or_b32 s78, s78, s72\n"
    "s_cmp_lt_u32 s78, 105\n"
    "s_cbranch_scc0 93f\n"
    "s_mul_i32 s75, s78, 3121\n"
    "s_lshr_b32 s75, s75, 16\n"
    "s_mul_i32 s73, s75, 21\n"
    "s_sub_u32 s73, s78, s73\n"
    "s_add_u32 s75, s75, 1\n"
    "s_mul_i32 s77, s73, 171\n"
    "s_lshr_b32 s77, s77, 9\n"
    "s_mul_i32 s58, s77, 3\n"
    "s_sub_u32 s76, s73, s58\n"
    "s_mul_i32 s67, s77, 129024\n"
    "s_mul_i32 s58, s75, 3072\n"
    "s_add_u32 s67, s67, s58\n"
    "s_lshl_b32 s58, s76, 6\n"
    "s_add_u32 s67, s67, s58\n"
    "s_add_u32 m0, s60, 43008\n"
    "s_add_u32 s72, s63, s67\n"
    "s_mul_i32 s73, s62, 6\n"
    "buffer_load_dwordx4 v9, s[20:23], s72 offen lds\n"
    "s_add_u32 m0, s61, s73\n"
    "s_add_u32 s72, s64, s67\n"
    "buffer_load_dwordx4 v9, s[20:23], s72 offen lds\n"
    "93:\n"
    "s_lshl_b32 s72, s37, 6\n"
    "s_add_u32 s63, s63, s72\n"
    "s_add_u32 s64, s64, s72\n"
    "s_add_u32 m0, s60, 0\n"
    "s_add_u32 s80, s63, 0\n"
    "buffer_load_dwordx4 v9, s[20:23], s80 offen lds\n"
    "s_mov_b32 m0, s61\n"
    "s_add_u32 s80, s64, 0\n"
    "buffer_load_dwordx4 v9, s[20:23], s80 offen lds\n"
    "s_add_u32 s61, s61, s62\n"
    "s_add_u32 m0, s60, 7168\n"
    "s_add_u32 s80, s63, 129024\n"
    "buffer_load_dwordx4 v9, s[20:23], s80 offen lds\n"
    "s_mov_b32 m0, s61\n"
    "s_add_u32 s80, s64, 129024\n"
    "buffer_load_dwordx4 v9, s[20:23], s80 offen lds\n"
    "s_add_u32 s61, s61, s62\n"
    "s_add_u32 s58, s51, 172032\n"
    "s_add_u32 m0, s48, 2816\n"
    "v_add_u32 v10, s58, v6\n"
    "buffer_load_dwordx4 v10, s[16:19], 0 offen lds\n"
    "s_add_u32 m0, s48, 3840\n"
    "v_add_u32 v10, s58, v7\n"
    "buffer_load_dwordx4 v10, s[16:19], 0 offen lds\n"
    "s_add_u32 m0, s48, 4864\n"
    "v_add_u32 v10, s58, v8\n"
    "s_mov_b32 exec_hi, 0xffff\n"
    "buffer_load_dwordx4 v10, s[16:19], 0 offen lds\n"
    "s_mov_b32 exec_hi, -1\n"
    "s_and_b32 s43, s42, 1\n"
    "s_lshr_b32 s44, s42, 1\n"
    "v_mov_b32 v39, 1\n"
    "s_mov_b32 s30, 1699840\n"
    "s_mov_b32 s31, s19\n"
    "s_cmp_lt_u32 s42, s46\n"
    "s_cselect_b32 s46, 1, 0\n"
    "s_cselect_b32 s59, 64, 0\n"
    "s_cselect_b32 s72, 1024, 0\n"
    "s_cselect_b32 s73, s48, 148224\n"
    "s_cselect_b32 s57, s51, 1073741824\n"
    "s_add_u32 s57, s57, 860160\n"
    "s_add_u32 s54, s73, 14080\n"
    "s_add_u32 s55, s54, s72\n"
    "s_add_u32 s56, s55, s72\n"
    "s_lshl_b32 s79, s78, 2\n"
    "s_cmp_lt_u32 s78, 21\n"
    "s_cselect_b32 s72, 0, 44\n"
    "s_add_u32 s79, s79, s72\n"
    "s_add_u32 s79, s79, 1695744\n"
    "s_cmp_lt_u32 s78, 105\n"
    "s_cselect_b32 s79, s79, 1073741824\n"
    "v_mov_b32 v3, s79\n"
    "s_mov_b32 s68, 1073741824\n"
    "s_mov_b32 s66, 1073741824\n"
    "s_mov_b64 s[70:71], -1\n"
    "v_add_u32 v48, 78848, v5\n"
    "s_waitcnt vmcnt(10)\n"
    "s_barrier\n"
    "ds_read_b128 v[20:23], v48 offset:2048\n"
    "ds_read_b128 v[24:27], v48 offset:3072\n"
    "ds_read_b128 v[12:15], v48 offset:0\n"
    "ds_read_b128 v[16:19], v48 offset:1024\n"
    "ds_read_b128 a[228:231], v48 offset:4096\n"
    "ds_read_b128 a[232:235], v48 offset:5120\n"
    "ds_read_b128 a[236:239], v48 offset:6144\n"
    "ds_read_b128 a[240:243], v48 offset:7168\n"
    "s_mov_b32 s80, 1695744\n"
    "buffer_load_dword v38, off, s[28:31], s80 offset:2048 sc1\n"
    "s_add_u32 s58, s51, 344064\n"
    "s_add_u32 m0, s48, 5632\n"
    "s_nop 0\n"
    "buffer_load_dwordx4 v6, s[16:19], s58 offen lds\n"
    "s_waitcnt lgkmcnt(6)\n"
    "s_cmp_eq_u32 s44, 1\n"
    "s_cselect_b64 vcc, -1, 0\n"
    "s_lshl_b32 s72, s43, 5\n"
    "v_xor_b32 v24, s72, v24\n"
    "v_xor_b32 v25, s72, v25\n"
    "v_xor_b32 v26, s72, v26\n"
    "v_cndmask_b32 v20, v20, v22, vcc\n"
    "v_cndmask_b32 v21, v21, v23, vcc\n"
    "v_xor_b32 v28, 64, v24\n"
    "v_xor_b32 v29, 128, v24\n"
    "v_xor_b32 v30, 192, v24\n"
    "s_waitcnt lgkmcnt(0)\n"
    "s_add_u32 m0, s48, 6656\n"
    "s_nop 0\n"
    "buffer_load_dwordx4 v7, s[16:19], s58 offen lds\n"
    "s_cmp_lt_u32 s78, 105\n"
    "s_cbranch_scc0 94f\n"
    "s_lshl_b32 s72, s76, 2\n"
    "s_lshl_b32 s73, s43, 1\n"
    "s_add_u32 s72, s72, s73\n"
    "s_add_u32 s72, s72, s44\n"
    "s_mul_i32 s72, s72, 141312\n"
    "s_mul_i32 s73, s75, 23552\n"
    "s_add_u32 s72, s72, s73\n"
    "s_lshl_b32 s73, s77, 10\n"
    "s_add_u32 s68, s72, s73\n"
    "s_cmp_ge_u32 s77, 4\n"
    "s_cselect_b32 s73, 3, 0\n"
    "s_cselect_b32 s75, 1, 0\n"
    "s_sub_u32 s73, s77, s73\n"
    "s_lshr_b32 s76, s73, 1\n"
    "s_add_u32 s76, s76, 21\n"
    "s_lshl_b32 s76, s76, 10\n"
    "s_add_u32 s66, s72, s76\n"
    "s_cmp_eq_u32 s75, s44\n"
    "s_cselect_b32 s66, s66, 1073741824\n"
    "s_bitcmp1_b32 s73, 0\n"
    "s_cselect_b32 s70, 0, -1\n"
    "s_cselect_b32 s71, -1, 0\n"
    "s_cmp_eq_u32 s77, 4\n"
    "s_cselect_b32 s70, -1, s70\n"
    "s_cselect_b32 s69, 163840, 0\n"
    "s_waitcnt vmcnt(9)\n"
    "s_barrier\n"
    "s_mov_b32 exec_hi, 0\n"
    "v_add_u32 v26, s69, v26\n"
    "s_mov_b32 exec_hi, -1\n"
    "ds_read_b64_tr_b16 a[0:1], v24 offset:43008\n"
    "ds_read_b64_tr_b16 a[2:3], v28 offset:43008\n"
    "ds_read_b64_tr_b16 a[4:5], v29 offset:43008\n"
    "ds_read_b64_tr_b16 a[6:7], v30 offset:43008\n"
    "ds_read_b64_tr_b16 a[8:9], v24 offset:43520\n"
    "ds_read_b64_tr_b16 a[10:11], v28 offset:43520\n"
    "ds_read_b64_tr_b16 a[12:13], v29 offset:43520\n"
    "ds_read_b64_tr_b16 a[14:15], v30 offset:43520\n"
    "ds_read_b64_tr_b16 v[252:253], v26 offset:0\n"
    "ds_read_b64_tr_b16 v[254:255], v26 offset:512\n"
    "s_waitcnt lgkmcnt(2)\n"
    "v_mfma_f32_4x4x4_16b_bf16 v[48:51], v[20:21], a[0:1], 0\n"
    "v_mfma_f32_4x4x4_16b_bf16 v[52:55], v[20:21], a[2:3], 0\n"
    "v_mfma_f32_4x4x4_16b_bf16 v[56:59], v[20:21], a[4:5], 0\n"
    "v_mfma_f32_4x4x4_16b_bf16 v[60:63], v[20:21], a[6:7], 0\n"
    "v_mfma_f32_4x4x4_16b_bf16 v[64:67], v[20:21], a[8:9], 0\n"
    "v_mfma_f32_4x4x4_16b_bf16 v[68:71], v[20:21], a[10:11], 0\n"
    "v_mfma_f32_4x4x4_16b_bf16 v[72:75], v[20:21], a[12:13], 0\n"
    "v_mfma_f32_4x4x4_16b_bf16 v[76:79], v[20:21], a[14:15], 0\n"
    "s_nop 1\n"
    "v_cvt_pk_bf16_f32 v132, v48, v52\n"
    "v_cvt_pk_bf16_f32 v133, v56, v60\n"
    "v_cvt_pk_bf16_f32 v134, v64, v68\n"
    "v_cvt_pk_bf16_f32 v135, v72, v76\n"
    "v_cvt_pk_bf16_f32 v172, v49, v53\n"
    "v_cvt_pk_bf16_f32 v173, v57, v61\n"
    "v_cvt_pk_bf16_f32 v174, v65, v69\n"
    "v_cvt_pk_bf16_f32 v175, v73, v77\n"
    "v_cvt_pk_bf16_f32 v212, v50, v54\n"
    "v_cvt_pk_bf16_f32 v213, v58, v62\n"
    "v_cvt_pk_bf16_f32 v214, v66, v70\n"
    "v_cvt_pk_bf16_f32 v215, v74, v78\n"
    "s_waitcnt lgkmcnt(0)\n"
    "s_cmp_lt_u32 s78, 21\n"
    "s_cbranch_scc0 94f\n"
    "buffer_store_dwordx4 v[132:135], v5, s[28:31], s68 offen sc0 sc1\n"
    "s_add_u32 s68, s68, 7168\n"
    "buffer_store_dwordx4 v[172:175], v5, s[28:31], s68 offen sc0 sc1\n"
    "s_add_u32 s68, s68, 7168\n"
    "buffer_store_dwordx4 v[212:215], v5, s[28:31], s68 offen sc0 sc1\n"
    "s_mov_b64 exec, s[70:71]\n"
    "buffer_store_dwordx4 v[252:255], v5, s[28:31], s66 offen sc0 sc1\n"
    "s_mov_b64 exec, -1\n"
    "s_mov_b32 s68, 1073741824\n"
    "s_mov_b32 s66, 1073741824\n"
    "94:\n"
    "s_add_u32 m0, s48, 7680\n"
    "s_mov_b32 exec_hi, 0xffff\n"
    "buffer_load_dwordx4 v8, s[16:19], s58 offen lds\n"
    "s_mov_b32 exec_hi, -1\n"
    "s_cmp_eq_u32 s46, 1\n"
    "s_cselect_b64 vcc, -1, 0\n"
    "s_cselect_b32 s72, 2816, 0\n"
    "s_cselect_b32 s73, 0, 163840\n"
    "s_lshl_b32 s74, s45, 6\n"
    "s_sub_u32 s74, 1152, s74\n"
    "s_add_u32 s72, s72, 5632\n"
    "s_mul_i32 s53, s47, s74\n"
    "v_cndmask_b32 v16, v17, v16, vcc\n"
    "v_add_u32 v12, s48, v12\n"
    "v_add_u32 v13, s48, v13\n"
    "v_add_u32 v14, s48, v14\n"
    "s_add_u32 s58, s51, 516096\n"
    "s_add_u32 m0, s48, 8448\n"
    "s_nop 0\n"
    "buffer_load_dwordx4 v6, s[16:19], s58 offen lds\n"
    "v_add_u32 v31, s72, v13\n"
    "v_add_u32 v32, s72, v14\n"
    "v_add_u32 v15, s53, v15\n"
    "v_add_u32 v16, s53, v16\n"
    "v_add_u32 v33, s73, v15\n"
    "v_add_u32 v37, s73, v12\n"
    "s_mul_i32 s72, s44, 240\n"
    "v_add_u32 v18, s72, v18\n"
    "s_add_u32 m0, s48, 9472\n"
    "s_nop 0\n"
    "buffer_load_dwordx4 v7, s[16:19], s58 offen lds\n"
    "s_cmp_eq_u32 s45, 0\n"
    "s_cbranch_scc1 90f\n"
    "s_waitcnt vmcnt(13)\n"
    "ds_read_b64_tr_b16 a[64:65], v12 offset:0\n"
    "ds_read_b64_tr_b16 a[66:67], v12 offset:256\n"
    "ds_read_b64_tr_b16 a[68:69], v12 offset:384\n"
    "ds_read_b64_tr_b16 a[70:71], v12 offset:640\n"
    "s_add_u32 m0, s48, 10496\n"
    "s_mov_b32 exec_hi, 0xffff\n"
    "buffer_load_dwordx4 v8, s[16:19], s58 offen lds\n"
    "s_mov_b32 exec_hi, -1\n"
    "v_mov_b32 v44, 0\n"
    "v_mov_b32 v45, 0\n"
    "v_mov_b32 v46, 0\n"
    "v_mov_b32 v47, 0\n"
    "s_mul_i32 s72, s43, 25088\n"
    "s_lshl_b32 s73, s44, 7\n"
    "s_add_u32 s72, s72, s73\n"
    "v_add_u32 v11, s72, v5\n"
    "ds_write_b128 v11, v[44:47] offset:23936\n"
    "v_add_u32 v11, 50176, v11\n"
    "s_waitcnt lgkmcnt(3)\n"
    "v_mfma_f32_32x32x16_bf16 v[48:63], a[64:67], a[228:231], 0\n"
    "s_waitcnt lgkmcnt(1)\n"
    "v_mfma_f32_32x32x16_bf16 v[48:63], a[68:71], a[232:235], v[48:63]\n"
    "s_add_u32 s58, s51, 688128\n"
    "s_add_u32 m0, s48, 11264\n"
    "s_nop 0\n"
    "buffer_load_dwordx4 v6, s[16:19], s58 offen lds\n"
    "s_waitcnt vmcnt(8)\n"
    "ds_read_b64_tr_b16 a[76:77], v12 offset:2816\n"
    "ds_read_b64_tr_b16 a[78:79], v12 offset:3072\n"
    "ds_read_b64_tr_b16 a[80:81], v12 offset:3200\n"
    "ds_read_b64_tr_b16 a[82:83], v12 offset:3456\n"
    "s_nop 7\n"
    "v_cvt_pk_bf16_f32 v48, v48, v49\n"
    "v_cvt_pk_bf16_f32 v49, v50, v51\n"
    "s_add_u32 m0, s48, 12288\n"
    "s_nop 0\n"
    "buffer_load_dwordx4 v7, s[16:19], s58 offen lds\n"
    "v_cvt_pk_bf16_f32 v50, v52, v53\n"
    "v_cvt_pk_bf16_f32 v51, v54, v55\n"
    "v_cvt_pk_bf16_f32 v52, v56, v57\n"
    "ds_write_b128 v15, v[48:51] offset:0\n"
    "v_cvt_pk_bf16_f32 v53, v58, v59\n"
    "v_cvt_pk_bf16_f32 v54, v60, v61\n"
    "v_cvt_pk_bf16_f32 v55, v62, v63\n"
    "s_nop 0\n"
    "ds_write_b128 v15, v[52:55] offset:544\n"
    "s_add_u32 m0, s48, 13312\n"
    "s_mov_b32 exec_hi, 0xffff\n"
    "buffer_load_dwordx4 v8, s[16:19], s58 offen lds\n"
    "s_mov_b32 exec_hi, -1\n"
    "s_barrier\n"
    "ds_read_b64_tr_b16 a[128:129], v24 offset:0\n"
    "ds_read_b64_tr_b16 a[130:131], v28 offset:0\n"
    "ds_read_b64_tr_b16 a[132:133], v29 offset:0\n"
    "ds_read_b64_tr_b16 a[134:135], v30 offset:0\n"
    "ds_read_b64_tr_b16 a[136:137], v24 offset:512\n"
    "ds_read_b64_tr_b16 a[138:139], v28 offset:512\n"
    "ds_read_b64_tr_b16 a[140:141], v29 offset:512\n"
    "ds_read_b64_tr_b16 a[142:143], v30 offset:512\n"
    "s_mov_b32 m0, s54\n"
    "s_nop 0\n"
    "buffer_load_dwordx4 v6, s[16:19], s57 offen lds\n"
    "ds_read_b64_tr_b16 a[144:145], v24 offset:7168\n"
    "ds_read_b64_tr_b16 a[146:147], v28 offset:7168\n"
    "ds_read_b64_tr_b16 a[148:149], v29 offset:7168\n"
    "ds_read_b64_tr_b16 a[150:151], v30 offset:7168\n"
    "ds_read_b64_tr_b16 a[152:153], v24 offset:7680\n"
    "ds_read_b64_tr_b16 a[154:155], v28 offset:7680\n"
    "ds_read_b64_tr_b16 a[156:157], v29 offset:7680\n"
    "ds_read_b64_tr_b16 a[158:159], v30 offset:7680\n"
    "s_mov_b32 m0, s55\n"
    "s_nop 0\n"
    "buffer_load_dwordx4 v7, s[16:19], s57 offen lds\n"
    "s_waitcnt lgkmcnt(8)\n"
    "v_mfma_f32_4x4x4_16b_bf16 v[48:51], v[20:21], a[128:129], 0\n"
    "v_mfma_f32_4x4x4_16b_bf16 v[52:55], v[20:21], a[130:131], 0\n"
    "v_mfma_f32_4x4x4_16b_bf16 v[56:59], v[20:21], a[132:133], 0\n"
    "v_mfma_f32_4x4x4_16b_bf16 v[60:63], v[20:21], a[134:135], 0\n"
    "v_mfma_f32_4x4x4_16b_bf16 v[64:67], v[20:21], a[136:137], 0\n"
    "v_mfma_f32_4x4x4_16b_bf16 v[68:71], v[20:21], a[138:139], 0\n"
    "v_mfma_f32_4x4x4_16b_bf16 v[72:75], v[20:21], a[140:141], 0\n"
    "v_mfma_f32_4x4x4_16b_bf16 v[76:79], v[20:21], a[142:143], 0\n"
    "s_mov_b32 m0, s56\n"
    "s_mov_b32 exec_hi, 0xffff\n"
    "buffer_load_dwordx4 v8, s[16:19], s57 offen lds\n"
    "s_mov_b32 exec_hi, -1\n"
    "s_add_u32 s51, s51, 64\n"
    "s_add_u32 s57, s57, s59\n"
    "s_nop 1\n"
    "v_cvt_pk_bf16_f32 v136, v48, v52\n"
    "v_cvt_pk_bf16_f32 v137, v56, v60\n"
    "v_cvt_pk_bf16_f32 v138, v64, v68\n"
    "v_cvt_pk_bf16_f32 v139, v72, v76\n"
    "v_cvt_pk_bf16_f32 v176, v49, v53\n"
    "v_cvt_pk_bf16_f32 v177, v57, v61\n"
    "v_cvt_pk_bf16_f32 v178, v65, v69\n"
    "s_add_u32 m0, s60, 14336\n"
    "s_add_u32 s80, s63, 258048\n"
    "buffer_load_dwordx4 v9, s[20:23], s80 offen lds\n"
    "v_cvt_pk_bf16_f32 v179, v73, v77\n"
    "v_cvt_pk_bf16_f32 v216, v50, v54\n"
    "v_cvt_pk_bf16_f32 v217, v58, v62\n"
    "v_cvt_pk_bf16_f32 v218, v66, v70\n"
    "v_cvt_pk_bf16_f32 v219, v74, v78\n"
    "s_waitcnt lgkmcnt(0)\n"
    "v_mfma_f32_4x4x4_16b_bf16 v[48:51], v[20:21], a[144:145], 0\n"
    "v_mfma_f32_4x4x4_16b_bf16 v[52:55], v[20:21], a[146:147], 0\n"
    "v_mfma_f32_4x4x4_16b_bf16 v[56:59], v[20:21], a[148:149], 0\n"
    "v_mfma_f32_4x4x4_16b_bf16 v[60:63], v[20:21], a[150:151], 0\n"
    "v_mfma_f32_4x4x4_16b_bf16 v[64:67], v[20:21], a[152:153], 0\n"
    "v_mfma_f32_4x4x4_16b_bf16 v[68:71], v[20:21], a[154:155], 0\n"
    "v_mfma_f32_4x4x4_16b_bf16 v[72:75], v[20:21], a[156:157], 0\n"
    "v_mfma_f32_4x4x4_16b_bf16 v[76:79], v[20:21], a[158:159], 0\n"
    "s_mov_b32 m0, s61\n"
    "s_add_u32 s80, s64, 258048\n"
    "buffer_load_dwordx4 v9, s[20:23], s80 offen lds\n"
    "s_add_u32 s61, s61, s62\n"
    "s_nop 1\n"
    "v_cvt_pk_bf16_f32 v140, v48, v52\n"
    "v_cvt_pk_bf16_f32 v141, v56, v60\n"
    "v_cvt_pk_bf16_f32 v142, v64, v68\n"
    "v_cvt_pk_bf16_f32 v143, v72, v76\n"
    "v_cvt_pk_bf16_f32 v180, v49, v53\n"
    "v_cvt_pk_bf16_f32 v181, v57, v61\n"
    "v_cvt_pk_bf16_f32 v182, v65, v69\n"
    "v_cvt_pk_bf16_f32 v183, v73, v77\n"
    "v_cvt_pk_bf16_f32 v220, v50, v54\n"
    "v_cvt_pk_bf16_f32 v221, v58, v62\n"
    "v_cvt_pk_bf16_f32 v222, v66, v70\n"
    "v_cvt_pk_bf16_f32 v223, v74, v78\n"
    "v_mfma_f32_32x32x16_bf16 v[64:79], a[76:79], a[228:231], 0\n"
    "v_mfma_f32_32x32x16_bf16 v[64:79], a[80:83], a[232:235], v[64:79]\n"
    "s_waitcnt vmcnt(11)\n"
    "ds_read_b64_tr_b16 a[64:65], v13\n"
    "ds_read_b64_tr_b16 a[66:67], v14\n"
    "ds_read_b64_tr_b16 a[68:69], v12 offset:5632\n"
    "ds_read_b64_tr_b16 a[70:71], v12 offset:5888\n"
    "ds_read_b64_tr_b16 a[72:73], v12 offset:6016\n"
    "ds_read_b64_tr_b16 a[74:75], v12 offset:6272\n"
    "s_waitcnt lgkmcnt(4)\n"
    "v_mfma_f32_32x32x16_bf16 v[80:95], a[64:67], a[236:239], 0\n"
    "s_waitcnt lgkmcnt(2)\n"
    "v_mfma_f32_32x32x16_bf16 v[48:63], a[68:71], a[228:231], 0\n"
    "s_waitcnt lgkmcnt(0)\n"
    "v_mfma_f32_32x32x16_bf16 v[48:63], a[72:75], a[232:235], v[48:63]\n"
    "s_waitcnt vmcnt(8)\n"
    "ds_read_b64_tr_b16 a[76:77], v12 offset:8448\n"
    "ds_read_b64_tr_b16 a[78:79], v12 offset:8704\n"
    "ds_read_b64_tr_b16 a[80:81], v12 offset:8832\n"
    "ds_read_b64_tr_b16 a[82:83], v12 offset:9088\n"
    "v_cvt_pk_bf16_f32 v64, v64, v65\n"
    "v_cvt_pk_bf16_f32 v65, v66, v67\n"
    "v_cvt_pk_bf16_f32 v66, v68, v69\n"
    "v_cvt_pk_bf16_f32 v67, v70, v71\n"
    "v_cvt_pk_bf16_f32 v68, v72, v73\n"
    "ds_write_b128 v15, v[64:67] offset:1088\n"
    "v_cvt_pk_bf16_f32 v69, v74, v75\n"
    "v_cvt_pk_bf16_f32 v70, v76, v77\n"
    "v_cvt_pk_bf16_f32 v71, v78, v79\n"
    "s_nop 0\n"
    "ds_write_b128 v15, v[68:71] offset:1632\n"
    "s_waitcnt lgkmcnt(4)\n"
    "v_mfma_f32_32x32x16_bf16 v[64:79], a[76:79], a[228:231], 0\n"
    "s_waitcnt lgkmcnt(2)\n"
    "v_mfma_f32_32x32x16_bf16 v[64:79], a[80:83], a[232:235], v[64:79]\n"
    "s_waitcnt vmcnt(5)\n"
    "ds_read_b64_tr_b16 a[64:65], v12 offset:11264\n"
    "ds_read_b64_tr_b16 a[66:67], v12 offset:11520\n"
    "ds_read_b64_tr_b16 a[68:69], v12 offset:11648\n"
    "ds_read_b64_tr_b16 a[70:71], v12 offset:11904\n"
    "s_mul_i32 s72, s44, 21504\n"
    "v_add_u32 v25, s72, v25\n"
    "v_mov_b32 v34, v25\n"
    "s_mul_i32 s72, s44, 163840\n"
    "s_mov_b32 exec_hi, 0\n"
    "v_add_u32 v34, s72, v34\n"
    "s_mov_b32 exec_hi, -1\n"
    "s_mul_i32 s72, s44, 1632\n"
    "v_add_u32 v19, s72, v19\n"
    "v_cvt_pk_bf16_f32 v48, v48, v49\n"
    "v_cvt_pk_bf16_f32 v49, v50, v51\n"
    "v_cvt_pk_bf16_f32 v50, v52, v53\n"
    "v_cvt_pk_bf16_f32 v51, v54, v55\n"
    "v_cvt_pk_bf16_f32 v52, v56, v57\n"
    "ds_write_b128 v15, v[48:51] offset:2176\n"
    "v_cvt_pk_bf16_f32 v53, v58, v59\n"
    "v_cvt_pk_bf16_f32 v54, v60, v61\n"
    "v_cvt_pk_bf16_f32 v55, v62, v63\n"
    "s_nop 0\n"
    "ds_write_b128 v15, v[52:55] offset:2720\n"
    "s_waitcnt lgkmcnt(4)\n"
    "v_mfma_f32_32x32x16_bf16 v[48:63], a[64:67], a[228:231], 0\n"
    "s_waitcnt lgkmcnt(2)\n"
    "v_mfma_f32_32x32x16_bf16 v[48:63], a[68:71], a[232:235], v[48:63]\n"
    "s_waitcnt vmcnt(2)\n"
    "ds_read_b64_tr_b16 a[76:77], v37 offset:14080\n"
    "ds_read_b64_tr_b16 a[78:79], v37 offset:14336\n"
    "ds_read_b64_tr_b16 a[80:81], v37 offset:14464\n"
    "ds_read_b64_tr_b16 a[82:83], v37 offset:14720\n"
    "ds_read_b64_tr_b16 a[84:85], v31\n"
    "ds_read_b64_tr_b16 a[86:87], v32\n"
    "v_xor_b32 v12, 32, v12\n"
    "v_xor_b32 v13, 32, v13\n"
    "v_xor_b32 v14, 32, v14\n"
    "v_xor_b32 v31, 32, v31\n"
    "v_xor_b32 v32, 32, v32\n"
    "v_xor_b32 v37, 32, v37\n"
    "v_cvt_pk_bf16_f32 v64, v64, v65\n"
    "v_cvt_pk_bf16_f32 v65, v66, v67\n"
    "v_cvt_pk_bf16_f32 v66, v68, v69\n"
    "v_cvt_pk_bf16_f32 v67, v70, v71\n"
    "v_cvt_pk_bf16_f32 v68, v72, v73\n"
    "ds_write_b128 v15, v[64:67] offset:3264\n"
    "v_cvt_pk_bf16_f32 v69, v74, v75\n"
    "v_cvt_pk_bf16_f32 v70, v76, v77\n"
    "v_cvt_pk_bf16_f32 v71, v78, v79\n"
    "s_nop 0\n"
    "ds_write_b128 v15, v[68:71] offset:3808\n"
    "s_waitcnt lgkmcnt(6)\n"
    "v_mfma_f32_32x32x16_bf16 v[64:79], a[76:79], a[228:231], 0\n"
    "s_waitcnt lgkmcnt(4)\n"
    "v_mfma_f32_32x32x16_bf16 v[64:79], a[80:83], a[232:235], v[64:79]\n"
    "s_waitcnt lgkmcnt(2)\n"
    "v_mfma_f32_32x32x16_bf16 v[80:95], a[84:87], a[240:243], v[80:95]\n"
    "s_add_u32 m0, s60, 21504\n"
    "s_add_u32 s80, s63, 387072\n"
    "buffer_load_dwordx4 v9, s[20:23], s80 offen lds\n"
    "v_cvt_pk_bf16_f32 v48, v48, v49\n"
    "v_cvt_pk_bf16_f32 v49, v50, v51\n"
    "v_cvt_pk_bf16_f32 v50, v52, v53\n"
    "v_cvt_pk_bf16_f32 v51, v54, v55\n"
    "v_cvt_pk_bf16_f32 v52, v56, v57\n"
    "ds_write_b128 v15, v[48:51] offset:4352\n"
    "v_cvt_pk_bf16_f32 v53, v58, v59\n"
    "v_cvt_pk_bf16_f32 v54, v60, v61\n"
    "s_mov_b32 m0, s61\n"
    "s_add_u32 s80, s64, 387072\n"
    "buffer_load_dwordx4 v9, s[20:23], s80 offen lds\n"
    "s_add_u32 s61, s61, s62\n"
    "v_cvt_pk_bf16_f32 v55, v62, v63\n"
    "s_nop 0\n"
    "ds_write_b128 v15, v[52:55] offset:4896\n"
    "v_cvt_pk_bf16_f32 v64, v64, v65\n"
    "v_cvt_pk_bf16_f32 v65, v66, v67\n"
    "v_cvt_pk_bf16_f32 v66, v68, v69\n"
    "v_cvt_pk_bf16_f32 v67, v70, v71\n"
    "v_cvt_pk_bf16_f32 v68, v72, v73\n"
    "s_add_u32 m0, s60, 28672\n"
    "s_add_u32 s80, s63, 516096\n"
    "buffer_load_dwordx4 v9, s[20:23], s80 offen lds\n"
    "ds_write_b128 v33, v[64:67] offset:5440\n"
    "v_cvt_pk_bf16_f32 v69, v74, v75\n"
    "v_cvt_pk_bf16_f32 v70, v76, v77\n"
    "v_cvt_pk_bf16_f32 v71, v78, v79\n"
    "s_nop 0\n"
    "ds_write_b128 v33, v[68:71] offset:5984\n"
    "v_cvt_pk_bf16_f32 v80, v80, v81\n"
    "v_cvt_pk_bf16_f32 v81, v82, v83\n"
    "s_mov_b32 m0, s61\n"
    "s_add_u32 s80, s64, 516096\n"
    "buffer_load_dwordx4 v9, s[20:23], s80 offen lds\n"
    "s_add_u32 s61, s61, s62\n"
    "v_cvt_pk_bf16_f32 v82, v84, v85\n"
    "v_cvt_pk_bf16_f32 v83, v86, v87\n"
    "v_cvt_pk_bf16_f32 v84, v88, v89\n"
    "ds_write_b128 v16, v[80:83] offset:0\n"
    "v_cvt_pk_bf16_f32 v85, v90, v91\n"
    "v_cvt_pk_bf16_f32 v86, v92, v93\n"
    "v_cvt_pk_bf16_f32 v87, v94, v95\n"
    "s_nop 0\n"
    "ds_write_b128 v16, v[84:87] offset:544\n"
    "s_add_u32 m0, s60, 35840\n"
    "s_add_u32 s80, s63, 645120\n"
    "buffer_load_dwordx4 v9, s[20:23], s80 offen lds\n"
    "s_mov_b32 m0, s61\n"
    "s_add_u32 s80, s64, 645120\n"
    "buffer_load_dwordx4 v9, s[20:23], s80 offen lds\n"
    "s_add_u32 s61, s61, s62\n"
    "s_add_u32 m0, s60, 43008\n"
    "s_add_u32 s80, s63, 774144\n"
    "buffer_load_dwordx4 v9, s[20:23], s80 offen lds\n"
    "s_mov_b32 m0, s61\n"
    "s_add_u32 s80, s64, 774144\n"
    "buffer_load_dwordx4 v9, s[20:23], s80 offen lds\n"
    "s_add_u32 s61, s61, s62\n"
    "s_mov_b32 s74, 1695744\n"
    "s_waitcnt vmcnt(8)\n"
    "s_waitcnt lgkmcnt(0)\n"
    "s_barrier\n"
    "ds_read_b128 a[0:3], v18 offset:0\n"
    "ds_read_b128 a[4:7], v18 offset:544\n"
    "ds_read_b128 a[8:11], v18 offset:1088\n"
    "ds_read_b64_tr_b16 a[128:129], v24 offset:14336\n"
    "ds_read_b64_tr_b16 a[130:131], v28 offset:14336\n"
    "ds_read_b64_tr_b16 a[132:133], v29 offset:14336\n"
    "ds_read_b64_tr_b16 a[134:135], v30 offset:14336\n"
    "ds_read_b64_tr_b16 a[136:137], v24 offset:14848\n"
    "ds_read_b64_tr_b16 a[138:139], v28 offset:14848\n"
    "ds_read_b64_tr_b16 a[140:141], v29 offset:14848\n"
    "ds_read_b64_tr_b16 a[142:143], v30 offset:14848\n"
    "ds_read_b128 a[12:15], v18 offset:1632\n"
    "ds_read_b128 a[16:19], v18 offset:2176\n"
    "ds_read_b128 a[20:23], v18 offset:2720\n"
    "ds_read_b128 a[24:27], v18 offset:3264\n"
    "ds_read_b128 a[28:31], v18 offset:3808\n"
    "ds_read_b128 a[32:35], v18 offset:4352\n"
    "ds_read_b128 a[36:39], v18 offset:4896\n"
    "s_waitcnt lgkmcnt(15)\n"
    "v_mfma_f32_16x16x32_bf16 v[96:99], v[136:139], a[0:3], 0\n"
    "v_mfma_f32_16x16x32_bf16 v[100:103], v[136:139], a[4:7], 0\n"
    "s_waitcnt lgkmcnt(7)\n"
    "ds_read_b128 a[64:67], v18 offset:80\n"
    "v_mfma_f32_4x4x4_16b_bf16 v[48:51], v[20:21], a[128:129], 0\n"
    "v_mfma_f32_4x4x4_16b_bf16 v[52:55], v[20:21], a[130:131], 0\n"
    "v_mfma_f32_4x4x4_16b_bf16 v[56:59], v[20:21], a[132:133], 0\n"
    "v_mfma_f32_4x4x4_16b_bf16 v[60:63], v[20:21], a[134:135], 0\n"
    "v_mfma_f32_4x4x4_16b_bf16 v[64:67], v[20:21], a[136:137], 0\n"
    "v_mfma_f32_4x4x4_16b_bf16 v[68:71], v[20:21], a[138:139], 0\n"
    "v_mfma_f32_4x4x4_16b_bf16 v[72:75], v[20:21], a[140:141], 0\n"
    "v_mfma_f32_4x4x4_16b_bf16 v[76:79], v[20:21], a[142:143], 0\n"
    "v_cvt_pk_bf16_f32 v144, v48, v52\n"
    "v_mfma_f32_16x16x32_bf16 v[104:107], v[136:139], a[8:11], 0\n"
    "s_waitcnt vmcnt(6) lgkmcnt(7)\n"
    "s_barrier\n"
    "v_cvt_pk_bf16_f32 v145, v56, v60\n"
    "v_mfma_f32_16x16x32_bf16 v[108:111], v[136:139], a[12:15], 0\n"
    "s_waitcnt lgkmcnt(6)\n"
    "ds_read_b64_tr_b16 a[144:145], v24 offset:21504\n"
    "ds_read_b64_tr_b16 a[146:147], v28 offset:21504\n"
    "v_mfma_f32_16x16x32_bf16 v[112:115], v[136:139], a[16:19], 0\n"
    "s_waitcnt lgkmcnt(7)\n"
    "ds_read_b64_tr_b16 a[148:149], v29 offset:21504\n"
    "ds_read_b64_tr_b16 a[150:151], v30 offset:21504\n"
    "v_mfma_f32_16x16x32_bf16 v[116:119], v[136:139], a[20:23], 0\n"
    "s_waitcnt lgkmcnt(8)\n"
    "ds_read_b64_tr_b16 a[152:153], v24 offset:22016\n"
    "ds_read_b64_tr_b16 a[154:155], v28 offset:22016\n"
    "v_mfma_f32_16x16x32_bf16 v[120:123], v[136:139], a[24:27], 0\n"
    "s_waitcnt lgkmcnt(9)\n"
    "ds_read_b64_tr_b16 a[156:157], v29 offset:22016\n"
    "ds_read_b64_tr_b16 a[158:159], v30 offset:22016\n"
    "v_mfma_f32_16x16x32_bf16 v[124:127], v[136:139], a[28:31], 0\n"
    "s_waitcnt lgkmcnt(10)\n"
    "v_cvt_pk_bf16_f32 v146, v64, v68\n"
    "v_cvt_pk_bf16_f32 v147, v72, v76\n"
    "v_mfma_f32_16x16x32_bf16 v[128:131], v[136:139], a[32:35], 0\n"
    "s_waitcnt lgkmcnt(9)\n"
    "ds_read_b128 a[40:43], v18 offset:5440\n"
    "v_cvt_pk_bf16_f32 v184, v49, v53\n"
    "v_mfma_f32_16x16x32_bf16 v[132:135], v[136:139], a[36:39], 0\n"
    "v_cvt_pk_bf16_f32 v185, v57, v61\n"
    "v_cvt_pk_bf16_f32 v186, v65, v69\n"
    "v_mfma_f32_16x16x32_bf16 v[96:99], v[140:143], a[4:7], v[96:99]\n"
    "v_cvt_pk_bf16_f32 v187, v73, v77\n"
    "v_cvt_pk_bf16_f32 v224, v50, v54\n"
    "v_mfma_f32_16x16x32_bf16 v[100:103], v[140:143], a[8:11], v[100:103]\n"
    "v_cvt_pk_bf16_f32 v225, v58, v62\n"
    "v_cvt_pk_bf16_f32 v226, v66, v70\n"
    "v_mfma_f32_16x16x32_bf16 v[104:107], v[140:143], a[12:15], v[104:107]\n"
    "v_cvt_pk_bf16_f32 v227, v74, v78\n"
    "ds_read_b128 a[68:71], v18 offset:624\n"
    "v_mfma_f32_16x16x32_bf16 v[108:111], v[140:143], a[16:19], v[108:111]\n"
    "ds_read_b128 a[72:75], v18 offset:1168\n"
    "ds_read_b128 a[76:79], v18 offset:1712\n"
    "v_mfma_f32_16x16x32_bf16 v[112:115], v[140:143], a[20:23], v[112:115]\n"
    "s_waitcnt lgkmcnt(4)\n"
    "v_mfma_f32_4x4x4_16b_bf16 v[48:51], v[20:21], a[144:145], 0\n"
    "v_mfma_f32_4x4x4_16b_bf16 v[52:55], v[20:21], a[146:147], 0\n"
    "v_mfma_f32_4x4x4_16b_bf16 v[56:59], v[20:21], a[148:149], 0\n"
    "v_mfma_f32_4x4x4_16b_bf16 v[60:63], v[20:21], a[150:151], 0\n"
    "v_mfma_f32_4x4x4_16b_bf16 v[64:67], v[20:21], a[152:153], 0\n"
    "v_mfma_f32_4x4x4_16b_bf16 v[68:71], v[20:21], a[154:155], 0\n"
    "v_mfma_f32_4x4x4_16b_bf16 v[72:75], v[20:21], a[156:157], 0\n"
    "v_mfma_f32_4x4x4_16b_bf16 v[76:79], v[20:21], a[158:159], 0\n"
    "v_cvt_pk_bf16_f32 v148, v48, v52\n"
    "v_mfma_f32_16x16x32_bf16 v[116:119], v[140:143], a[24:27], v[116:119]\n"
    "s_waitcnt vmcnt(4)\n"
    "s_barrier\n"
    "v_cvt_pk_bf16_f32 v149, v56, v60\n"
    "v_mfma_f32_16x16x32_bf16 v[120:123], v[140:143], a[28:31], v[120:123]\n"
    "ds_read_b64_tr_b16 a[128:129], v24 offset:28672\n"
    "ds_read_b64_tr_b16 a[130:131], v28 offset:28672\n"
    "v_mfma_f32_16x16x32_bf16 v[124:127], v[140:143], a[32:35], v[124:127]\n"
    "ds_read_b64_tr_b16 a[132:133], v29 offset:28672\n"
    "ds_read_b64_tr_b16 a[134:135], v30 offset:28672\n"
    "v_mfma_f32_16x16x32_bf16 v[128:131], v[140:143], a[36:39], v[128:131]\n"
    "s_waitcnt lgkmcnt(7)\n"
    "ds_read_b128 a[44:47], v18 offset:5984\n"
    "ds_read_b64_tr_b16 a[136:137], v24 offset:29184\n"
    "v_mfma_f32_16x16x32_bf16 v[132:135], v[140:143], a[40:43], v[132:135]\n"
    "ds_read_b64_tr_b16 a[138:139], v28 offset:29184\n"
    "ds_read_b64_tr_b16 a[140:141], v29 offset:29184\n"
    "v_mfma_f32_16x16x32_bf16 v[96:99], v[144:147], a[8:11], v[96:99]\n"
    "ds_read_b64_tr_b16 a[142:143], v30 offset:29184\n"
    "v_cvt_pk_bf16_f32 v150, v64, v68\n"
    "v_mfma_f32_16x16x32_bf16 v[100:103], v[144:147], a[12:15], v[100:103]\n"
    "v_cvt_pk_bf16_f32 v151, v72, v76\n"
    "v_cvt_pk_bf16_f32 v188, v49, v53\n"
    "v_mfma_f32_16x16x32_bf16 v[104:107], v[144:147], a[16:19], v[104:107]\n"
    "v_cvt_pk_bf16_f32 v189, v57, v61\n"
    "v_cvt_pk_bf16_f32 v190, v65, v69\n"
    "v_mfma_f32_16x16x32_bf16 v[108:111], v[144:147], a[20:23], v[108:111]\n"
    "v_cvt_pk_bf16_f32 v191, v73, v77\n"
    "v_cvt_pk_bf16_f32 v228, v50, v54\n"
    "v_mfma_f32_16x16x32_bf16 v[112:115], v[144:147], a[24:27], v[112:115]\n"
    "v_cvt_pk_bf16_f32 v229, v58, v62\n"
    "v_cvt_pk_bf16_f32 v230, v66, v70\n"
    "v_mfma_f32_16x16x32_bf16 v[116:119], v[144:147], a[28:31], v[116:119]\n"
    "v_cvt_pk_bf16_f32 v231, v74, v78\n"
    "ds_read_b128 a[80:83], v18 offset:2256\n"
    "v_mfma_f32_16x16x32_bf16 v[120:123], v[144:147], a[32:35], v[120:123]\n"
    "ds_read_b128 a[84:87], v18 offset:2800\n"
    "ds_read_b128 a[88:91], v18 offset:3344\n"
    "v_mfma_f32_16x16x32_bf16 v[124:127], v[144:147], a[36:39], v[124:127]\n"
    "s_waitcnt lgkmcnt(3)\n"
    "ds_read_b128 a[92:95], v18 offset:3888\n"
    "v_mfma_f32_4x4x4_16b_bf16 v[48:51], v[20:21], a[128:129], 0\n"
    "v_mfma_f32_4x4x4_16b_bf16 v[52:55], v[20:21], a[130:131], 0\n"
    "v_mfma_f32_4x4x4_16b_bf16 v[56:59], v[20:21], a[132:133], 0\n"
    "v_mfma_f32_4x4x4_16b_bf16 v[60:63], v[20:21], a[134:135], 0\n"
    "v_mfma_f32_4x4x4_16b_bf16 v[64:67], v[20:21], a[136:137], 0\n"
    "v_mfma_f32_4x4x4_16b_bf16 v[68:71], v[20:21], a[138:139], 0\n"
    "v_mfma_f32_4x4x4_16b_bf16 v[72:75], v[20:21], a[140:141], 0\n"
    "v_mfma_f32_4x4x4_16b_bf16 v[76:79], v[20:21], a[142:143], 0\n"
    "v_cvt_pk_bf16_f32 v152, v48, v52\n"
    "v_mfma_f32_16x16x32_bf16 v[128:131], v[144:147], a[40:43], v[128:131]\n"
    "s_waitcnt vmcnt(2)\n"
    "ds_read_b128 a[48:51], v18 offset:6528\n"
    "s_barrier\n"
    "v_mfma_f32_16x16x32_bf16 v[132:135], v[144:147], a[44:47], v[132:135]\n"
    "ds_read_b64_tr_b16 a[144:145], v24 offset:35840\n"
    "ds_read_b64_tr_b16 a[146:147], v28 offset:35840\n"
    "v_mfma_f32_16x16x32_bf16 v[96:99], v[148:151], a[12:15], v[96:99]\n"
    "ds_read_b64_tr_b16 a[148:149], v29 offset:35840\n"
    "ds_read_b64_tr_b16 a[150:151], v30 offset:35840\n"
    "v_mfma_f32_16x16x32_bf16 v[100:103], v[148:151], a[16:19], v[100:103]\n"
    "ds_read_b64_tr_b16 a[152:153], v24 offset:36352\n"
    "ds_read_b64_tr_b16 a[154:155], v28 offset:36352\n"
    "v_mfma_f32_16x16x32_bf16 v[104:107], v[148:151], a[20:23], v[104:107]\n"
    "ds_read_b64_tr_b16 a[156:157], v29 offset:36352\n"
    "ds_read_b64_tr_b16 a[158:159], v30 offset:36352\n"
    "v_mfma_f32_16x16x32_bf16 v[108:111], v[148:151], a[24:27], v[108:111]\n"
    "v_cvt_pk_bf16_f32 v153, v56, v60\n"
    "v_cvt_pk_bf16_f32 v154, v64, v68\n"
    "v_mfma_f32_16x16x32_bf16 v[112:115], v[148:151], a[28:31], v[112:115]\n"
    "v_cvt_pk_bf16_f32 v155, v72, v76\n"
    "v_cvt_pk_bf16_f32 v192, v49, v53\n"
    "v_mfma_f32_16x16x32_bf16 v[116:119], v[148:151], a[32:35], v[116:119]\n"
    "v_cvt_pk_bf16_f32 v193, v57, v61\n"
    "v_cvt_pk_bf16_f32 v194, v65, v69\n"
    "v_mfma_f32_16x16x32_bf16 v[120:123], v[148:151], a[36:39], v[120:123]\n"
    "v_cvt_pk_bf16_f32 v195, v73, v77\n"
    "v_cvt_pk_bf16_f32 v232, v50, v54\n"
    "v_mfma_f32_16x16x32_bf16 v[124:127], v[148:151], a[40:43], v[124:127]\n"
    "v_cvt_pk_bf16_f32 v233, v58, v62\n"
    "v_cvt_pk_bf16_f32 v234, v66, v70\n"
    "v_mfma_f32_16x16x32_bf16 v[128:131], v[148:151], a[44:47], v[128:131]\n"
    "s_waitcnt lgkmcnt(8)\n"
    "ds_read_b128 a[52:55], v18 offset:7072\n"
    "v_cvt_pk_bf16_f32 v235, v74, v78\n"
    "v_mfma_f32_16x16x32_bf16 v[132:135], v[148:151], a[48:51], v[132:135]\n"
    "ds_read_b128 a[96:99], v18 offset:4432\n"
    "ds_read_b128 a[100:103], v18 offset:4976\n"
    "v_mfma_f32_16x16x32_bf16 v[96:99], v[152:155], a[16:19], v[96:99]\n"
    "s_waitcnt lgkmcnt(3)\n"
    "ds_read_b128 a[104:107], v18 offset:5520\n"
    "v_mfma_f32_4x4x4_16b_bf16 v[48:51], v[20:21], a[144:145], 0\n"
    "v_mfma_f32_4x4x4_16b_bf16 v[52:55], v[20:21], a[146:147], 0\n"
    "v_mfma_f32_4x4x4_16b_bf16 v[56:59], v[20:21], a[148:149], 0\n"
    "v_mfma_f32_4x4x4_16b_bf16 v[60:63], v[20:21], a[150:151], 0\n"
    "v_mfma_f32_4x4x4_16b_bf16 v[64:67], v[20:21], a[152:153], 0\n"
    "v_mfma_f32_4x4x4_16b_bf16 v[68:71], v[20:21], a[154:155], 0\n"
    "v_mfma_f32_4x4x4_16b_bf16 v[72:75], v[20:21], a[156:157], 0\n"
    "v_mfma_f32_4x4x4_16b_bf16 v[76:79], v[20:21], a[158:159], 0\n"
    "v_cvt_pk_bf16_f32 v156, v48, v52\n"
    "v_mfma_f32_16x16x32_bf16 v[100:103], v[152:155], a[20:23], v[100:103]\n"
    "v_cvt_pk_bf16_f32 v157, v56, v60\n"
    "v_cvt_pk_bf16_f32 v158, v64, v68\n"
    "v_mfma_f32_16x16x32_bf16 v[104:107], v[152:155], a[24:27], v[104:107]\n"
    "s_waitcnt vmcnt(0)\n"
    "v_cvt_pk_bf16_f32 v159, v72, v76\n"
    "s_barrier\n"
    "v_mfma_f32_16x16x32_bf16 v[108:111], v[152:155], a[28:31], v[108:111]\n"
    "ds_read_b64_tr_b16 a[128:129], v24 offset:43008\n"
    "ds_read_b64_tr_b16 a[130:131], v28 offset:43008\n"
    "v_mfma_f32_16x16x32_bf16 v[112:115], v[152:155], a[32:35], v[112:115]\n"
    "ds_read_b64_tr_b16 a[132:133], v29 offset:43008\n"
    "ds_read_b64_tr_b16 a[134:135], v30 offset:43008\n"
    "v_mfma_f32_16x16x32_bf16 v[116:119], v[152:155], a[36:39], v[116:119]\n"
    "ds_read_b64_tr_b16 a[136:137], v24 offset:43520\n"
    "ds_read_b64_tr_b16 a[138:139], v28 offset:43520\n"
    "v_mfma_f32_16x16x32_bf16 v[120:123], v[152:155], a[40:43], v[120:123]\n"
    "ds_read_b64_tr_b16 a[140:141], v29 offset:43520\n"
    "ds_read_b64_tr_b16 a[142:143], v30 offset:43520\n"
    "v_mfma_f32_16x16x32_bf16 v[124:127], v[152:155], a[44:47], v[124:127]\n"
    "v_cvt_pk_bf16_f32 v196, v49, v53\n"
    "v_cvt_pk_bf16_f32 v197, v57, v61\n"
    "v_mfma_f32_16x16x32_bf16 v[128:131], v[152:155], a[48:51], v[128:131]\n"
    "s_waitcnt lgkmcnt(9)\n"
    "ds_read_b128 a[56:59], v18 offset:7616\n"
    "v_cvt_pk_bf16_f32 v198, v65, v69\n"
    "v_mfma_f32_16x16x32_bf16 v[132:135], v[152:155], a[52:55], v[132:135]\n"
    "v_cvt_pk_bf16_f32 v199, v73, v77\n"
    "v_cvt_pk_bf16_f32 v236, v50, v54\n"
    "v_mfma_f32_16x16x32_bf16 v[96:99], v[156:159], a[20:23], v[96:99]\n"
    "v_cvt_pk_bf16_f32 v237, v58, v62\n"
    "v_cvt_pk_bf16_f32 v238, v66, v70\n"
    "v_mfma_f32_16x16x32_bf16 v[100:103], v[156:159], a[24:27], v[100:103]\n"
    "v_cvt_pk_bf16_f32 v239, v74, v78\n"
    "ds_read_b64_tr_b16 a[220:221], v13\n"
    "v_mfma_f32_16x16x32_bf16 v[104:107], v[156:159], a[28:31], v[104:107]\n"
    "ds_read_b64_tr_b16 a[222:223], v14\n"
    "buffer_load_dword v36, off, s[28:31], s74 offset:2048 sc1\n"
    "v_mfma_f32_16x16x32_bf16 v[108:111], v[156:159], a[32:35], v[108:111]\n"
    "s_waitcnt lgkmcnt(3)\n"
    "ds_read_b128 a[108:111], v18 offset:6064\n"
    "ds_read_b128 a[112:115], v18 offset:6608\n"
    "v_mfma_f32_4x4x4_16b_bf16 v[48:51], v[20:21], a[128:129], 0\n"
    "v_mfma_f32_4x4x4_16b_bf16 v[52:55], v[20:21], a[130:131], 0\n"
    "v_mfma_f32_4x4x4_16b_bf16 v[56:59], v[20:21], a[132:133], 0\n"
    "v_mfma_f32_4x4x4_16b_bf16 v[60:63], v[20:21], a[134:135], 0\n"
    "v_mfma_f32_4x4x4_16b_bf16 v[64:67], v[20:21], a[136:137], 0\n"
    "v_mfma_f32_4x4x4_16b_bf16 v[68:71], v[20:21], a[138:139], 0\n"
    "v_mfma_f32_4x4x4_16b_bf16 v[72:75], v[20:21], a[140:141], 0\n"
    "v_mfma_f32_4x4x4_16b_bf16 v[76:79], v[20:21], a[142:143], 0\n"
    "v_cvt_pk_bf16_f32 v160, v48, v52\n"
    "v_mfma_f32_16x16x32_bf16 v[112:115], v[156:159], a[36:39], v[112:115]\n"
    "v_cvt_pk_bf16_f32 v161, v56, v60\n"
    "v_cvt_pk_bf16_f32 v162, v64, v68\n"
    "v_mfma_f32_16x16x32_bf16 v[116:119], v[156:159], a[40:43], v[116:119]\n"
    "v_cvt_pk_bf16_f32 v163, v72, v76\n"
    "v_cvt_pk_bf16_f32 v200, v49, v53\n"
    "v_mfma_f32_16x16x32_bf16 v[120:123], v[156:159], a[44:47], v[120:123]\n"
    "ds_read_b64_tr_b16 a[212:213], v34 offset:0\n"
    "ds_read_b64_tr_b16 a[214:215], v34 offset:512\n"
    "v_mfma_f32_16x16x32_bf16 v[124:127], v[156:159], a[48:51], v[124:127]\n"
    "ds_read_b64_tr_b16 a[216:217], v25 offset:14336\n"
    "ds_read_b64_tr_b16 a[218:219], v25 offset:14848\n"
    "v_mfma_f32_16x16x32_bf16 v[128:131], v[156:159], a[52:55], v[128:131]\n"
    "s_waitcnt lgkmcnt(8)\n"
    "ds_read_b128 a[60:63], v18 offset:8160\n"
    "v_cvt_pk_bf16_f32 v201, v57, v61\n"
    "v_mfma_f32_16x16x32_bf16 v[132:135], v[156:159], a[56:59], v[132:135]\n"
    "v_cvt_pk_bf16_f32 v202, v65, v69\n"
    "v_cvt_pk_bf16_f32 v203, v73, v77\n"
    "v_mfma_f32_16x16x32_bf16 v[96:99], v[160:163], a[24:27], v[96:99]\n"
    "v_cvt_pk_bf16_f32 v240, v50, v54\n"
    "v_cvt_pk_bf16_f32 v241, v58, v62\n"
    "v_mfma_f32_16x16x32_bf16 v[100:103], v[160:163], a[28:31], v[100:103]\n"
    "s_waitcnt lgkmcnt(0)\n"
    "s_barrier\n"
    "v_mfma_f32_16x16x32_bf16 v[104:107], v[160:163], a[32:35], v[104:107]\n"
    "v_cvt_pk_bf16_f32 v242, v66, v70\n"
    "v_cvt_pk_bf16_f32 v243, v74, v78\n"
    "v_mfma_f32_16x16x32_bf16 v[108:111], v[160:163], a[36:39], v[108:111]\n"
    "ds_read_b128 a[116:119], v18 offset:7152\n"
    "ds_read_b128 a[120:123], v18 offset:7696\n"
    "v_mfma_f32_16x16x32_bf16 v[112:115], v[160:163], a[40:43], v[112:115]\n"
    "ds_read_b128 a[124:127], v18 offset:8240\n"
    "v_min_u32 v17, 20, v4\n"
    "v_mfma_f32_32x32x16_bf16 v[80:95], a[220:223], a[236:239], 0\n"
    "v_lshlrev_b32 v17, 2, v17\n"
    "v_min_u32 v27, 41, v4\n"
    "v_lshlrev_b32 v27, 3, v27\n"
    "s_cmp_lt_u32 s78, 21\n"
    "s_cselect_b32 s60, 1073741824, s65\n"
    "s_cselect_b32 s61, s65, 1073741824\n"
    "s_mov_b32 s52, 0\n"
    "v_mfma_f32_16x16x32_bf16 v[116:119], v[160:163], a[44:47], v[116:119]\n"
    "s_lshl_b32 s72, s36, 2\n"
    "ds_read_b64_tr_b16 a[224:225], v12 offset:0\n"
    "ds_read_b64_tr_b16 a[226:227], v12 offset:256\n"
    "v_mfma_f32_16x16x32_bf16 v[120:123], v[160:163], a[48:51], v[120:123]\n"
    "s_add_u32 s72, s72, 1695744\n"
    "v_mov_b32 v35, s72\n"
    "s_lshl_b32 s72, s37, 2\n"
    "v_mfma_f32_16x16x32_bf16 v[124:127], v[160:163], a[52:55], v[124:127]\n"
    "s_lshl_b32 s73, s43, 1\n"
    "s_add_u32 s72, s72, s73\n"
    "s_add_u32 s72, s72, s44\n"
    "v_mfma_f32_16x16x32_bf16 v[128:131], v[160:163], a[56:59], v[128:131]\n"
    "s_mul_i32 s50, s72, 141312\n"
    "s_add_u32 s50, s50, 23552\n"
    "s_xor_b32 s72, s38, 1\n"
    "v_mfma_f32_16x16x32_bf16 v[132:135], v[160:163], a[60:63], v[132:135]\n"
    "s_mul_i32 s73, s72, 0\n"
    "s_add_u32 s50, s50, s73\n"
    "s_mul_i32 s73, s72, 0\n"
    "v_mfma_f32_16x16x32_bf16 v[136:139], v[176:179], a[64:67], 0\n"
    "s_sub_u32 s62, 3072, s73\n"
    "s_xor_b32 s72, s42, 2\n"
    "s_min_u32 s73, s72, 2\n"
    "v_mfma_f32_16x16x32_bf16 v[136:139], v[180:183], a[68:71], v[136:139]\n"
    "s_mul_i32 s72, s72, 5\n"
    "ds_read_b128 a[64:67], v19 offset:0\n"
    "s_add_u32 s72, s72, s73\n"
    "v_mfma_f32_16x16x32_bf16 v[136:139], v[184:187], a[72:75], v[136:139]\n"
    "s_waitcnt lgkmcnt(1)\n"
    "ds_read_b128 a[0:3], v18 offset:160\n"
    "s_mul_i32 s49, s72, 2816\n"
    "v_mfma_f32_32x32x16_bf16 v[48:63], a[224:227], a[228:231], 0\n"
    "s_add_u32 s49, s49, 100352\n"
    "s_mov_b32 s26, s18\n"
    "s_mov_b32 s27, s19\n"
    "s_movk_i32 s72, 13312\n"
    "buffer_load_dwordx4 v[40:43], v5, s[12:15], s72 offen\n"
    "v_mfma_f32_16x16x32_bf16 v[136:139], v[188:191], a[76:79], v[136:139]\n"
    "s_mul_i32 s53, s39, 72\n"
    "ds_read_b64_tr_b16 a[220:221], v12 offset:384\n"
    "ds_read_b64_tr_b16 a[222:223], v12 offset:640\n"
    "v_mfma_f32_16x16x32_bf16 v[136:139], v[192:195], a[80:83], v[136:139]\n"
    "s_add_u32 s53, s53, s41\n"
    "ds_read_b128 a[4:7], v18 offset:704\n"
    "s_mul_i32 s53, s53, 43008\n"
    "v_mfma_f32_16x16x32_bf16 v[136:139], v[196:199], a[84:87], v[136:139]\n"
    "s_lshl_b32 s73, s40, 4\n"
    "s_add_u32 s73, s73, s42\n"
    "s_lshl_b32 s73, s73, 1\n"
    "v_mfma_f32_16x16x32_bf16 v[136:139], v[200:203], a[88:91], v[136:139]\n"
    "s_add_u32 s73, s73, s38\n"
    "s_mul_i32 s73, s73, 192\n"
    "s_add_u32 s53, s53, s73\n"
    "v_mfma_f32_16x16x32_bf16 v[140:143], v[176:179], a[68:71], 0\n"
    "s_lshl_b32 s73, s37, 6\n"
    "s_add_u32 s53, s53, s73\n"
    "v_mfma_f32_16x16x32_bf16 v[140:143], v[180:183], a[72:75], v[140:143]\n"
    "ds_read_b128 a[68:71], v19 offset:544\n"
    "v_mfma_f32_16x16x32_bf16 v[140:143], v[184:187], a[76:79], v[140:143]\n"
    "ds_read_b128 a[8:11], v18 offset:1248\n"
    "v_mfma_f32_16x16x32_bf16 v[140:143], v[188:191], a[80:83], v[140:143]\n"
    "s_waitcnt vmcnt(1) lgkmcnt(6)\n"
    "v_cmp_ne_u32 vcc, v36, v38\n"
    "s_cbranch_vccz 25f\n"
    "26:\n"
    "v_mfma_f32_16x16x32_bf16 v[140:143], v[192:195], a[84:87], v[140:143]\n"
    "s_waitcnt lgkmcnt(3)\n"
    "v_mfma_f32_32x32x16_bf16 v[48:63], a[220:223], a[232:235], v[48:63]\n"
    "s_add_u32 s58, s51, 0\n"
    "ds_write_b128 v11, v[44:47] offset:23936\n"
    "s_add_u32 m0, s48, 0\n"
    "v_mfma_f32_16x16x32_bf16 v[140:143], v[196:199], a[88:91], v[140:143]\n"
    "v_add_u32 v10, s58, v6\n"
    "v_mfma_f32_16x16x32_bf16 v[140:143], v[200:203], a[92:95], v[140:143]\n"
    "buffer_load_dwordx4 v10, s[16:19], 0 offen lds\n"
    "v_mfma_f32_16x16x32_bf16 v[144:147], v[176:179], a[72:75], 0\n"
    "ds_read_b64_tr_b16 a[224:225], v12 offset:2816\n"
    "ds_read_b64_tr_b16 a[226:227], v12 offset:3072\n"
    "v_mfma_f32_16x16x32_bf16 v[144:147], v[180:183], a[76:79], v[144:147]\n"
    "v_cvt_pk_bf16_f32 v48, v48, v49\n"
    "v_cvt_pk_bf16_f32 v49, v50, v51\n"
    "v_mfma_f32_16x16x32_bf16 v[144:147], v[184:187], a[80:83], v[144:147]\n"
    "v_cvt_pk_bf16_f32 v50, v52, v53\n"
    "v_cvt_pk_bf16_f32 v51, v54, v55\n"
    "v_mfma_f32_16x16x32_bf16 v[144:147], v[188:191], a[84:87], v[144:147]\n"
    "v_cvt_pk_bf16_f32 v52, v56, v57\n"
    "v_cvt_pk_bf16_f32 v53, v58, v59\n"
    "v_mfma_f32_16x16x32_bf16 v[144:147], v[192:195], a[88:91], v[144:147]\n"
    "s_add_u32 m0, s48, 1024\n"
    "v_cvt_pk_bf16_f32 v54, v60, v61\n"
    "v_cvt_pk_bf16_f32 v55, v62, v63\n"
    "v_mfma_f32_16x16x32_bf16 v[144:147], v[196:199], a[92:95], v[144:147]\n"
    "ds_read_b128 a[72:75], v19 offset:1088\n"
    "v_add_u32 v10, s58, v7\n"
    "v_mfma_f32_16x16x32_bf16 v[144:147], v[200:203], a[96:99], v[144:147]\n"
    "s_waitcnt lgkmcnt(1)\n"
    "buffer_load_dwordx4 v10, s[16:19], 0 offen lds\n"
    "v_mfma_f32_32x32x16_bf16 v[64:79], a[224:227], a[228:231], 0\n"
    "ds_write_b128 v15, v[48:51] offset:50176\n"
    "v_mfma_f32_16x16x32_bf16 v[148:151], v[176:179], a[76:79], 0\n"
    "v_mfma_f32_16x16x32_bf16 v[148:151], v[180:183], a[80:83], v[148:151]\n"
    "v_mfma_f32_16x16x32_bf16 v[148:151], v[184:187], a[84:87], v[148:151]\n"
    "ds_read_b64_tr_b16 a[220:221], v12 offset:3200\n"
    "ds_read_b64_tr_b16 a[222:223], v12 offset:3456\n"
    "v_mfma_f32_16x16x32_bf16 v[148:151], v[188:191], a[88:91], v[148:151]\n"
    "s_add_u32 m0, s48, 2048\n"
    "ds_read_b128 a[76:79], v19 offset:1632\n"
    "v_add_u32 v10, s58, v8\n"
    "v_mfma_f32_16x16x32_bf16 v[148:151], v[192:195], a[92:95], v[148:151]\n"
    "ds_read_b128 a[12:15], v18 offset:1792\n"
    "v_mfma_f32_16x16x32_bf16 v[148:151], v[196:199], a[96:99], v[148:151]\n"
    "ds_read_b128 a[16:19], v18 offset:2336\n"
    "v_mfma_f32_16x16x32_bf16 v[148:151], v[200:203], a[100:103], v[148:151]\n"
    "ds_read_b128 a[20:23], v18 offset:2880\n"
    "v_mfma_f32_16x16x32_bf16 v[152:155], v[176:179], a[80:83], 0\n"
    "ds_read_b128 a[24:27], v18 offset:3424\n"
    "v_mfma_f32_16x16x32_bf16 v[152:155], v[180:183], a[84:87], v[152:155]\n"
    "s_waitcnt lgkmcnt(5)\n"
    "v_mfma_f32_32x32x16_bf16 v[64:79], a[220:223], a[232:235], v[64:79]\n"
    "ds_write_b128 v15, v[52:55] offset:50720\n"
    "v_mfma_f32_16x16x32_bf16 v[152:155], v[184:187], a[88:91], v[152:155]\n"
    "s_mov_b32 exec_hi, 0xffff\n"
    "buffer_load_dwordx4 v10, s[16:19], 0 offen lds\n"
    "s_mov_b32 exec_hi, -1\n"
    "v_mfma_f32_16x16x32_bf16 v[152:155], v[188:191], a[92:95], v[152:155]\n"
    "s_add_u32 s58, s51, 172032\n"
    "s_add_u32 m0, s48, 2816\n"
    "v_add_u32 v10, s58, v6\n"
    "v_mfma_f32_16x16x32_bf16 v[152:155], v[192:195], a[96:99], v[152:155]\n"
    "ds_read_b64_tr_b16 a[224:225], v12 offset:5632\n"
    "ds_read_b64_tr_b16 a[226:227], v12 offset:5888\n"
    "v_mfma_f32_16x16x32_bf16 v[152:155], v[196:199], a[100:103], v[152:155]\n"
    "v_cvt_pk_bf16_f32 v64, v64, v65\n"
    "v_cvt_pk_bf16_f32 v65, v66, v67\n"
    "v_mfma_f32_16x16x32_bf16 v[152:155], v[200:203], a[104:107], v[152:155]\n"
    "v_cvt_pk_bf16_f32 v66, v68, v69\n"
    "v_cvt_pk_bf16_f32 v67, v70, v71\n"
    "v_mfma_f32_16x16x32_bf16 v[156:159], v[176:179], a[84:87], 0\n"
    "v_cvt_pk_bf16_f32 v68, v72, v73\n"
    "v_cvt_pk_bf16_f32 v69, v74, v75\n"
    "v_mfma_f32_16x16x32_bf16 v[156:159], v[180:183], a[88:91], v[156:159]\n"
    "v_cvt_pk_bf16_f32 v70, v76, v77\n"
    "v_cvt_pk_bf16_f32 v71, v78, v79\n"
    "v_mfma_f32_16x16x32_bf16 v[156:159], v[184:187], a[92:95], v[156:159]\n"
    "ds_read_b128 a[80:83], v19 offset:2176\n"
    "buffer_load_dwordx4 v10, s[16:19], 0 offen lds\n"
    "v_mfma_f32_16x16x32_bf16 v[156:159], v[188:191], a[96:99], v[156:159]\n"
    "s_waitcnt lgkmcnt(1)\n"
    "s_add_u32 m0, s48, 3840\n"
    "v_add_u32 v10, s58, v7\n"
    "v_mfma_f32_32x32x16_bf16 v[48:63], a[224:227], a[228:231], 0\n"
    "ds_write_b128 v15, v[64:67] offset:51264\n"
    "buffer_load_dwordx4 v10, s[16:19], 0 offen lds\n"
    "v_mfma_f32_16x16x32_bf16 v[156:159], v[192:195], a[100:103], v[156:159]\n"
    "v_mfma_f32_16x16x32_bf16 v[156:159], v[196:199], a[104:107], v[156:159]\n"
    "buffer_load_dword v22, v17, s[28:31], s74 offen sc1\n"
    "v_mfma_f32_16x16x32_bf16 v[156:159], v[200:203], a[108:111], v[156:159]\n"
    "ds_read_b64_tr_b16 a[220:221], v12 offset:6016\n"
    "ds_read_b64_tr_b16 a[222:223], v12 offset:6272\n"
    "v_mfma_f32_16x16x32_bf16 v[160:163], v[176:179], a[88:91], 0\n"
    "ds_read_b128 a[84:87], v19 offset:2720\n"
    "v_mfma_f32_16x16x32_bf16 v[160:163], v[180:183], a[92:95], v[160:163]\n"
    "ds_read_b128 a[28:31], v18 offset:3968\n"
    "v_mfma_f32_16x16x32_bf16 v[160:163], v[184:187], a[96:99], v[160:163]\n"
    "s_add_u32 m0, s48, 4864\n"
    "ds_read_b128 a[88:91], v19 offset:3264\n"
    "v_add_u32 v10, s58, v8\n"
    "v_mfma_f32_16x16x32_bf16 v[160:163], v[188:191], a[100:103], v[160:163]\n"
    "ds_read_b128 a[32:35], v18 offset:4512\n"
    "v_mfma_f32_16x16x32_bf16 v[160:163], v[192:195], a[104:107], v[160:163]\n"
    "ds_read_b128 a[36:39], v18 offset:5056\n"
    "v_mfma_f32_16x16x32_bf16 v[160:163], v[196:199], a[108:111], v[160:163]\n"
    "s_waitcnt lgkmcnt(5)\n"
    "v_mfma_f32_32x32x16_bf16 v[48:63], a[220:223], a[232:235], v[48:63]\n"
    "ds_write_b128 v15, v[68:71] offset:51808\n"
    "v_mfma_f32_16x16x32_bf16 v[160:163], v[200:203], a[112:115], v[160:163]\n"
    "s_mov_b32 exec_hi, 0xffff\n"
    "buffer_load_dwordx4 v10, s[16:19], 0 offen lds\n"
    "s_mov_b32 exec_hi, -1\n"
    "v_mfma_f32_16x16x32_bf16 v[164:167], v[176:179], a[92:95], 0\n"
    "v_mfma_f32_16x16x32_bf16 v[164:167], v[180:183], a[96:99], v[164:167]\n"
    "s_add_u32 s58, s51, 344064\n"
    "ds_read_b64_tr_b16 a[224:225], v31\n"
    "ds_read_b64_tr_b16 a[226:227], v32\n"
    "v_mfma_f32_16x16x32_bf16 v[164:167], v[184:187], a[100:103], v[164:167]\n"
    "s_add_u32 m0, s48, 5632\n"
    "v_cvt_pk_bf16_f32 v48, v48, v49\n"
    "v_cvt_pk_bf16_f32 v49, v50, v51\n"
    "v_mfma_f32_16x16x32_bf16 v[164:167], v[188:191], a[104:107], v[164:167]\n"
    "v_cvt_pk_bf16_f32 v50, v52, v53\n"
    "v_cvt_pk_bf16_f32 v51, v54, v55\n"
    "v_mfma_f32_16x16x32_bf16 v[164:167], v[192:195], a[108:111], v[164:167]\n"
    "v_cvt_pk_bf16_f32 v52, v56, v57\n"
    "v_cvt_pk_bf16_f32 v53, v58, v59\n"
    "v_mfma_f32_16x16x32_bf16 v[164:167], v[196:199], a[112:115], v[164:167]\n"
    "v_cvt_pk_bf16_f32 v54, v60, v61\n"
    "v_cvt_pk_bf16_f32 v55, v62, v63\n"
    "v_mfma_f32_16x16x32_bf16 v[164:167], v[200:203], a[116:119], v[164:167]\n"
    "ds_read_b128 a[92:95], v19 offset:3808\n"
    "buffer_load_dwordx4 v6, s[16:19], s58 offen lds\n"
    "v_mfma_f32_16x16x32_bf16 v[168:171], v[176:179], a[96:99], 0\n"
    "s_waitcnt lgkmcnt(1)\n"
    "v_mfma_f32_32x32x16_bf16 v[80:95], a[224:227], a[240:243], v[80:95]\n"
    "s_add_u32 m0, s48, 6656\n"
    "ds_write_b128 v15, v[48:51] offset:52352\n"
    "v_mfma_f32_16x16x32_bf16 v[168:171], v[180:183], a[100:103], v[168:171]\n"
    "buffer_load_dwordx4 v7, s[16:19], s58 offen lds\n"
    "v_mfma_f32_16x16x32_bf16 v[168:171], v[184:187], a[104:107], v[168:171]\n"
    "v_mfma_f32_16x16x32_bf16 v[168:171], v[188:191], a[108:111], v[168:171]\n"
    "ds_read_b64_tr_b16 a[220:221], v12 offset:8448\n"
    "ds_read_b64_tr_b16 a[222:223], v12 offset:8704\n"
    "v_mfma_f32_16x16x32_bf16 v[168:171], v[192:195], a[112:115], v[168:171]\n"
    "v_cvt_pk_bf16_f32 v80, v80, v81\n"
    "v_cvt_pk_bf16_f32 v81, v82, v83\n"
    "v_mfma_f32_16x16x32_bf16 v[168:171], v[196:199], a[116:119], v[168:171]\n"
    "v_cvt_pk_bf16_f32 v82, v84, v85\n"
    "v_cvt_pk_bf16_f32 v83, v86, v87\n"
    "v_mfma_f32_16x16x32_bf16 v[168:171], v[200:203], a[120:123], v[168:171]\n"
    "v_cvt_pk_bf16_f32 v84, v88, v89\n"
    "v_cvt_pk_bf16_f32 v85, v90, v91\n"
    "v_mfma_f32_16x16x32_bf16 v[172:175], v[176:179], a[100:103], 0\n"
    "s_add_u32 m0, s48, 7680\n"
    "v_cvt_pk_bf16_f32 v86, v92, v93\n"
    "v_cvt_pk_bf16_f32 v87, v94, v95\n"
    "v_mfma_f32_16x16x32_bf16 v[172:175], v[180:183], a[104:107], v[172:175]\n"
    "ds_read_b128 a[96:99], v19 offset:4352\n"
    "v_mfma_f32_16x16x32_bf16 v[172:175], v[184:187], a[108:111], v[172:175]\n"
    "s_waitcnt lgkmcnt(1)\n"
    "v_mfma_f32_32x32x16_bf16 v[64:79], a[220:223], a[228:231], 0\n"
    "ds_write_b128 v15, v[52:55] offset:52896\n"
    "v_mfma_f32_16x16x32_bf16 v[172:175], v[188:191], a[112:115], v[172:175]\n"
    "s_mov_b32 exec_hi, 0xffff\n"
    "buffer_load_dwordx4 v8, s[16:19], s58 offen lds\n"
    "s_mov_b32 exec_hi, -1\n"
    "v_mfma_f32_16x16x32_bf16 v[172:175], v[192:195], a[116:119], v[172:175]\n"
    "v_mfma_f32_16x16x32_bf16 v[172:175], v[196:199], a[120:123], v[172:175]\n"
    "ds_read_b64_tr_b16 a[224:225], v12 offset:8832\n"
    "ds_read_b64_tr_b16 a[226:227], v12 offset:9088\n"
    "v_mfma_f32_16x16x32_bf16 v[172:175], v[200:203], a[124:127], v[172:175]\n"
    "ds_read_b128 a[100:103], v19 offset:4896\n"
    "v_mfma_f32_16x16x32_bf16 v[176:179], v[216:219], a[0:3], 0\n"
    "s_waitcnt vmcnt(4)\n"
    "v_cmp_eq_u32 vcc, 0, v22\n"
    "s_cbranch_vccnz 251f\n"
    "261:\n"
    "v_mfma_f32_16x16x32_bf16 v[176:179], v[220:223], a[4:7], v[176:179]\n"
    "buffer_load_dwordx4 a[128:131], v5, s[28:31], s50 offen offset:0 sc1\n"
    "ds_read_b128 a[104:107], v19 offset:5440\n"
    "v_mfma_f32_16x16x32_bf16 v[176:179], v[224:227], a[8:11], v[176:179]\n"
    "ds_read_b128 a[40:43], v18 offset:5600\n"
    "v_mfma_f32_16x16x32_bf16 v[176:179], v[228:231], a[12:15], v[176:179]\n"
    "ds_read_b128 a[108:111], v19 offset:5984\n"
    "v_mfma_f32_16x16x32_bf16 v[176:179], v[232:235], a[16:19], v[176:179]\n"
    "s_waitcnt lgkmcnt(4)\n"
    "buffer_load_dwordx4 a[132:135], v5, s[28:31], s50 offen offset:1024 sc1\n"
    "v_mfma_f32_32x32x16_bf16 v[64:79], a[224:227], a[232:235], v[64:79]\n"
    "s_add_u32 s58, s51, 516096\n"
    "ds_write_b128 v16, v[80:83] offset:50176\n"
    "s_add_u32 m0, s48, 8448\n"
    "v_mfma_f32_16x16x32_bf16 v[176:179], v[236:239], a[20:23], v[176:179]\n"
    "buffer_load_dwordx4 v6, s[16:19], s58 offen lds\n"
    "v_mfma_f32_16x16x32_bf16 v[176:179], v[240:243], a[24:27], v[176:179]\n"
    "v_mfma_f32_16x16x32_bf16 v[180:183], v[216:219], a[4:7], 0\n"
    "ds_read_b64_tr_b16 a[220:221], v12 offset:11264\n"
    "ds_read_b64_tr_b16 a[222:223], v12 offset:11520\n"
    "v_mfma_f32_16x16x32_bf16 v[180:183], v[220:223], a[8:11], v[180:183]\n"
    "v_cvt_pk_bf16_f32 v64, v64, v65\n"
    "v_cvt_pk_bf16_f32 v65, v66, v67\n"
    "v_mfma_f32_16x16x32_bf16 v[180:183], v[224:227], a[12:15], v[180:183]\n"
    "v_cvt_pk_bf16_f32 v66, v68, v69\n"
    "v_cvt_pk_bf16_f32 v67, v70, v71\n"
    "v_mfma_f32_16x16x32_bf16 v[180:183], v[228:231], a[16:19], v[180:183]\n"
    "v_cvt_pk_bf16_f32 v68, v72, v73\n"
    "v_cvt_pk_bf16_f32 v69, v74, v75\n"
    "v_mfma_f32_16x16x32_bf16 v[180:183], v[232:235], a[20:23], v[180:183]\n"
    "s_add_u32 m0, s48, 9472\n"
    "v_cvt_pk_bf16_f32 v70, v76, v77\n"
    "v_cvt_pk_bf16_f32 v71, v78, v79\n"
    "v_mfma_f32_16x16x32_bf16 v[180:183], v[236:239], a[24:27], v[180:183]\n"
    "buffer_load_dwordx4 a[136:139], v5, s[28:31], s50 offen offset:2048 sc1\n"
    "ds_read_b128 a[44:47], v18 offset:6144\n"
    "v_mfma_f32_16x16x32_bf16 v[180:183], v[240:243], a[28:31], v[180:183]\n"
    "s_waitcnt lgkmcnt(1)\n"
    "buffer_load_dwordx4 v7, s[16:19], s58 offen lds\n"
    "v_mfma_f32_32x32x16_bf16 v[48:63], a[220:223], a[228:231], 0\n"
    "ds_write_b128 v15, v[64:67] offset:53440\n"
    "buffer_load_dwordx4 a[140:143], v5, s[28:31], s50 offen offset:3072 sc1\n"
    "v_mfma_f32_16x16x32_bf16 v[184:187], v[216:219], a[8:11], 0\n"
    "s_add_u32 s50, s50, 4096\n"
    "v_mfma_f32_16x16x32_bf16 v[184:187], v[220:223], a[12:15], v[184:187]\n"
    "v_mfma_f32_16x16x32_bf16 v[184:187], v[224:227], a[16:19], v[184:187]\n"
    "ds_read_b64_tr_b16 a[224:225], v12 offset:11648\n"
    "ds_read_b64_tr_b16 a[226:227], v12 offset:11904\n"
    "v_mfma_f32_16x16x32_bf16 v[184:187], v[228:231], a[20:23], v[184:187]\n"
    "s_add_u32 m0, s48, 10496\n"
    "buffer_load_dwordx4 a[144:147], v5, s[28:31], s50 offen offset:0 sc1\n"
    "ds_read_b128 a[48:51], v18 offset:6688\n"
    "v_mfma_f32_16x16x32_bf16 v[184:187], v[232:235], a[24:27], v[184:187]\n"
    "ds_read_b128 a[52:55], v18 offset:7232\n"
    "v_mfma_f32_16x16x32_bf16 v[184:187], v[236:239], a[28:31], v[184:187]\n"
    "ds_read_b128 a[56:59], v18 offset:7776\n"
    "v_mfma_f32_16x16x32_bf16 v[184:187], v[240:243], a[32:35], v[184:187]\n"
    "buffer_load_dwordx4 a[148:151], v5, s[28:31], s50 offen offset:1024 sc1\n"
    "ds_read_b128 a[60:63], v18 offset:8320\n"
    "v_mfma_f32_16x16x32_bf16 v[188:191], v[216:219], a[12:15], 0\n"
    "s_mov_b32 exec_hi, 0xffff\n"
    "buffer_load_dwordx4 v8, s[16:19], s58 offen lds\n"
    "s_mov_b32 exec_hi, -1\n"
    "v_mfma_f32_16x16x32_bf16 v[188:191], v[220:223], a[16:19], v[188:191]\n"
    "s_waitcnt lgkmcnt(4)\n"
    "v_mfma_f32_32x32x16_bf16 v[48:63], a[224:227], a[232:235], v[48:63]\n"
    "ds_write_b128 v15, v[68:71] offset:53984\n"
    "v_mfma_f32_16x16x32_bf16 v[188:191], v[224:227], a[20:23], v[188:191]\n"
    "s_add_u32 s58, s51, 688128\n"
    "s_add_u32 m0, s48, 11264\n"
    "v_mfma_f32_16x16x32_bf16 v[188:191], v[228:231], a[24:27], v[188:191]\n"
    "buffer_load_dwordx4 a[152:155], v5, s[28:31], s50 offen offset:2048 sc1\n"
    "v_mfma_f32_16x16x32_bf16 v[188:191], v[232:235], a[28:31], v[188:191]\n"
    "ds_read_b64_tr_b16 a[220:221], v37 offset:14080\n"
    "ds_read_b64_tr_b16 a[222:223], v37 offset:14336\n"
    "v_mfma_f32_16x16x32_bf16 v[188:191], v[236:239], a[32:35], v[188:191]\n"
    "v_cvt_pk_bf16_f32 v48, v48, v49\n"
    "v_cvt_pk_bf16_f32 v49, v50, v51\n"
    "v_mfma_f32_16x16x32_bf16 v[188:191], v[240:243], a[36:39], v[188:191]\n"
    "v_cvt_pk_bf16_f32 v50, v52, v53\n"
    "v_cvt_pk_bf16_f32 v51, v54, v55\n"
    "v_mfma_f32_16x16x32_bf16 v[192:195], v[216:219], a[16:19], 0\n"
    "v_cvt_pk_bf16_f32 v52, v56, v57\n"
    "v_cvt_pk_bf16_f32 v53, v58, v59\n"
    "v_mfma_f32_16x16x32_bf16 v[192:195], v[220:223], a[20:23], v[192:195]\n"
    "v_cvt_pk_bf16_f32 v54, v60, v61\n"
    "v_cvt_pk_bf16_f32 v55, v62, v63\n"
    "v_mfma_f32_16x16x32_bf16 v[192:195], v[224:227], a[24:27], v[192:195]\n"
    "buffer_load_dwordx4 v6, s[16:19], s58 offen lds\n"
    "v_mfma_f32_16x16x32_bf16 v[192:195], v[228:231], a[28:31], v[192:195]\n"
    "s_waitcnt lgkmcnt(0)\n"
    "s_add_u32 m0, s48, 12288\n"
    "v_mfma_f32_32x32x16_bf16 v[64:79], a[220:223], a[228:231], 0\n"
    "ds_write_b128 v15, v[48:51] offset:54528\n"
    "buffer_load_dwordx4 v7, s[16:19], s58 offen lds\n"
    "v_mfma_f32_16x16x32_bf16 v[192:195], v[232:235], a[32:35], v[192:195]\n"
    "v_mfma_f32_16x16x32_bf16 v[192:195], v[236:239], a[36:39], v[192:195]\n"
    "v_mfma_f32_16x16x32_bf16 v[192:195], v[240:243], a[40:43], v[192:195]\n"
    "ds_read_b64_tr_b16 a[224:225], v37 offset:14464\n"
    "ds_read_b64_tr_b16 a[226:227], v37 offset:14720\n"
    "v_mfma_f32_16x16x32_bf16 v[196:199], v[216:219], a[20:23], 0\n"
    "v_xor_b32 v12, 32, v12\n"
    "v_xor_b32 v13, 32, v13\n"
    "v_mfma_f32_16x16x32_bf16 v[196:199], v[220:223], a[24:27], v[196:199]\n"
    "v_xor_b32 v14, 32, v14\n"
    "v_xor_b32 v31, 32, v31\n"
    "v_mfma_f32_16x16x32_bf16 v[196:199], v[224:227], a[28:31], v[196:199]\n"
    "s_add_u32 m0, s48, 13312\n"
    "v_xor_b32 v32, 32, v32\n"
    "v_xor_b32 v37, 32, v37\n"
    "v_mfma_f32_16x16x32_bf16 v[196:199], v[228:231], a[32:35], v[196:199]\n"
    "s_mov_b32 exec_hi, 0xffff\n"
    "buffer_load_dwordx4 v8, s[16:19], s58 offen lds\n"
    "s_mov_b32 exec_hi, -1\n"
    "v_mfma_f32_16x16x32_bf16 v[196:199], v[232:235], a[36:39], v[196:199]\n"
    "v_mfma_f32_16x16x32_bf16 v[196:199], v[236:239], a[40:43], v[196:199]\n"
    "s_waitcnt lgkmcnt(0)\n"
    "v_mfma_f32_32x32x16_bf16 v[64:79], a[224:227], a[232:235], v[64:79]\n"
    "ds_write_b128 v15, v[52:55] offset:55072\n"
    "v_mfma_f32_16x16x32_bf16 v[196:199], v[240:243], a[44:47], v[196:199]\n"
    "v_mfma_f32_16x16x32_bf16 v[200:203], v[216:219], a[24:27], 0\n"
    "v_mfma_f32_16x16x32_bf16 v[200:203], v[220:223], a[28:31], v[200:203]\n"
    "s_mov_b32 m0, s54\n"
    "v_cvt_pk_bf16_f32 v64, v64, v65\n"
    "v_cvt_pk_bf16_f32 v65, v66, v67\n"
    "v_mfma_f32_16x16x32_bf16 v[200:203], v[224:227], a[32:35], v[200:203]\n"
    "v_cvt_pk_bf16_f32 v66, v68, v69\n"
    "v_cvt_pk_bf16_f32 v67, v70, v71\n"
    "v_mfma_f32_16x16x32_bf16 v[200:203], v[228:231], a[36:39], v[200:203]\n"
    "v_cvt_pk_bf16_f32 v68, v72, v73\n"
    "v_cvt_pk_bf16_f32 v69, v74, v75\n"
    "v_mfma_f32_16x16x32_bf16 v[200:203], v[232:235], a[40:43], v[200:203]\n"
    "v_cvt_pk_bf16_f32 v70, v76, v77\n"
    "v_cvt_pk_bf16_f32 v71, v78, v79\n"
    "v_mfma_f32_16x16x32_bf16 v[200:203], v[236:239], a[44:47], v[200:203]\n"
    "ds_write_b128 v16, v[84:87] offset:50720\n"
    "v_mfma_f32_16x16x32_bf16 v[200:203], v[240:243], a[48:51], v[200:203]\n"
    "buffer_load_dwordx4 v6, s[16:19], s57 offen lds\n"
    "v_mfma_f32_16x16x32_bf16 v[204:207], v[216:219], a[28:31], 0\n"
    "v_mfma_f32_16x16x32_bf16 v[204:207], v[220:223], a[32:35], v[204:207]\n"
    "s_mov_b32 m0, s55\n"
    "ds_write_b128 v33, v[64:67] offset:55616\n"
    "v_mfma_f32_16x16x32_bf16 v[204:207], v[224:227], a[36:39], v[204:207]\n"
    "buffer_load_dwordx4 v7, s[16:19], s57 offen lds\n"
    "v_mfma_f32_16x16x32_bf16 v[204:207], v[228:231], a[40:43], v[204:207]\n"
    "v_mfma_f32_16x16x32_bf16 v[204:207], v[232:235], a[44:47], v[204:207]\n"
    "ds_write_b128 v33, v[68:71] offset:56160\n"
    "v_mfma_f32_16x16x32_bf16 v[204:207], v[236:239], a[48:51], v[204:207]\n"
    "buffer_load_dwordx4 a[156:159], v5, s[28:31], s50 offen offset:3072 sc1\n"
    "s_add_u32 s50, s50, 4096\n"
    "v_mfma_f32_16x16x32_bf16 v[204:207], v[240:243], a[52:55], v[204:207]\n"
    "v_mfma_f32_16x16x32_bf16 v[208:211], v[216:219], a[32:35], 0\n"
    "v_mfma_f32_16x16x32_bf16 v[208:211], v[220:223], a[36:39], v[208:211]\n"
    "s_mov_b32 m0, s56\n"
    "v_mfma_f32_16x16x32_bf16 v[208:211], v[224:227], a[40:43], v[208:211]\n"
    "s_mov_b32 exec_hi, 0xffff\n"
    "buffer_load_dwordx4 v8, s[16:19], s57 offen lds\n"
    "s_mov_b32 exec_hi, -1\n"
    "v_mfma_f32_16x16x32_bf16 v[208:211], v[228:231], a[44:47], v[208:211]\n"
    "s_add_u32 s51, s51, 64\n"
    "s_add_u32 s57, s57, s59\n"
    "v_mfma_f32_16x16x32_bf16 v[208:211], v[232:235], a[48:51], v[208:211]\n"
    "s_waitcnt lgkmcnt(0)\n"
    "s_barrier\n"
    "v_mfma_f32_16x16x32_bf16 v[208:211], v[236:239], a[52:55], v[208:211]\n"
    "ds_read_b64_tr_b16 a[220:221], v12 offset:0\n"
    "ds_read_b64_tr_b16 a[222:223], v12 offset:256\n"
    "v_mfma_f32_16x16x32_bf16 v[208:211], v[240:243], a[56:59], v[208:211]\n"
    "buffer_load_dwordx4 a[160:163], v5, s[28:31], s50 offen offset:0 sc1\n"
    "ds_read_b128 a[0:3], v18 offset:50176\n"
    "v_mfma_f32_16x16x32_bf16 v[212:215], v[216:219], a[36:39], 0\n"
    "s_mov_b64 exec, 1\n"
    "buffer_store_dword v39, v35, s[28:31], s65 offen offset:1024 sc0 sc1\n"
    "s_mov_b64 exec, -1\n"
    "v_mfma_f32_16x16x32_bf16 v[212:215], v[220:223], a[40:43], v[212:215]\n"
    "ds_read_b128 a[4:7], v18 offset:50720\n"
    "v_mfma_f32_16x16x32_bf16 v[212:215], v[224:227], a[44:47], v[212:215]\n"
    "ds_read_b128 a[8:11], v18 offset:51264\n"
    "v_mfma_f32_16x16x32_bf16 v[212:215], v[228:231], a[48:51], v[212:215]\n"
    "ds_read_b128 a[12:15], v18 offset:51808\n"
    "v_mfma_f32_16x16x32_bf16 v[212:215], v[232:235], a[52:55], v[212:215]\n"
    "ds_read_b64_tr_b16 a[224:225], v12 offset:384\n"
    "ds_read_b64_tr_b16 a[226:227], v12 offset:640\n"
    "v_mfma_f32_16x16x32_bf16 v[212:215], v[236:239], a[56:59], v[212:215]\n"
    "s_waitcnt lgkmcnt(6)\n"
    "ds_read_b128 a[16:19], v18 offset:52352\n"
    "v_mfma_f32_32x32x16_bf16 v[48:63], a[220:223], a[228:231], 0\n"
    "ds_read_b128 a[20:23], v18 offset:52896\n"
    "v_mfma_f32_16x16x32_bf16 v[212:215], v[240:243], a[60:63], v[212:215]\n"
    "ds_read_b128 a[24:27], v18 offset:53440\n"
    "v_mfma_f32_16x16x32_bf16 v[216:219], a[212:215], a[64:67], 0\n"
    "buffer_load_dwordx4 a[164:167], v5, s[28:31], s50 offen offset:1024 sc1\n"
    "ds_read_b128 a[28:31], v18 offset:53984\n"
    "v_mfma_f32_16x16x32_bf16 v[216:219], a[216:219], a[72:75], v[216:219]\n"
    "ds_read_b128 a[32:35], v18 offset:54528\n"
    "v_mfma_f32_16x16x32_bf16 v[220:223], a[212:215], a[68:71], 0\n"
    "ds_read_b128 a[36:39], v18 offset:55072\n"
    "v_mfma_f32_16x16x32_bf16 v[220:223], a[216:219], a[76:79], v[220:223]\n"
    "s_waitcnt vmcnt(20) lgkmcnt(10)\n"
    "ds_read_b64_tr_b16 a[220:221], v13\n"
    "ds_read_b64_tr_b16 a[222:223], v14\n"
    "v_mfma_f32_16x16x32_bf16 v[224:227], a[212:215], a[72:75], 0\n"
    "s_waitcnt lgkmcnt(8)\n"
    "ds_read_b128 a[40:43], v18 offset:55616\n"
    "v_mfma_f32_32x32x16_bf16 v[48:63], a[224:227], a[232:235], v[48:63]\n"
    "ds_read_b128 a[44:47], v18 offset:56160\n"
    "v_mfma_f32_16x16x32_bf16 v[224:227], a[216:219], a[80:83], v[224:227]\n"
    "ds_read_b128 a[48:51], v18 offset:56704\n"
    "v_mfma_f32_16x16x32_bf16 v[228:231], a[212:215], a[76:79], 0\n"
    "ds_read_b128 a[52:55], v18 offset:57248\n"
    "v_mfma_f32_16x16x32_bf16 v[228:231], a[216:219], a[84:87], v[228:231]\n"
    "v_cvt_pk_bf16_f32 v48, v48, v49\n"
    "v_cvt_pk_bf16_f32 v49, v50, v51\n"
    "v_mfma_f32_16x16x32_bf16 v[232:235], a[212:215], a[80:83], 0\n"
    "v_cvt_pk_bf16_f32 v50, v52, v53\n"
    "v_cvt_pk_bf16_f32 v51, v54, v55\n"
    "v_mfma_f32_16x16x32_bf16 v[232:235], a[216:219], a[88:91], v[232:235]\n"
    "v_cvt_pk_bf16_f32 v52, v56, v57\n"
    "v_cvt_pk_bf16_f32 v53, v58, v59\n"
    "v_mfma_f32_16x16x32_bf16 v[236:239], a[212:215], a[84:87], 0\n"
    "s_waitcnt lgkmcnt(4)\n"
    "v_cvt_pk_bf16_f32 v54, v60, v61\n"
    "v_cvt_pk_bf16_f32 v55, v62, v63\n"
    "v_mfma_f32_32x32x16_bf16 v[80:95], a[220:223], a[236:239], 0\n"
    "ds_read_b64_tr_b16 a[224:225], v12 offset:2816\n"
    "ds_read_b64_tr_b16 a[226:227], v12 offset:3072\n"
    "buffer_load_dwordx4 a[168:171], v5, s[28:31], s50 offen offset:2048 sc1\n"
    "v_mfma_f32_16x16x32_bf16 v[236:239], a[216:219], a[92:95], v[236:239]\n"
    "ds_read_b128 a[56:59], v18 offset:57792\n"
    "v_mfma_f32_16x16x32_bf16 v[240:243], a[212:215], a[88:91], 0\n"
    "ds_read_b128 a[60:63], v18 offset:58336\n"
    "v_mfma_f32_16x16x32_bf16 v[240:243], a[216:219], a[96:99], v[240:243]\n"
    "v_mfma_f32_16x16x32_bf16 v[244:247], a[212:215], a[92:95], 0\n"
    "v_mfma_f32_16x16x32_bf16 v[244:247], a[216:219], a[100:103], v[244:247]\n"
    "v_mfma_f32_16x16x32_bf16 v[248:251], a[212:215], a[96:99], 0\n"
    "s_waitcnt lgkmcnt(2)\n"
    "v_mfma_f32_32x32x16_bf16 v[64:79], a[224:227], a[228:231], 0\n"
    "ds_write_b128 v15, v[48:51] offset:0\n"
    "v_mfma_f32_16x16x32_bf16 v[248:251], a[216:219], a[104:107], v[248:251]\n"
    "v_mfma_f32_16x16x32_bf16 v[252:255], a[212:215], a[100:103], 0\n"
    "v_mfma_f32_16x16x32_bf16 v[252:255], a[216:219], a[108:111], v[252:255]\n"
    "s_waitcnt vmcnt(11)\n"
    ".p2align 6\n"
    "21:\n"
    "v_mfma_f32_16x16x32_bf16 v[96:99], a[128:131], a[0:3], v[96:99]\n"
    "ds_read_b64_tr_b16 a[220:221], v12 offset:3200\n"
    "ds_read_b64_tr_b16 a[222:223], v12 offset:3456\n"
    "v_mfma_f32_16x16x32_bf16 v[96:99], a[132:135], a[4:7], v[96:99]\n"
    "buffer_load_dwordx4 a[172:175], v5, s[28:31], s50 offen offset:3072 sc1\n"
    "s_add_u32 s50, s50, 4096\n"
    "v_mfma_f32_16x16x32_bf16 v[96:99], a[136:139], a[8:11], v[96:99]\n"
    "ds_read_b128 a[64:67], v18 offset:50256\n"
    "v_mfma_f32_16x16x32_bf16 v[96:99], a[140:143], a[12:15], v[96:99]\n"
    "buffer_load_dwordx4 a[176:179], v5, s[28:31], s50 offen offset:0 sc1\n"
    "v_mfma_f32_16x16x32_bf16 v[96:99], a[144:147], a[16:19], v[96:99]\n"
    "ds_read_b128 a[68:71], v18 offset:50800\n"
    "v_mfma_f32_16x16x32_bf16 v[96:99], a[148:151], a[20:23], v[96:99]\n"
    "buffer_load_dwordx4 a[180:183], v5, s[28:31], s50 offen offset:1024 sc1\n"
    "v_mfma_f32_16x16x32_bf16 v[96:99], a[152:155], a[24:27], v[96:99]\n"
    "s_waitcnt lgkmcnt(2)\n"
    "v_mfma_f32_32x32x16_bf16 v[64:79], a[220:223], a[232:235], v[64:79]\n"
    "ds_write_b128 v15, v[52:55] offset:544\n"
    "v_mfma_f32_16x16x32_bf16 v[100:103], a[128:131], a[4:7], v[100:103]\n"
    "v_mfma_f32_16x16x32_bf16 v[100:103], a[132:135], a[8:11], v[100:103]\n"
    "v_mfma_f32_16x16x32_bf16 v[100:103], a[136:139], a[12:15], v[100:103]\n"
    "v_cvt_pk_bf16_f32 v64, v64, v65\n"
    "v_cvt_pk_bf16_f32 v65, v66, v67\n"
    "v_mfma_f32_16x16x32_bf16 v[100:103], a[140:143], a[16:19], v[100:103]\n"
    "v_cvt_pk_bf16_f32 v66, v68, v69\n"
    "v_cvt_pk_bf16_f32 v67, v70, v71\n"
    "v_mfma_f32_16x16x32_bf16 v[100:103], a[144:147], a[20:23], v[100:103]\n"
    "ds_read_b64_tr_b16 a[224:225], v12 offset:5632\n"
    "ds_read_b64_tr_b16 a[226:227], v12 offset:5888\n"
    "v_mfma_f32_16x16x32_bf16 v[100:103], a[148:151], a[24:27], v[100:103]\n"
    "v_cvt_pk_bf16_f32 v68, v72, v73\n"
    "v_cvt_pk_bf16_f32 v69, v74, v75\n"
    "v_mfma_f32_16x16x32_bf16 v[100:103], a[152:155], a[28:31], v[100:103]\n"
    "v_cvt_pk_bf16_f32 v70, v76, v77\n"
    "v_cvt_pk_bf16_f32 v71, v78, v79\n"
    "v_mfma_f32_16x16x32_bf16 v[104:107], a[128:131], a[8:11], v[104:107]\n"
    "ds_read_b128 a[72:75], v18 offset:51344\n"
    "v_mfma_f32_16x16x32_bf16 v[104:107], a[132:135], a[12:15], v[104:107]\n"
    "ds_read_b128 a[76:79], v18 offset:51888\n"
    "v_mfma_f32_16x16x32_bf16 v[104:107], a[136:139], a[16:19], v[104:107]\n"
    "ds_read_b128 a[80:83], v18 offset:52432\n"
    "v_mfma_f32_16x16x32_bf16 v[104:107], a[140:143], a[20:23], v[104:107]\n"
    "v_mfma_f32_16x16x32_bf16 v[104:107], a[144:147], a[24:27], v[104:107]\n"
    "v_mfma_f32_16x16x32_bf16 v[104:107], a[148:151], a[28:31], v[104:107]\n"
    "s_waitcnt lgkmcnt(3)\n"
    "v_mfma_f32_32x32x16_bf16 v[48:63], a[224:227], a[228:231], 0\n"
    "ds_write_b128 v15, v[64:67] offset:1088\n"
    "v_mfma_f32_16x16x32_bf16 v[104:107], a[152:155], a[32:35], v[104:107]\n"
    "v_mfma_f32_16x16x32_bf16 v[108:111], a[128:131], a[12:15], v[108:111]\n"
    "v_mfma_f32_16x16x32_bf16 v[108:111], a[132:135], a[16:19], v[108:111]\n"
    "ds_read_b128 a[84:87], v18 offset:52976\n"
    "v_mfma_f32_16x16x32_bf16 v[108:111], a[136:139], a[20:23], v[108:111]\n"
    "ds_read_b128 a[88:91], v18 offset:53520\n"
    "v_mfma_f32_16x16x32_bf16 v[108:111], a[140:143], a[24:27], v[108:111]\n"
    "ds_read_b64_tr_b16 a[220:221], v12 offset:6016\n"
    "ds_read_b64_tr_b16 a[222:223], v12 offset:6272\n"
    "v_mfma_f32_16x16x32_bf16 v[108:111], a[144:147], a[28:31], v[108:111]\n"
    "buffer_load_dwordx4 a[184:187], v5, s[28:31], s50 offen offset:2048 sc1\n"
    "v_mfma_f32_16x16x32_bf16 v[108:111], a[148:151], a[32:35], v[108:111]\n"
    "v_mfma_f32_16x16x32_bf16 v[108:111], a[152:155], a[36:39], v[108:111]\n"
    "ds_read_b128 a[92:95], v18 offset:54064\n"
    "v_mfma_f32_16x16x32_bf16 v[112:115], a[128:131], a[16:19], v[112:115]\n"
    "v_mfma_f32_16x16x32_bf16 v[112:115], a[132:135], a[20:23], v[112:115]\n"
    "v_mfma_f32_16x16x32_bf16 v[112:115], a[136:139], a[24:27], v[112:115]\n"
    "v_mfma_f32_16x16x32_bf16 v[112:115], a[140:143], a[28:31], v[112:115]\n"
    "ds_read_b128 a[96:99], v18 offset:54608\n"
    "v_mfma_f32_16x16x32_bf16 v[112:115], a[144:147], a[32:35], v[112:115]\n"
    "s_waitcnt lgkmcnt(2)\n"
    "buffer_load_dwordx4 a[188:191], v5, s[28:31], s50 offen offset:3072 sc1\n"
    "s_add_u32 s50, s50, 4096\n"
    "v_mfma_f32_32x32x16_bf16 v[48:63], a[220:223], a[232:235], v[48:63]\n"
    "ds_write_b128 v15, v[68:71] offset:1632\n"
    "v_mfma_f32_16x16x32_bf16 v[112:115], a[148:151], a[36:39], v[112:115]\n"
    "v_mfma_f32_16x16x32_bf16 v[112:115], a[152:155], a[40:43], v[112:115]\n"
    "v_mfma_f32_16x16x32_bf16 v[116:119], a[128:131], a[20:23], v[116:119]\n"
    "v_cvt_pk_bf16_f32 v48, v48, v49\n"
    "v_cvt_pk_bf16_f32 v49, v50, v51\n"
    "v_mfma_f32_16x16x32_bf16 v[116:119], a[132:135], a[24:27], v[116:119]\n"
    "v_cvt_pk_bf16_f32 v50, v52, v53\n"
    "v_cvt_pk_bf16_f32 v51, v54, v55\n"
    "v_mfma_f32_16x16x32_bf16 v[116:119], a[136:139], a[28:31], v[116:119]\n"
    "v_cvt_pk_bf16_f32 v52, v56, v57\n"
    "v_cvt_pk_bf16_f32 v53, v58, v59\n"
    "v_mfma_f32_16x16x32_bf16 v[116:119], a[140:143], a[32:35], v[116:119]\n"
    "s_waitcnt vmcnt(9) lgkmcnt(2)\n"
    "ds_read_b64_tr_b16 a[224:225], v31\n"
    "ds_read_b64_tr_b16 a[226:227], v32\n"
    "v_mfma_f32_16x16x32_bf16 v[116:119], a[144:147], a[36:39], v[116:119]\n"
    "v_cvt_pk_bf16_f32 v54, v60, v61\n"
    "v_cvt_pk_bf16_f32 v55, v62, v63\n"
    "v_mfma_f32_16x16x32_bf16 v[116:119], a[148:151], a[40:43], v[116:119]\n"
    "buffer_load_dwordx4 a[192:195], v5, s[28:31], s50 offen offset:0 sc1\n"
    "ds_read_b128 a[100:103], v18 offset:55152\n"
    "v_mfma_f32_16x16x32_bf16 v[116:119], a[152:155], a[44:47], v[116:119]\n"
    "ds_read_b128 a[104:107], v18 offset:55696\n"
    "v_mfma_f32_16x16x32_bf16 v[120:123], a[128:131], a[24:27], v[120:123]\n"
    "ds_read_b128 a[108:111], v18 offset:56240\n"
    "v_mfma_f32_16x16x32_bf16 v[120:123], a[132:135], a[28:31], v[120:123]\n"
    "v_mfma_f32_16x16x32_bf16 v[120:123], a[136:139], a[32:35], v[120:123]\n"
    "v_mfma_f32_16x16x32_bf16 v[120:123], a[140:143], a[36:39], v[120:123]\n"
    "v_mfma_f32_16x16x32_bf16 v[120:123], a[144:147], a[40:43], v[120:123]\n"
    "s_waitcnt lgkmcnt(3)\n"
    "v_mfma_f32_32x32x16_bf16 v[80:95], a[224:227], a[240:243], v[80:95]\n"
    "ds_write_b128 v15, v[48:51] offset:2176\n"
    "v_mfma_f32_16x16x32_bf16 v[120:123], a[148:151], a[44:47], v[120:123]\n"
    "v_mfma_f32_16x16x32_bf16 v[120:123], a[152:155], a[48:51], v[120:123]\n"
    "buffer_load_dwordx4 a[196:199], v5, s[28:31], s50 offen offset:1024 sc1\n"
    "v_mfma_f32_16x16x32_bf16 v[124:127], a[128:131], a[28:31], v[124:127]\n"
    "v_cvt_pk_bf16_f32 v80, v80, v81\n"
    "v_cvt_pk_bf16_f32 v81, v82, v83\n"
    "v_mfma_f32_16x16x32_bf16 v[124:127], a[132:135], a[32:35], v[124:127]\n"
    "v_cvt_pk_bf16_f32 v82, v84, v85\n"
    "v_cvt_pk_bf16_f32 v83, v86, v87\n"
    "v_mfma_f32_16x16x32_bf16 v[124:127], a[136:139], a[36:39], v[124:127]\n"
    "ds_read_b64_tr_b16 a[220:221], v12 offset:8448\n"
    "ds_read_b64_tr_b16 a[222:223], v12 offset:8704\n"
    "v_mfma_f32_16x16x32_bf16 v[124:127], a[140:143], a[40:43], v[124:127]\n"
    "v_cvt_pk_bf16_f32 v84, v88, v89\n"
    "v_cvt_pk_bf16_f32 v85, v90, v91\n"
    "v_mfma_f32_16x16x32_bf16 v[124:127], a[144:147], a[44:47], v[124:127]\n"
    "v_cvt_pk_bf16_f32 v86, v92, v93\n"
    "v_cvt_pk_bf16_f32 v87, v94, v95\n"
    "v_mfma_f32_16x16x32_bf16 v[124:127], a[148:151], a[48:51], v[124:127]\n"
    "ds_read_b128 a[112:115], v18 offset:56784\n"
    "v_mfma_f32_16x16x32_bf16 v[124:127], a[152:155], a[52:55], v[124:127]\n"
    "ds_read_b128 a[116:119], v18 offset:57328\n"
    "v_mfma_f32_16x16x32_bf16 v[128:131], a[128:131], a[32:35], v[128:131]\n"
    "buffer_load_dwordx4 a[200:203], v5, s[28:31], s50 offen offset:2048 sc1\n"
    "ds_read_b128 a[120:123], v18 offset:57872\n"
    "v_mfma_f32_16x16x32_bf16 v[128:131], a[132:135], a[36:39], v[128:131]\n"
    "ds_read_b128 a[124:127], v18 offset:58416\n"
    "v_mfma_f32_16x16x32_bf16 v[128:131], a[136:139], a[40:43], v[128:131]\n"
    "v_mfma_f32_16x16x32_bf16 v[128:131], a[140:143], a[44:47], v[128:131]\n"
    "s_waitcnt lgkmcnt(4)\n"
    "v_mfma_f32_32x32x16_bf16 v[64:79], a[220:223], a[228:231], 0\n"
    "ds_write_b128 v15, v[52:55] offset:2720\n"
    "v_mfma_f32_16x16x32_bf16 v[128:131], a[144:147], a[48:51], v[128:131]\n"
    "buffer_load_dwordx2 v[22:23], v27, s[28:31], s74 offen offset:128 sc1\n"
    "v_mfma_f32_16x16x32_bf16 v[128:131], a[148:151], a[52:55], v[128:131]\n"
    "v_mfma_f32_16x16x32_bf16 v[128:131], a[152:155], a[56:59], v[128:131]\n"
    "v_mfma_f32_16x16x32_bf16 v[132:135], a[128:131], a[36:39], v[132:135]\n"
    "v_mfma_f32_16x16x32_bf16 v[132:135], a[132:135], a[40:43], v[132:135]\n"
    "ds_read_b64_tr_b16 a[224:225], v12 offset:8832\n"
    "ds_read_b64_tr_b16 a[226:227], v12 offset:9088\n"
    "v_mfma_f32_16x16x32_bf16 v[132:135], a[136:139], a[44:47], v[132:135]\n"
    "buffer_load_dwordx4 a[204:207], v5, s[28:31], s50 offen offset:3072 sc1\n"
    "s_add_u32 s50, s50, 4096\n"
    "v_mfma_f32_16x16x32_bf16 v[132:135], a[140:143], a[48:51], v[132:135]\n"
    "v_mfma_f32_16x16x32_bf16 v[132:135], a[144:147], a[52:55], v[132:135]\n"
    "v_mfma_f32_16x16x32_bf16 v[132:135], a[148:151], a[56:59], v[132:135]\n"
    "v_mfma_f32_16x16x32_bf16 v[132:135], a[152:155], a[60:63], v[132:135]\n"
    "s_waitcnt vmcnt(7) lgkmcnt(2)\n"
    "v_mfma_f32_16x16x32_bf16 v[136:139], a[156:159], a[64:67], v[136:139]\n"
    "v_mfma_f32_16x16x32_bf16 v[136:139], a[160:163], a[68:71], v[136:139]\n"
    "ds_read_b128 a[64:67], v19 offset:50176\n"
    "v_mfma_f32_16x16x32_bf16 v[136:139], a[164:167], a[72:75], v[136:139]\n"
    "s_waitcnt lgkmcnt(1)\n"
    "buffer_load_dwordx4 a[208:211], v5, s[28:31], s50 offen offset:0 sc1\n"
    "v_mfma_f32_32x32x16_bf16 v[64:79], a[224:227], a[232:235], v[64:79]\n"
    "ds_write_b128 v16, v[80:83] offset:0\n"
    "v_mfma_f32_16x16x32_bf16 v[136:139], a[168:171], a[76:79], v[136:139]\n"
    "v_mfma_f32_16x16x32_bf16 v[136:139], a[172:175], a[80:83], v[136:139]\n"
    "v_mfma_f32_16x16x32_bf16 v[136:139], a[176:179], a[84:87], v[136:139]\n"
    "v_cvt_pk_bf16_f32 v64, v64, v65\n"
    "v_cvt_pk_bf16_f32 v65, v66, v67\n"
    "v_mfma_f32_16x16x32_bf16 v[136:139], a[180:183], a[88:91], v[136:139]\n"
    "v_cvt_pk_bf16_f32 v66, v68, v69\n"
    "v_cvt_pk_bf16_f32 v67, v70, v71\n"
    "v_mfma_f32_16x16x32_bf16 v[140:143], a[156:159], a[68:71], v[140:143]\n"
    "v_cvt_pk_bf16_f32 v68, v72, v73\n"
    "v_cvt_pk_bf16_f32 v69, v74, v75\n"
    "v_mfma_f32_16x16x32_bf16 v[140:143], a[160:163], a[72:75], v[140:143]\n"
    "ds_read_b64_tr_b16 a[220:221], v12 offset:11264\n"
    "ds_read_b64_tr_b16 a[222:223], v12 offset:11520\n"
    "v_mfma_f32_16x16x32_bf16 v[140:143], a[164:167], a[76:79], v[140:143]\n"
    "v_cvt_pk_bf16_f32 v70, v76, v77\n"
    "v_cvt_pk_bf16_f32 v71, v78, v79\n"
    "v_mfma_f32_16x16x32_bf16 v[140:143], a[168:171], a[80:83], v[140:143]\n"
    "buffer_load_dwordx4 a[212:215], v5, s[28:31], s50 offen offset:1024 sc1\n"
    "ds_read_b128 a[68:71], v19 offset:50720\n"
    "v_mfma_f32_16x16x32_bf16 v[140:143], a[172:175], a[84:87], v[140:143]\n"
    "ds_read_b128 a[0:3], v18 offset:50336\n"
    "v_mfma_f32_16x16x32_bf16 v[140:143], a[176:179], a[88:91], v[140:143]\n"
    "ds_read_b128 a[4:7], v18 offset:50880\n"
    "v_mfma_f32_16x16x32_bf16 v[140:143], a[180:183], a[92:95], v[140:143]\n"
    "ds_read_b128 a[8:11], v18 offset:51424\n"
    "v_mfma_f32_16x16x32_bf16 v[144:147], a[156:159], a[72:75], v[144:147]\n"
    "ds_read_b128 a[12:15], v18 offset:51968\n"
    "v_mfma_f32_16x16x32_bf16 v[144:147], a[160:163], a[76:79], v[144:147]\n"
    "ds_read_b128 a[72:75], v19 offset:51264\n"
    "v_mfma_f32_16x16x32_bf16 v[144:147], a[164:167], a[80:83], v[144:147]\n"
    "s_waitcnt lgkmcnt(6)\n"
    "v_mfma_f32_32x32x16_bf16 v[48:63], a[220:223], a[228:231], 0\n"
    "ds_write_b128 v15, v[64:67] offset:3264\n"
    "v_mfma_f32_16x16x32_bf16 v[144:147], a[168:171], a[84:87], v[144:147]\n"
    "v_mfma_f32_16x16x32_bf16 v[144:147], a[172:175], a[88:91], v[144:147]\n"
    "buffer_load_dwordx4 a[216:219], v5, s[28:31], s50 offen offset:2048 sc1\n"
    "s_add_u32 s50, s50, s62\n"
    "v_mfma_f32_16x16x32_bf16 v[144:147], a[176:179], a[92:95], v[144:147]\n"
    "s_movk_i32 s62, 3072\n"
    "ds_read_b128 a[16:19], v18 offset:52512\n"
    "v_mfma_f32_16x16x32_bf16 v[144:147], a[180:183], a[96:99], v[144:147]\n"
    "ds_read_b128 a[20:23], v18 offset:53056\n"
    "v_mfma_f32_16x16x32_bf16 v[148:151], a[156:159], a[76:79], v[148:151]\n"
    "ds_read_b64_tr_b16 a[224:225], v12 offset:11648\n"
    "ds_read_b64_tr_b16 a[226:227], v12 offset:11904\n"
    "v_mfma_f32_16x16x32_bf16 v[148:151], a[160:163], a[80:83], v[148:151]\n"
    "ds_read_b128 a[76:79], v19 offset:51808\n"
    "v_mfma_f32_16x16x32_bf16 v[148:151], a[164:167], a[84:87], v[148:151]\n"
    "ds_read_b128 a[24:27], v18 offset:53600\n"
    "v_mfma_f32_16x16x32_bf16 v[148:151], a[168:171], a[88:91], v[148:151]\n"
    "v_mfma_f32_16x16x32_bf16 v[148:151], a[172:175], a[92:95], v[148:151]\n"
    "v_mfma_f32_16x16x32_bf16 v[148:151], a[176:179], a[96:99], v[148:151]\n"
    "s_waitcnt vmcnt(4) lgkmcnt(6)\n"
    "v_min_u32 v22, v22, v23\n"
    "v_mfma_f32_16x16x32_bf16 v[148:151], a[180:183], a[100:103], v[148:151]\n"
    "v_cmp_eq_u32 vcc, 0, v22\n"
    "s_cbranch_vccnz 252f\n"
    "262:\n"
    "v_mfma_f32_16x16x32_bf16 v[152:155], a[156:159], a[80:83], v[152:155]\n"
    "ds_read_b128 a[28:31], v18 offset:54144\n"
    "v_mfma_f32_16x16x32_bf16 v[152:155], a[160:163], a[84:87], v[152:155]\n"
    "s_waitcnt lgkmcnt(3)\n"
    "v_mfma_f32_32x32x16_bf16 v[48:63], a[224:227], a[232:235], v[48:63]\n"
    "ds_write_b128 v15, v[68:71] offset:3808\n"
    "v_mfma_f32_16x16x32_bf16 v[152:155], a[164:167], a[88:91], v[152:155]\n"
    "buffer_load_dwordx4 a[128:131], v5, s[28:31], s50 offen offset:0 sc1\n"
    "v_mfma_f32_16x16x32_bf16 v[152:155], a[168:171], a[92:95], v[152:155]\n"
    "v_mfma_f32_16x16x32_bf16 v[152:155], a[172:175], a[96:99], v[152:155]\n"
    "v_cvt_pk_bf16_f32 v48, v48, v49\n"
    "v_cvt_pk_bf16_f32 v49, v50, v51\n"
    "v_mfma_f32_16x16x32_bf16 v[152:155], a[176:179], a[100:103], v[152:155]\n"
    "v_cvt_pk_bf16_f32 v50, v52, v53\n"
    "v_cvt_pk_bf16_f32 v51, v54, v55\n"
    "v_mfma_f32_16x16x32_bf16 v[152:155], a[180:183], a[104:107], v[152:155]\n"
    "v_cvt_pk_bf16_f32 v52, v56, v57\n"
    "v_cvt_pk_bf16_f32 v53, v58, v59\n"
    "v_mfma_f32_16x16x32_bf16 v[156:159], a[156:159], a[84:87], v[156:159]\n"
    "ds_read_b64_tr_b16 a[220:221], v37 offset:14080\n"
    "ds_read_b64_tr_b16 a[222:223], v37 offset:14336\n"
    "v_mfma_f32_16x16x32_bf16 v[156:159], a[160:163], a[88:91], v[156:159]\n"
    "v_cvt_pk_bf16_f32 v54, v60, v61\n"
    "v_cvt_pk_bf16_f32 v55, v62, v63\n"
    "v_mfma_f32_16x16x32_bf16 v[156:159], a[164:167], a[92:95], v[156:159]\n"
    "buffer_load_dwordx4 a[132:135], v5, s[28:31], s50 offen offset:1024 sc1\n"
    "ds_read_b128 a[80:83], v19 offset:52352\n"
    "v_mfma_f32_16x16x32_bf16 v[156:159], a[168:171], a[96:99], v[156:159]\n"
    "ds_read_b128 a[32:35], v18 offset:54688\n"
    "v_mfma_f32_16x16x32_bf16 v[156:159], a[172:175], a[100:103], v[156:159]\n"
    "ds_read_b128 a[84:87], v19 offset:52896\n"
    "v_mfma_f32_16x16x32_bf16 v[156:159], a[176:179], a[104:107], v[156:159]\n"
    "ds_read_b128 a[36:39], v18 offset:55232\n"
    "v_mfma_f32_16x16x32_bf16 v[156:159], a[180:183], a[108:111], v[156:159]\n"
    "ds_read_b128 a[40:43], v18 offset:55776\n"
    "v_mfma_f32_16x16x32_bf16 v[160:163], a[156:159], a[88:91], v[160:163]\n"
    "ds_read_b128 a[44:47], v18 offset:56320\n"
    "v_mfma_f32_16x16x32_bf16 v[160:163], a[160:163], a[92:95], v[160:163]\n"
    "s_waitcnt lgkmcnt(6)\n"
    "v_mfma_f32_32x32x16_bf16 v[64:79], a[220:223], a[228:231], 0\n"
    "ds_write_b128 v15, v[48:51] offset:4352\n"
    "v_mfma_f32_16x16x32_bf16 v[160:163], a[164:167], a[96:99], v[160:163]\n"
    "v_mfma_f32_16x16x32_bf16 v[160:163], a[168:171], a[100:103], v[160:163]\n"
    "buffer_load_dwordx4 a[136:139], v5, s[28:31], s50 offen offset:2048 sc1\n"
    "v_mfma_f32_16x16x32_bf16 v[160:163], a[172:175], a[104:107], v[160:163]\n"
    "ds_read_b128 a[88:91], v19 offset:53440\n"
    "v_mfma_f32_16x16x32_bf16 v[160:163], a[176:179], a[108:111], v[160:163]\n"
    "ds_read_b128 a[48:51], v18 offset:56864\n"
    "v_mfma_f32_16x16x32_bf16 v[160:163], a[180:183], a[112:115], v[160:163]\n"
    "ds_read_b64_tr_b16 a[224:225], v37 offset:14464\n"
    "ds_read_b64_tr_b16 a[226:227], v37 offset:14720\n"
    "v_mfma_f32_16x16x32_bf16 v[164:167], a[156:159], a[92:95], v[164:167]\n"
    "v_xor_b32 v12, 32, v12\n"
    "v_xor_b32 v13, 32, v13\n"
    "v_mfma_f32_16x16x32_bf16 v[164:167], a[160:163], a[96:99], v[164:167]\n"
    "v_xor_b32 v14, 32, v14\n"
    "v_xor_b32 v31, 32, v31\n"
    "v_mfma_f32_16x16x32_bf16 v[164:167], a[164:167], a[100:103], v[164:167]\n"
    "v_xor_b32 v32, 32, v32\n"
    "v_xor_b32 v37, 32, v37\n"
    "v_mfma_f32_16x16x32_bf16 v[164:167], a[168:171], a[104:107], v[164:167]\n"
    "ds_read_b128 a[92:95], v19 offset:53984\n"
    "v_mfma_f32_16x16x32_bf16 v[164:167], a[172:175], a[108:111], v[164:167]\n"
    "buffer_load_dwordx4 a[140:143], v5, s[28:31], s50 offen offset:3072 sc1\n"
    "s_add_u32 s50, s50, 4096\n"
    "v_mfma_f32_16x16x32_bf16 v[164:167], a[176:179], a[112:115], v[164:167]\n"
    "ds_read_b128 a[52:55], v18 offset:57408\n"
    "v_mfma_f32_16x16x32_bf16 v[164:167], a[180:183], a[116:119], v[164:167]\n"
    "ds_read_b128 a[56:59], v18 offset:57952\n"
    "v_mfma_f32_16x16x32_bf16 v[168:171], a[156:159], a[96:99], v[168:171]\n"
    "s_waitcnt lgkmcnt(3)\n"
    "v_mfma_f32_32x32x16_bf16 v[64:79], a[224:227], a[232:235], v[64:79]\n"
    "ds_write_b128 v15, v[52:55] offset:4896\n"
    "v_mfma_f32_16x16x32_bf16 v[168:171], a[160:163], a[100:103], v[168:171]\n"
    "v_mfma_f32_16x16x32_bf16 v[168:171], a[164:167], a[104:107], v[168:171]\n"
    "v_mfma_f32_16x16x32_bf16 v[168:171], a[168:171], a[108:111], v[168:171]\n"
    "v_cvt_pk_bf16_f32 v64, v64, v65\n"
    "v_cvt_pk_bf16_f32 v65, v66, v67\n"
    "v_mfma_f32_16x16x32_bf16 v[168:171], a[172:175], a[112:115], v[168:171]\n"
    "v_cvt_pk_bf16_f32 v66, v68, v69\n"
    "v_cvt_pk_bf16_f32 v67, v70, v71\n"
    "v_mfma_f32_16x16x32_bf16 v[168:171], a[176:179], a[116:119], v[168:171]\n"
    "v_cvt_pk_bf16_f32 v68, v72, v73\n"
    "v_cvt_pk_bf16_f32 v69, v74, v75\n"
    "v_mfma_f32_16x16x32_bf16 v[168:171], a[180:183], a[120:123], v[168:171]\n"
    "v_cvt_pk_bf16_f32 v70, v76, v77\n"
    "v_cvt_pk_bf16_f32 v71, v78, v79\n"
    "v_mfma_f32_16x16x32_bf16 v[172:175], a[156:159], a[100:103], v[172:175]\n"
    "ds_read_b64_tr_b16 a[220:221], v12 offset:0\n"
    "ds_read_b64_tr_b16 a[222:223], v12 offset:256\n"
    "v_mfma_f32_16x16x32_bf16 v[172:175], a[160:163], a[104:107], v[172:175]\n"
    "buffer_load_dwordx4 a[144:147], v5, s[28:31], s50 offen offset:0 sc1\n"
    "ds_read_b128 a[96:99], v19 offset:54528\n"
    "v_mfma_f32_16x16x32_bf16 v[172:175], a[164:167], a[108:111], v[172:175]\n"
    "ds_read_b128 a[60:63], v18 offset:58496\n"
    "v_mfma_f32_16x16x32_bf16 v[172:175], a[168:171], a[112:115], v[172:175]\n"
    "ds_read_b128 a[100:103], v19 offset:55072\n"
    "v_mfma_f32_16x16x32_bf16 v[172:175], a[172:175], a[116:119], v[172:175]\n"
    "v_mfma_f32_16x16x32_bf16 v[172:175], a[176:179], a[120:123], v[172:175]\n"
    "ds_read_b128 a[104:107], v19 offset:55616\n"
    "v_mfma_f32_16x16x32_bf16 v[172:175], a[180:183], a[124:127], v[172:175]\n"
    "s_waitcnt lgkmcnt(4)\n"
    "buffer_load_dwordx4 a[148:151], v5, s[28:31], s50 offen offset:1024 sc1\n"
    "v_mfma_f32_32x32x16_bf16 v[48:63], a[220:223], a[228:231], 0\n"
    "s_waitcnt vmcnt(8)\n"
    "ds_write_b128 v16, v[84:87] offset:544\n"
    "v_mfma_f32_16x16x32_bf16 v[176:179], a[184:187], a[0:3], v[176:179]\n"
    "v_mfma_f32_16x16x32_bf16 v[176:179], a[188:191], a[4:7], v[176:179]\n"
    "v_mfma_f32_16x16x32_bf16 v[176:179], a[192:195], a[8:11], v[176:179]\n"
    "ds_read_b128 a[108:111], v19 offset:56160\n"
    "v_mfma_f32_16x16x32_bf16 v[176:179], a[196:199], a[12:15], v[176:179]\n"
    "v_mfma_f32_16x16x32_bf16 v[176:179], a[200:203], a[16:19], v[176:179]\n"
    "v_mfma_f32_16x16x32_bf16 v[176:179], a[204:207], a[20:23], v[176:179]\n"
    "ds_read_b64_tr_b16 a[224:225], v12 offset:384\n"
    "ds_read_b64_tr_b16 a[226:227], v12 offset:640\n"
    "v_mfma_f32_16x16x32_bf16 v[176:179], a[208:211], a[24:27], v[176:179]\n"
    "v_mfma_f32_16x16x32_bf16 v[180:183], a[184:187], a[4:7], v[180:183]\n"
    "buffer_load_dwordx4 a[152:155], v5, s[28:31], s50 offen offset:2048 sc1\n"
    "v_mfma_f32_16x16x32_bf16 v[180:183], a[188:191], a[8:11], v[180:183]\n"
    "v_mfma_f32_16x16x32_bf16 v[180:183], a[192:195], a[12:15], v[180:183]\n"
    "v_mfma_f32_16x16x32_bf16 v[180:183], a[196:199], a[16:19], v[180:183]\n"
    "v_mfma_f32_16x16x32_bf16 v[180:183], a[200:203], a[20:23], v[180:183]\n"
    "v_mfma_f32_16x16x32_bf16 v[180:183], a[204:207], a[24:27], v[180:183]\n"
    "v_mfma_f32_16x16x32_bf16 v[180:183], a[208:211], a[28:31], v[180:183]\n"
    "s_waitcnt lgkmcnt(0)\n"
    "v_mfma_f32_32x32x16_bf16 v[48:63], a[224:227], a[232:235], v[48:63]\n"
    "ds_write_b128 v33, v[64:67] offset:5440\n"
    "v_mfma_f32_16x16x32_bf16 v[184:187], a[184:187], a[8:11], v[184:187]\n"
    "v_mfma_f32_16x16x32_bf16 v[184:187], a[188:191], a[12:15], v[184:187]\n"
    "buffer_load_dwordx4 a[156:159], v5, s[28:31], s50 offen offset:3072 sc1\n"
    "s_add_u32 s50, s50, 4096\n"
    "v_mfma_f32_16x16x32_bf16 v[184:187], a[192:195], a[16:19], v[184:187]\n"
    "v_cvt_pk_bf16_f32 v48, v48, v49\n"
    "v_cvt_pk_bf16_f32 v49, v50, v51\n"
    "v_mfma_f32_16x16x32_bf16 v[184:187], a[196:199], a[20:23], v[184:187]\n"
    "v_cvt_pk_bf16_f32 v50, v52, v53\n"
    "v_cvt_pk_bf16_f32 v51, v54, v55\n"
    "v_mfma_f32_16x16x32_bf16 v[184:187], a[200:203], a[24:27], v[184:187]\n"
    "v_cvt_pk_bf16_f32 v52, v56, v57\n"
    "v_cvt_pk_bf16_f32 v53, v58, v59\n"
    "v_mfma_f32_16x16x32_bf16 v[184:187], a[204:207], a[28:31], v[184:187]\n"
    "v_cvt_pk_bf16_f32 v54, v60, v61\n"
    "v_cvt_pk_bf16_f32 v55, v62, v63\n"
    "v_mfma_f32_16x16x32_bf16 v[184:187], a[208:211], a[32:35], v[184:187]\n"
    "ds_read_b64_tr_b16 a[220:221], v13\n"
    "ds_read_b64_tr_b16 a[222:223], v14\n"
    "v_mfma_f32_16x16x32_bf16 v[188:191], a[184:187], a[12:15], v[188:191]\n"
    "v_mfma_f32_16x16x32_bf16 v[188:191], a[188:191], a[16:19], v[188:191]\n"
    "v_mfma_f32_16x16x32_bf16 v[188:191], a[192:195], a[20:23], v[188:191]\n"
    "buffer_load_dwordx4 a[160:163], v5, s[28:31], s50 offen offset:0 sc1\n"
    "v_mfma_f32_16x16x32_bf16 v[188:191], a[196:199], a[24:27], v[188:191]\n"
    "v_mfma_f32_16x16x32_bf16 v[188:191], a[200:203], a[28:31], v[188:191]\n"
    "v_mfma_f32_16x16x32_bf16 v[188:191], a[204:207], a[32:35], v[188:191]\n"
    "s_waitcnt lgkmcnt(0)\n"
    "v_mfma_f32_32x32x16_bf16 v[80:95], a[220:223], a[236:239], 0\n"
    "ds_write_b128 v33, v[68:71] offset:5984\n"
    "v_mfma_f32_16x16x32_bf16 v[188:191], a[208:211], a[36:39], v[188:191]\n"
    "v_mfma_f32_16x16x32_bf16 v[192:195], a[184:187], a[16:19], v[192:195]\n"
    "v_mfma_f32_16x16x32_bf16 v[192:195], a[188:191], a[20:23], v[192:195]\n"
    "v_mfma_f32_16x16x32_bf16 v[192:195], a[192:195], a[24:27], v[192:195]\n"
    "buffer_load_dwordx4 a[164:167], v5, s[28:31], s50 offen offset:1024 sc1\n"
    "v_mfma_f32_16x16x32_bf16 v[192:195], a[196:199], a[28:31], v[192:195]\n"
    "v_mfma_f32_16x16x32_bf16 v[192:195], a[200:203], a[32:35], v[192:195]\n"
    "ds_read_b64_tr_b16 a[224:225], v12 offset:2816\n"
    "ds_read_b64_tr_b16 a[226:227], v12 offset:3072\n"
    "v_mfma_f32_16x16x32_bf16 v[192:195], a[204:207], a[36:39], v[192:195]\n"
    "v_mfma_f32_16x16x32_bf16 v[192:195], a[208:211], a[40:43], v[192:195]\n"
    "v_mfma_f32_16x16x32_bf16 v[196:199], a[184:187], a[20:23], v[196:199]\n"
    "v_mfma_f32_16x16x32_bf16 v[196:199], a[188:191], a[24:27], v[196:199]\n"
    "v_mfma_f32_16x16x32_bf16 v[196:199], a[192:195], a[28:31], v[196:199]\n"
    "v_mfma_f32_16x16x32_bf16 v[196:199], a[196:199], a[32:35], v[196:199]\n"
    "buffer_load_dwordx4 a[168:171], v5, s[28:31], s50 offen offset:2048 sc1\n"
    "v_mfma_f32_16x16x32_bf16 v[196:199], a[200:203], a[36:39], v[196:199]\n"
    "v_mfma_f32_16x16x32_bf16 v[196:199], a[204:207], a[40:43], v[196:199]\n"
    "s_waitcnt lgkmcnt(0)\n"
    "v_mfma_f32_32x32x16_bf16 v[64:79], a[224:227], a[228:231], 0\n"
    "s_waitcnt lgkmcnt(0)\n"
    "s_barrier\n"
    "v_mfma_f32_16x16x32_bf16 v[196:199], a[208:211], a[44:47], v[196:199]\n"
    "ds_write_b128 v15, v[48:51] offset:50176\n"
    "v_mfma_f32_16x16x32_bf16 v[200:203], a[184:187], a[24:27], v[200:203]\n"
    "v_mfma_f32_16x16x32_bf16 v[200:203], a[188:191], a[28:31], v[200:203]\n"
    "v_mfma_f32_16x16x32_bf16 v[200:203], a[192:195], a[32:35], v[200:203]\n"
    "ds_read_b128 a[0:3], v18 offset:0\n"
    "v_mfma_f32_16x16x32_bf16 v[200:203], a[196:199], a[36:39], v[200:203]\n"
    "v_mfma_f32_16x16x32_bf16 v[200:203], a[200:203], a[40:43], v[200:203]\n"
    "buffer_load_dwordx4 a[172:175], v5, s[28:31], s50 offen offset:3072 sc1\n"
    "s_add_u32 s50, s50, 4096\n"
    "v_mfma_f32_16x16x32_bf16 v[200:203], a[204:207], a[44:47], v[200:203]\n"
    "ds_read_b128 a[4:7], v18 offset:544\n"
    "v_mfma_f32_16x16x32_bf16 v[200:203], a[208:211], a[48:51], v[200:203]\n"
    "v_mfma_f32_16x16x32_bf16 v[204:207], a[184:187], a[28:31], v[204:207]\n"
    "ds_read_b128 a[8:11], v18 offset:1088\n"
    "v_mfma_f32_16x16x32_bf16 v[204:207], a[188:191], a[32:35], v[204:207]\n"
    "v_mfma_f32_16x16x32_bf16 v[204:207], a[192:195], a[36:39], v[204:207]\n"
    "ds_read_b128 a[12:15], v18 offset:1632\n"
    "v_mfma_f32_16x16x32_bf16 v[204:207], a[196:199], a[40:43], v[204:207]\n"
    "v_mfma_f32_16x16x32_bf16 v[204:207], a[200:203], a[44:47], v[204:207]\n"
    "ds_read_b128 a[16:19], v18 offset:2176\n"
    "v_mfma_f32_16x16x32_bf16 v[204:207], a[204:207], a[48:51], v[204:207]\n"
    "buffer_load_dwordx4 a[176:179], v5, s[28:31], s50 offen offset:0 sc1\n"
    "v_mfma_f32_16x16x32_bf16 v[204:207], a[208:211], a[52:55], v[204:207]\n"
    "ds_read_b128 a[20:23], v18 offset:2720\n"
    "v_mfma_f32_16x16x32_bf16 v[208:211], a[184:187], a[32:35], v[208:211]\n"
    "v_mfma_f32_16x16x32_bf16 v[208:211], a[188:191], a[36:39], v[208:211]\n"
    "ds_read_b128 a[24:27], v18 offset:3264\n"
    "v_mfma_f32_16x16x32_bf16 v[208:211], a[192:195], a[40:43], v[208:211]\n"
    "v_mfma_f32_16x16x32_bf16 v[208:211], a[196:199], a[44:47], v[208:211]\n"
    "ds_read_b128 a[28:31], v18 offset:3808\n"
    "v_mfma_f32_16x16x32_bf16 v[208:211], a[200:203], a[48:51], v[208:211]\n"
    "v_mfma_f32_16x16x32_bf16 v[208:211], a[204:207], a[52:55], v[208:211]\n"
    "ds_read_b128 a[32:35], v18 offset:4352\n"
    "v_mfma_f32_16x16x32_bf16 v[208:211], a[208:211], a[56:59], v[208:211]\n"
    "buffer_load_dwordx4 a[180:183], v5, s[28:31], s50 offen offset:1024 sc1\n"
    "v_mfma_f32_16x16x32_bf16 v[212:215], a[184:187], a[36:39], v[212:215]\n"
    "v_mfma_f32_16x16x32_bf16 v[212:215], a[188:191], a[40:43], v[212:215]\n"
    "ds_read_b128 a[36:39], v18 offset:4896\n"
    "v_mfma_f32_16x16x32_bf16 v[212:215], a[192:195], a[44:47], v[212:215]\n"
    "v_mfma_f32_16x16x32_bf16 v[212:215], a[196:199], a[48:51], v[212:215]\n"
    "ds_read_b128 a[40:43], v18 offset:5440\n"
    "v_mfma_f32_16x16x32_bf16 v[212:215], a[200:203], a[52:55], v[212:215]\n"
    "v_mfma_f32_16x16x32_bf16 v[212:215], a[204:207], a[56:59], v[212:215]\n"
    "ds_read_b128 a[44:47], v18 offset:5984\n"
    "v_mfma_f32_16x16x32_bf16 v[212:215], a[208:211], a[60:63], v[212:215]\n"
    "s_waitcnt vmcnt(14) lgkmcnt(4)\n"
    "v_mfma_f32_16x16x32_bf16 v[216:219], a[212:215], a[64:67], v[216:219]\n"
    "buffer_load_dwordx4 a[184:187], v5, s[28:31], s50 offen offset:2048 sc1\n"
    "ds_read_b128 a[48:51], v18 offset:6528\n"
    "v_mfma_f32_16x16x32_bf16 v[216:219], a[216:219], a[72:75], v[216:219]\n"
    "v_mfma_f32_16x16x32_bf16 v[220:223], a[212:215], a[68:71], v[220:223]\n"
    "ds_read_b128 a[52:55], v18 offset:7072\n"
    "v_mfma_f32_16x16x32_bf16 v[220:223], a[216:219], a[76:79], v[220:223]\n"
    "v_mfma_f32_16x16x32_bf16 v[224:227], a[212:215], a[72:75], v[224:227]\n"
    "ds_read_b128 a[56:59], v18 offset:7616\n"
    "v_mfma_f32_16x16x32_bf16 v[224:227], a[216:219], a[80:83], v[224:227]\n"
    "v_mfma_f32_16x16x32_bf16 v[228:231], a[212:215], a[76:79], v[228:231]\n"
    "ds_read_b128 a[60:63], v18 offset:8160\n"
    "v_mfma_f32_16x16x32_bf16 v[228:231], a[216:219], a[84:87], v[228:231]\n"
    "v_mfma_f32_16x16x32_bf16 v[232:235], a[212:215], a[80:83], v[232:235]\n"
    "buffer_load_dwordx4 a[188:191], v5, s[28:31], s50 offen offset:3072 sc1\n"
    "s_add_u32 s50, s50, 4096\n"
    "v_mfma_f32_16x16x32_bf16 v[232:235], a[216:219], a[88:91], v[232:235]\n"
    "v_mfma_f32_16x16x32_bf16 v[236:239], a[212:215], a[84:87], v[236:239]\n"
    "v_mfma_f32_16x16x32_bf16 v[236:239], a[216:219], a[92:95], v[236:239]\n"
    "v_mfma_f32_16x16x32_bf16 v[240:243], a[212:215], a[88:91], v[240:243]\n"
    "v_mfma_f32_16x16x32_bf16 v[240:243], a[216:219], a[96:99], v[240:243]\n"
    "v_mfma_f32_16x16x32_bf16 v[244:247], a[212:215], a[92:95], v[244:247]\n"
    "v_mfma_f32_16x16x32_bf16 v[244:247], a[216:219], a[100:103], v[244:247]\n"
    "v_mfma_f32_16x16x32_bf16 v[248:251], a[212:215], a[96:99], v[248:251]\n"
    "v_mfma_f32_16x16x32_bf16 v[248:251], a[216:219], a[104:107], v[248:251]\n"
    "v_mfma_f32_16x16x32_bf16 v[252:255], a[212:215], a[100:103], v[252:255]\n"
    "v_mfma_f32_16x16x32_bf16 v[252:255], a[216:219], a[108:111], v[252:255]\n"
    "s_waitcnt vmcnt(9) lgkmcnt(0)\n"
    "s_cmp_eq_u32 s52, 1\n"
    "s_cbranch_scc1 22f\n"
    "v_mfma_f32_16x16x32_bf16 v[96:99], a[128:131], a[0:3], v[96:99]\n"
    "s_add_u32 s58, s51, 0\n"
    "ds_read_b64_tr_b16 a[220:221], v12 offset:3200\n"
    "ds_read_b64_tr_b16 a[222:223], v12 offset:3456\n"
    "v_mfma_f32_16x16x32_bf16 v[96:99], a[132:135], a[4:7], v[96:99]\n"
    "s_add_u32 m0, s48, 0\n"
    "buffer_load_dwordx4 a[192:195], v5, s[28:31], s50 offen offset:0 sc1\n"
    "ds_read_b128 a[64:67], v18 offset:80\n"
    "v_mfma_f32_16x16x32_bf16 v[96:99], a[136:139], a[8:11], v[96:99]\n"
    "v_add_u32 v10, s58, v6\n"
    "v_mfma_f32_16x16x32_bf16 v[96:99], a[140:143], a[12:15], v[96:99]\n"
    "buffer_load_dwordx4 v10, s[16:19], 0 offen lds\n"
    "v_mfma_f32_16x16x32_bf16 v[96:99], a[144:147], a[16:19], v[96:99]\n"
    "ds_read_b128 a[68:71], v18 offset:624\n"
    "v_mfma_f32_16x16x32_bf16 v[96:99], a[148:151], a[20:23], v[96:99]\n"
    "v_mfma_f32_16x16x32_bf16 v[96:99], a[152:155], a[24:27], v[96:99]\n"
    "s_waitcnt lgkmcnt(2)\n"
    "v_mfma_f32_32x32x16_bf16 v[64:79], a[220:223], a[232:235], v[64:79]\n"
    "ds_write_b128 v15, v[52:55] offset:50720\n"
    "v_mfma_f32_16x16x32_bf16 v[100:103], a[128:131], a[4:7], v[100:103]\n"
    "v_mfma_f32_16x16x32_bf16 v[100:103], a[132:135], a[8:11], v[100:103]\n"
    "v_mfma_f32_16x16x32_bf16 v[100:103], a[136:139], a[12:15], v[100:103]\n"
    "s_add_u32 m0, s48, 1024\n"
    "v_cvt_pk_bf16_f32 v64, v64, v65\n"
    "v_cvt_pk_bf16_f32 v65, v66, v67\n"
    "v_mfma_f32_16x16x32_bf16 v[100:103], a[140:143], a[16:19], v[100:103]\n"
    "v_cvt_pk_bf16_f32 v66, v68, v69\n"
    "v_cvt_pk_bf16_f32 v67, v70, v71\n"
    "v_mfma_f32_16x16x32_bf16 v[100:103], a[144:147], a[20:23], v[100:103]\n"
    "ds_read_b64_tr_b16 a[224:225], v12 offset:5632\n"
    "ds_read_b64_tr_b16 a[226:227], v12 offset:5888\n"
    "v_mfma_f32_16x16x32_bf16 v[100:103], a[148:151], a[24:27], v[100:103]\n"
    "v_cvt_pk_bf16_f32 v68, v72, v73\n"
    "v_cvt_pk_bf16_f32 v69, v74, v75\n"
    "v_mfma_f32_16x16x32_bf16 v[100:103], a[152:155], a[28:31], v[100:103]\n"
    "v_cvt_pk_bf16_f32 v70, v76, v77\n"
    "v_cvt_pk_bf16_f32 v71, v78, v79\n"
    "v_mfma_f32_16x16x32_bf16 v[104:107], a[128:131], a[8:11], v[104:107]\n"
    "buffer_load_dwordx4 a[196:199], v5, s[28:31], s50 offen offset:1024 sc1\n"
    "ds_read_b128 a[72:75], v18 offset:1168\n"
    "v_mfma_f32_16x16x32_bf16 v[104:107], a[132:135], a[12:15], v[104:107]\n"
    "ds_read_b128 a[76:79], v18 offset:1712\n"
    "v_add_u32 v10, s58, v7\n"
    "v_mfma_f32_16x16x32_bf16 v[104:107], a[136:139], a[16:19], v[104:107]\n"
    "ds_read_b128 a[80:83], v18 offset:2256\n"
    "buffer_load_dwordx4 v10, s[16:19], 0 offen lds\n"
    "v_mfma_f32_16x16x32_bf16 v[104:107], a[140:143], a[20:23], v[104:107]\n"
    "v_mfma_f32_16x16x32_bf16 v[104:107], a[144:147], a[24:27], v[104:107]\n"
    "buffer_load_dwordx4 a[200:203], v5, s[28:31], s50 offen offset:2048 sc1\n"
    "v_mfma_f32_16x16x32_bf16 v[104:107], a[148:151], a[28:31], v[104:107]\n"
    "s_waitcnt lgkmcnt(3)\n"
    "s_add_u32 m0, s48, 2048\n"
    "v_add_u32 v10, s58, v8\n"
    "v_mfma_f32_32x32x16_bf16 v[48:63], a[224:227], a[228:231], 0\n"
    "ds_write_b128 v15, v[64:67] offset:51264\n"
    "v_mfma_f32_16x16x32_bf16 v[104:107], a[152:155], a[32:35], v[104:107]\n"
    "s_mov_b32 exec_hi, 0xffff\n"
    "buffer_load_dwordx4 v10, s[16:19], 0 offen lds\n"
    "s_mov_b32 exec_hi, -1\n"
    "v_mfma_f32_16x16x32_bf16 v[108:111], a[128:131], a[12:15], v[108:111]\n"
    "v_mfma_f32_16x16x32_bf16 v[108:111], a[132:135], a[16:19], v[108:111]\n"
    "ds_read_b128 a[84:87], v18 offset:2800\n"
    "v_mfma_f32_16x16x32_bf16 v[108:111], a[136:139], a[20:23], v[108:111]\n"
    "ds_read_b128 a[88:91], v18 offset:3344\n"
    "v_mfma_f32_16x16x32_bf16 v[108:111], a[140:143], a[24:27], v[108:111]\n"
    "ds_read_b64_tr_b16 a[220:221], v12 offset:6016\n"
    "ds_read_b64_tr_b16 a[222:223], v12 offset:6272\n"
    "v_mfma_f32_16x16x32_bf16 v[108:111], a[144:147], a[28:31], v[108:111]\n"
    "v_mfma_f32_16x16x32_bf16 v[108:111], a[148:151], a[32:35], v[108:111]\n"
    "v_mfma_f32_16x16x32_bf16 v[108:111], a[152:155], a[36:39], v[108:111]\n"
    "buffer_load_dwordx4 a[204:207], v5, s[28:31], s50 offen offset:3072 sc1\n"
    "s_add_u32 s50, s50, 4096\n"
    "v_mfma_f32_16x16x32_bf16 v[112:115], a[128:131], a[16:19], v[112:115]\n"
    "s_add_u32 s58, s51, 172032\n"
    "ds_read_b128 a[92:95], v18 offset:3888\n"
    "s_add_u32 m0, s48, 2816\n"
    "v_mfma_f32_16x16x32_bf16 v[112:115], a[132:135], a[20:23], v[112:115]\n"
    "v_add_u32 v10, s58, v6\n"
    "v_mfma_f32_16x16x32_bf16 v[112:115], a[136:139], a[24:27], v[112:115]\n"
    "buffer_load_dwordx4 v10, s[16:19], 0 offen lds\n"
    "v_mfma_f32_16x16x32_bf16 v[112:115], a[140:143], a[28:31], v[112:115]\n"
    "ds_read_b128 a[96:99], v18 offset:4432\n"
    "v_mfma_f32_16x16x32_bf16 v[112:115], a[144:147], a[32:35], v[112:115]\n"
    "s_waitcnt lgkmcnt(2)\n"
    "v_mfma_f32_32x32x16_bf16 v[48:63], a[220:223], a[232:235], v[48:63]\n"
    "ds_write_b128 v15, v[68:71] offset:51808\n"
    "v_mfma_f32_16x16x32_bf16 v[112:115], a[148:151], a[36:39], v[112:115]\n"
    "v_mfma_f32_16x16x32_bf16 v[112:115], a[152:155], a[40:43], v[112:115]\n"
    "v_mfma_f32_16x16x32_bf16 v[116:119], a[128:131], a[20:23], v[116:119]\n"
    "v_cvt_pk_bf16_f32 v48, v48, v49\n"
    "v_cvt_pk_bf16_f32 v49, v50, v51\n"
    "v_mfma_f32_16x16x32_bf16 v[116:119], a[132:135], a[24:27], v[116:119]\n"
    "v_cvt_pk_bf16_f32 v50, v52, v53\n"
    "v_cvt_pk_bf16_f32 v51, v54, v55\n"
    "v_mfma_f32_16x16x32_bf16 v[116:119], a[136:139], a[28:31], v[116:119]\n"
    "s_add_u32 m0, s48, 3840\n"
    "v_cvt_pk_bf16_f32 v52, v56, v57\n"
    "v_cvt_pk_bf16_f32 v53, v58, v59\n"
    "v_mfma_f32_16x16x32_bf16 v[116:119], a[140:143], a[32:35], v[116:119]\n"
    "ds_read_b64_tr_b16 a[224:225], v31\n"
    "ds_read_b64_tr_b16 a[226:227], v32\n"
    "v_mfma_f32_16x16x32_bf16 v[116:119], a[144:147], a[36:39], v[116:119]\n"
    "v_cvt_pk_bf16_f32 v54, v60, v61\n"
    "v_cvt_pk_bf16_f32 v55, v62, v63\n"
    "v_mfma_f32_16x16x32_bf16 v[116:119], a[148:151], a[40:43], v[116:119]\n"
    "buffer_load_dwordx4 a[208:211], v5, s[28:31], s50 offen offset:0 sc1\n"
    "ds_read_b128 a[100:103], v18 offset:4976\n"
    "v_mfma_f32_16x16x32_bf16 v[116:119], a[152:155], a[44:47], v[116:119]\n"
    "ds_read_b128 a[104:107], v18 offset:5520\n"
    "v_add_u32 v10, s58, v7\n"
    "v_mfma_f32_16x16x32_bf16 v[120:123], a[128:131], a[24:27], v[120:123]\n"
    "ds_read_b128 a[108:111], v18 offset:6064\n"
    "buffer_load_dwordx4 v10, s[16:19], 0 offen lds\n"
    "v_mfma_f32_16x16x32_bf16 v[120:123], a[132:135], a[28:31], v[120:123]\n"
    "v_mfma_f32_16x16x32_bf16 v[120:123], a[136:139], a[32:35], v[120:123]\n"
    "v_mfma_f32_16x16x32_bf16 v[120:123], a[140:143], a[36:39], v[120:123]\n"
    "buffer_load_dwordx4 a[212:215], v5, s[28:31], s50 offen offset:1024 sc1\n"
    "v_mfma_f32_16x16x32_bf16 v[120:123], a[144:147], a[40:43], v[120:123]\n"
    "s_waitcnt lgkmcnt(3)\n"
    "v_mfma_f32_32x32x16_bf16 v[80:95], a[224:227], a[240:243], v[80:95]\n"
    "s_add_u32 m0, s48, 4864\n"
    "ds_write_b128 v15, v[48:51] offset:52352\n"
    "v_add_u32 v10, s58, v8\n"
    "v_mfma_f32_16x16x32_bf16 v[120:123], a[148:151], a[44:47], v[120:123]\n"
    "s_mov_b32 exec_hi, 0xffff\n"
    "buffer_load_dwordx4 v10, s[16:19], 0 offen lds\n"
    "s_mov_b32 exec_hi, -1\n"
    "v_mfma_f32_16x16x32_bf16 v[120:123], a[152:155], a[48:51], v[120:123]\n"
    "v_mfma_f32_16x16x32_bf16 v[124:127], a[128:131], a[28:31], v[124:127]\n"
    "v_cvt_pk_bf16_f32 v80, v80, v81\n"
    "v_cvt_pk_bf16_f32 v81, v82, v83\n"
    "v_mfma_f32_16x16x32_bf16 v[124:127], a[132:135], a[32:35], v[124:127]\n"
    "v_cvt_pk_bf16_f32 v82, v84, v85\n"
    "v_cvt_pk_bf16_f32 v83, v86, v87\n"
    "v_mfma_f32_16x16x32_bf16 v[124:127], a[136:139], a[36:39], v[124:127]\n"
    "ds_read_b64_tr_b16 a[220:221], v12 offset:8448\n"
    "ds_read_b64_tr_b16 a[222:223], v12 offset:8704\n"
    "v_mfma_f32_16x16x32_bf16 v[124:127], a[140:143], a[40:43], v[124:127]\n"
    "v_cvt_pk_bf16_f32 v84, v88, v89\n"
    "v_cvt_pk_bf16_f32 v85, v90, v91\n"
    "v_mfma_f32_16x16x32_bf16 v[124:127], a[144:147], a[44:47], v[124:127]\n"
    "v_cvt_pk_bf16_f32 v86, v92, v93\n"
    "v_cvt_pk_bf16_f32 v87, v94, v95\n"
    "v_mfma_f32_16x16x32_bf16 v[124:127], a[148:151], a[48:51], v[124:127]\n"
    "buffer_load_dwordx4 a[216:219], v5, s[28:31], s50 offen offset:2048 sc1\n"
    "s_add_u32 s50, s50, 3072\n"
    "v_mfma_f32_16x16x32_bf16 v[124:127], a[152:155], a[52:55], v[124:127]\n"
    "ds_read_b128 a[112:115], v18 offset:6608\n"
    "v_mfma_f32_16x16x32_bf16 v[128:131], a[128:131], a[32:35], v[128:131]\n"
    "s_add_u32 s58, s51, 344064\n"
    "ds_read_b128 a[116:119], v18 offset:7152\n"
    "s_add_u32 m0, s48, 5632\n"
    "v_mfma_f32_16x16x32_bf16 v[128:131], a[132:135], a[36:39], v[128:131]\n"
    "ds_read_b128 a[120:123], v18 offset:7696\n"
    "buffer_load_dwordx4 v6, s[16:19], s58 offen lds\n"
    "v_mfma_f32_16x16x32_bf16 v[128:131], a[136:139], a[40:43], v[128:131]\n"
    "ds_read_b128 a[124:127], v18 offset:8240\n"
    "v_mfma_f32_16x16x32_bf16 v[128:131], a[140:143], a[44:47], v[128:131]\n"
    "s_waitcnt lgkmcnt(4)\n"
    "v_mfma_f32_32x32x16_bf16 v[64:79], a[220:223], a[228:231], 0\n"
    "ds_write_b128 v15, v[52:55] offset:52896\n"
    "v_mfma_f32_16x16x32_bf16 v[128:131], a[144:147], a[48:51], v[128:131]\n"
    "v_mfma_f32_16x16x32_bf16 v[128:131], a[148:151], a[52:55], v[128:131]\n"
    "v_mfma_f32_16x16x32_bf16 v[128:131], a[152:155], a[56:59], v[128:131]\n"
    "v_mfma_f32_16x16x32_bf16 v[132:135], a[128:131], a[36:39], v[132:135]\n"
    "v_mfma_f32_16x16x32_bf16 v[132:135], a[132:135], a[40:43], v[132:135]\n"
    "ds_read_b64_tr_b16 a[224:225], v12 offset:8832\n"
    "ds_read_b64_tr_b16 a[226:227], v12 offset:9088\n"
    "v_mfma_f32_16x16x32_bf16 v[132:135], a[136:139], a[44:47], v[132:135]\n"
    "s_add_u32 m0, s48, 6656\n"
    "v_mfma_f32_16x16x32_bf16 v[132:135], a[140:143], a[48:51], v[132:135]\n"
    "buffer_load_dwordx4 v7, s[16:19], s58 offen lds\n"
    "v_mfma_f32_16x16x32_bf16 v[132:135], a[144:147], a[52:55], v[132:135]\n"
    "v_mfma_f32_16x16x32_bf16 v[132:135], a[148:151], a[56:59], v[132:135]\n"
    "v_mfma_f32_16x16x32_bf16 v[132:135], a[152:155], a[60:63], v[132:135]\n"
    "s_waitcnt vmcnt(17) lgkmcnt(2)\n"
    "v_mfma_f32_16x16x32_bf16 v[136:139], a[156:159], a[64:67], v[136:139]\n"
    "buffer_load_dwordx4 a[128:131], v5, s[28:31], s50 offen offset:0 sc1\n"
    "v_mfma_f32_16x16x32_bf16 v[136:139], a[160:163], a[68:71], v[136:139]\n"
    "ds_read_b128 a[64:67], v19 offset:0\n"
    "v_mfma_f32_16x16x32_bf16 v[136:139], a[164:167], a[72:75], v[136:139]\n"
    "s_waitcnt lgkmcnt(1)\n"
    "v_mfma_f32_32x32x16_bf16 v[64:79], a[224:227], a[232:235], v[64:79]\n"
    "ds_write_b128 v16, v[80:83] offset:50176\n"
    "v_mfma_f32_16x16x32_bf16 v[136:139], a[168:171], a[76:79], v[136:139]\n"
    "v_mfma_f32_16x16x32_bf16 v[136:139], a[172:175], a[80:83], v[136:139]\n"
    "s_add_u32 m0, s48, 7680\n"
    "v_mfma_f32_16x16x32_bf16 v[136:139], a[176:179], a[84:87], v[136:139]\n"
    "v_cvt_pk_bf16_f32 v64, v64, v65\n"
    "v_cvt_pk_bf16_f32 v65, v66, v67\n"
    "v_mfma_f32_16x16x32_bf16 v[136:139], a[180:183], a[88:91], v[136:139]\n"
    "v_cvt_pk_bf16_f32 v66, v68, v69\n"
    "v_cvt_pk_bf16_f32 v67, v70, v71\n"
    "v_mfma_f32_16x16x32_bf16 v[140:143], a[156:159], a[68:71], v[140:143]\n"
    "v_cvt_pk_bf16_f32 v68, v72, v73\n"
    "v_cvt_pk_bf16_f32 v69, v74, v75\n"
    "v_mfma_f32_16x16x32_bf16 v[140:143], a[160:163], a[72:75], v[140:143]\n"
    "ds_read_b64_tr_b16 a[220:221], v12 offset:11264\n"
    "ds_read_b64_tr_b16 a[222:223], v12 offset:11520\n"
    "v_mfma_f32_16x16x32_bf16 v[140:143], a[164:167], a[76:79], v[140:143]\n"
    "v_cvt_pk_bf16_f32 v70, v76, v77\n"
    "v_cvt_pk_bf16_f32 v71, v78, v79\n"
    "v_mfma_f32_16x16x32_bf16 v[140:143], a[168:171], a[80:83], v[140:143]\n"
    "ds_read_b128 a[68:71], v19 offset:544\n"
    "v_mfma_f32_16x16x32_bf16 v[140:143], a[172:175], a[84:87], v[140:143]\n"
    "ds_read_b128 a[0:3], v18 offset:160\n"
    "v_mfma_f32_16x16x32_bf16 v[140:143], a[176:179], a[88:91], v[140:143]\n"
    "ds_read_b128 a[4:7], v18 offset:704\n"
    "v_mfma_f32_16x16x32_bf16 v[140:143], a[180:183], a[92:95], v[140:143]\n"
    "ds_read_b128 a[8:11], v18 offset:1248\n"
    "v_mfma_f32_16x16x32_bf16 v[144:147], a[156:159], a[72:75], v[144:147]\n"
    "buffer_load_dwordx4 a[132:135], v5, s[28:31], s50 offen offset:1024 sc1\n"
    "ds_read_b128 a[12:15], v18 offset:1792\n"
    "v_mfma_f32_16x16x32_bf16 v[144:147], a[160:163], a[76:79], v[144:147]\n"
    "ds_read_b128 a[72:75], v19 offset:1088\n"
    "v_mfma_f32_16x16x32_bf16 v[144:147], a[164:167], a[80:83], v[144:147]\n"
    "s_waitcnt lgkmcnt(6)\n"
    "v_mfma_f32_32x32x16_bf16 v[48:63], a[220:223], a[228:231], 0\n"
    "ds_write_b128 v15, v[64:67] offset:53440\n"
    "v_mfma_f32_16x16x32_bf16 v[144:147], a[168:171], a[84:87], v[144:147]\n"
    "s_mov_b32 exec_hi, 0xffff\n"
    "buffer_load_dwordx4 v8, s[16:19], s58 offen lds\n"
    "s_mov_b32 exec_hi, -1\n"
    "v_mfma_f32_16x16x32_bf16 v[144:147], a[172:175], a[88:91], v[144:147]\n"
    "s_add_u32 s58, s51, 516096\n"
    "s_add_u32 m0, s48, 8448\n"
    "v_mfma_f32_16x16x32_bf16 v[144:147], a[176:179], a[92:95], v[144:147]\n"
    "ds_read_b128 a[16:19], v18 offset:2336\n"
    "buffer_load_dwordx4 v6, s[16:19], s58 offen lds\n"
    "v_mfma_f32_16x16x32_bf16 v[144:147], a[180:183], a[96:99], v[144:147]\n"
    "ds_read_b128 a[20:23], v18 offset:2880\n"
    "v_mfma_f32_16x16x32_bf16 v[148:151], a[156:159], a[76:79], v[148:151]\n"
    "ds_read_b64_tr_b16 a[224:225], v12 offset:11648\n"
    "ds_read_b64_tr_b16 a[226:227], v12 offset:11904\n"
    "v_mfma_f32_16x16x32_bf16 v[148:151], a[160:163], a[80:83], v[148:151]\n"
    "ds_read_b128 a[76:79], v19 offset:1632\n"
    "v_mfma_f32_16x16x32_bf16 v[148:151], a[164:167], a[84:87], v[148:151]\n"
    "ds_read_b128 a[24:27], v18 offset:3424\n"
    "v_mfma_f32_16x16x32_bf16 v[148:151], a[168:171], a[88:91], v[148:151]\n"
    "v_mfma_f32_16x16x32_bf16 v[148:151], a[172:175], a[92:95], v[148:151]\n"
    "v_mfma_f32_16x16x32_bf16 v[148:151], a[176:179], a[96:99], v[148:151]\n"
    "v_mfma_f32_16x16x32_bf16 v[148:151], a[180:183], a[100:103], v[148:151]\n"
    "ds_read_b128 a[28:31], v18 offset:3968\n"
    "v_mfma_f32_16x16x32_bf16 v[152:155], a[156:159], a[80:83], v[152:155]\n"
    "s_add_u32 m0, s48, 9472\n"
    "buffer_load_dwordx4 a[136:139], v5, s[28:31], s50 offen offset:2048 sc1\n"
    "v_mfma_f32_16x16x32_bf16 v[152:155], a[160:163], a[84:87], v[152:155]\n"
    "s_waitcnt lgkmcnt(3)\n"
    "buffer_load_dwordx4 v7, s[16:19], s58 offen lds\n"
    "v_mfma_f32_32x32x16_bf16 v[48:63], a[224:227], a[232:235], v[48:63]\n"
    "ds_write_b128 v15, v[68:71] offset:53984\n"
    "v_mfma_f32_16x16x32_bf16 v[152:155], a[164:167], a[88:91], v[152:155]\n"
    "v_mfma_f32_16x16x32_bf16 v[152:155], a[168:171], a[92:95], v[152:155]\n"
    "v_mfma_f32_16x16x32_bf16 v[152:155], a[172:175], a[96:99], v[152:155]\n"
    "v_cvt_pk_bf16_f32 v48, v48, v49\n"
    "v_cvt_pk_bf16_f32 v49, v50, v51\n"
    "v_mfma_f32_16x16x32_bf16 v[152:155], a[176:179], a[100:103], v[152:155]\n"
    "v_cvt_pk_bf16_f32 v50, v52, v53\n"
    "v_cvt_pk_bf16_f32 v51, v54, v55\n"
    "v_mfma_f32_16x16x32_bf16 v[152:155], a[180:183], a[104:107], v[152:155]\n"
    "v_cvt_pk_bf16_f32 v52, v56, v57\n"
    "v_cvt_pk_bf16_f32 v53, v58, v59\n"
    "v_mfma_f32_16x16x32_bf16 v[156:159], a[156:159], a[84:87], v[156:159]\n"
    "ds_read_b64_tr_b16 a[220:221], v37 offset:14080\n"
    "ds_read_b64_tr_b16 a[222:223], v37 offset:14336\n"
    "v_mfma_f32_16x16x32_bf16 v[156:159], a[160:163], a[88:91], v[156:159]\n"
    "v_cvt_pk_bf16_f32 v54, v60, v61\n"
    "v_cvt_pk_bf16_f32 v55, v62, v63\n"
    "v_mfma_f32_16x16x32_bf16 v[156:159], a[164:167], a[92:95], v[156:159]\n"
    "s_add_u32 m0, s48, 10496\n"
    "ds_read_b128 a[80:83], v19 offset:2176\n"
    "v_mfma_f32_16x16x32_bf16 v[156:159], a[168:171], a[96:99], v[156:159]\n"
    "ds_read_b128 a[32:35], v18 offset:4512\n"
    "v_mfma_f32_16x16x32_bf16 v[156:159], a[172:175], a[100:103], v[156:159]\n"
    "ds_read_b128 a[84:87], v19 offset:2720\n"
    "v_mfma_f32_16x16x32_bf16 v[156:159], a[176:179], a[104:107], v[156:159]\n"
    "ds_read_b128 a[36:39], v18 offset:5056\n"
    "v_mfma_f32_16x16x32_bf16 v[156:159], a[180:183], a[108:111], v[156:159]\n"
    "ds_read_b128 a[40:43], v18 offset:5600\n"
    "v_mfma_f32_16x16x32_bf16 v[160:163], a[156:159], a[88:91], v[160:163]\n"
    "buffer_load_dwordx4 a[140:143], v5, s[28:31], s50 offen offset:3072 sc1\n"
    "s_add_u32 s50, s50, 4096\n"
    "v_mfma_f32_16x16x32_bf16 v[160:163], a[160:163], a[92:95], v[160:163]\n"
    "s_waitcnt lgkmcnt(5)\n"
    "v_mfma_f32_32x32x16_bf16 v[64:79], a[220:223], a[228:231], 0\n"
    "ds_write_b128 v15, v[48:51] offset:54528\n"
    "v_mfma_f32_16x16x32_bf16 v[160:163], a[164:167], a[96:99], v[160:163]\n"
    "s_mov_b32 exec_hi, 0xffff\n"
    "buffer_load_dwordx4 v8, s[16:19], s58 offen lds\n"
    "s_mov_b32 exec_hi, -1\n"
    "v_mfma_f32_16x16x32_bf16 v[160:163], a[168:171], a[100:103], v[160:163]\n"
    "v_mfma_f32_16x16x32_bf16 v[160:163], a[172:175], a[104:107], v[160:163]\n"
    "s_add_u32 s58, s51, 688128\n"
    "ds_read_b128 a[88:91], v19 offset:3264\n"
    "s_add_u32 m0, s48, 11264\n"
    "v_mfma_f32_16x16x32_bf16 v[160:163], a[176:179], a[108:111], v[160:163]\n"
    "ds_read_b128 a[44:47], v18 offset:6144\n"
    "buffer_load_dwordx4 v6, s[16:19], s58 offen lds\n"
    "v_mfma_f32_16x16x32_bf16 v[160:163], a[180:183], a[112:115], v[160:163]\n"
    "ds_read_b64_tr_b16 a[224:225], v37 offset:14464\n"
    "ds_read_b64_tr_b16 a[226:227], v37 offset:14720\n"
    "v_mfma_f32_16x16x32_bf16 v[164:167], a[156:159], a[92:95], v[164:167]\n"
    "v_xor_b32 v12, 32, v12\n"
    "v_xor_b32 v13, 32, v13\n"
    "v_mfma_f32_16x16x32_bf16 v[164:167], a[160:163], a[96:99], v[164:167]\n"
    "v_xor_b32 v14, 32, v14\n"
    "v_xor_b32 v31, 32, v31\n"
    "v_mfma_f32_16x16x32_bf16 v[164:167], a[164:167], a[100:103], v[164:167]\n"
    "v_xor_b32 v32, 32, v32\n"
    "v_xor_b32 v37, 32, v37\n"
    "v_mfma_f32_16x16x32_bf16 v[164:167], a[168:171], a[104:107], v[164:167]\n"
    "ds_read_b128 a[92:95], v19 offset:3808\n"
    "v_mfma_f32_16x16x32_bf16 v[164:167], a[172:175], a[108:111], v[164:167]\n"
    "ds_read_b128 a[48:51], v18 offset:6688\n"
    "v_mfma_f32_16x16x32_bf16 v[164:167], a[176:179], a[112:115], v[164:167]\n"
    "ds_read_b128 a[52:55], v18 offset:7232\n"
    "v_mfma_f32_16x16x32_bf16 v[164:167], a[180:183], a[116:119], v[164:167]\n"
    "ds_read_b128 a[56:59], v18 offset:7776\n"
    "v_mfma_f32_16x16x32_bf16 v[168:171], a[156:159], a[96:99], v[168:171]\n"
    "s_waitcnt lgkmcnt(4)\n"
    "buffer_load_dwordx4 a[144:147], v5, s[28:31], s50 offen offset:0 sc1\n"
    "s_add_u32 m0, s48, 12288\n"
    "v_mfma_f32_32x32x16_bf16 v[64:79], a[224:227], a[232:235], v[64:79]\n"
    "ds_write_b128 v15, v[52:55] offset:55072\n"
    "buffer_load_dwordx4 v7, s[16:19], s58 offen lds\n"
    "v_mfma_f32_16x16x32_bf16 v[168:171], a[160:163], a[100:103], v[168:171]\n"
    "v_mfma_f32_16x16x32_bf16 v[168:171], a[164:167], a[104:107], v[168:171]\n"
    "v_mfma_f32_16x16x32_bf16 v[168:171], a[168:171], a[108:111], v[168:171]\n"
    "v_cvt_pk_bf16_f32 v64, v64, v65\n"
    "v_cvt_pk_bf16_f32 v65, v66, v67\n"
    "v_mfma_f32_16x16x32_bf16 v[168:171], a[172:175], a[112:115], v[168:171]\n"
    "v_cvt_pk_bf16_f32 v66, v68, v69\n"
    "v_cvt_pk_bf16_f32 v67, v70, v71\n"
    "v_mfma_f32_16x16x32_bf16 v[168:171], a[176:179], a[116:119], v[168:171]\n"
    "v_cvt_pk_bf16_f32 v68, v72, v73\n"
    "v_cvt_pk_bf16_f32 v69, v74, v75\n"
    "v_mfma_f32_16x16x32_bf16 v[168:171], a[180:183], a[120:123], v[168:171]\n"
    "v_cvt_pk_bf16_f32 v70, v76, v77\n"
    "v_cvt_pk_bf16_f32 v71, v78, v79\n"
    "v_mfma_f32_16x16x32_bf16 v[172:175], a[156:159], a[100:103], v[172:175]\n"
    "s_waitcnt vmcnt(20) lgkmcnt(2)\n"
    "ds_read_b64_tr_b16 a[220:221], v12 offset:0\n"
    "ds_read_b64_tr_b16 a[222:223], v12 offset:256\n"
    "v_mfma_f32_16x16x32_bf16 v[172:175], a[160:163], a[104:107], v[172:175]\n"
    "ds_read_b128 a[96:99], v19 offset:4352\n"
    "v_mfma_f32_16x16x32_bf16 v[172:175], a[164:167], a[108:111], v[172:175]\n"
    "s_add_u32 m0, s48, 13312\n"
    "ds_read_b128 a[60:63], v18 offset:8320\n"
    "v_mfma_f32_16x16x32_bf16 v[172:175], a[168:171], a[112:115], v[172:175]\n"
    "ds_read_b128 a[100:103], v19 offset:4896\n"
    "v_mfma_f32_16x16x32_bf16 v[172:175], a[172:175], a[116:119], v[172:175]\n"
    "s_mov_b32 exec_hi, 0xffff\n"
    "buffer_load_dwordx4 v8, s[16:19], s58 offen lds\n"
    "s_mov_b32 exec_hi, -1\n"
    "v_mfma_f32_16x16x32_bf16 v[172:175], a[176:179], a[120:123], v[172:175]\n"
    "ds_read_b128 a[104:107], v19 offset:5440\n"
    "v_mfma_f32_16x16x32_bf16 v[172:175], a[180:183], a[124:127], v[172:175]\n"
    "s_waitcnt lgkmcnt(4)\n"
    "v_mfma_f32_32x32x16_bf16 v[48:63], a[220:223], a[228:231], 0\n"
    "s_waitcnt vmcnt(18)\n"
    "ds_write_b128 v16, v[84:87] offset:50720\n"
    "v_mfma_f32_16x16x32_bf16 v[176:179], a[184:187], a[0:3], v[176:179]\n"
    "buffer_load_dwordx4 a[148:151], v5, s[28:31], s50 offen offset:1024 sc1\n"
    "v_mfma_f32_16x16x32_bf16 v[176:179], a[188:191], a[4:7], v[176:179]\n"
    "v_mfma_f32_16x16x32_bf16 v[176:179], a[192:195], a[8:11], v[176:179]\n"
    "ds_read_b128 a[108:111], v19 offset:5984\n"
    "v_mfma_f32_16x16x32_bf16 v[176:179], a[196:199], a[12:15], v[176:179]\n"
    "v_mfma_f32_16x16x32_bf16 v[176:179], a[200:203], a[16:19], v[176:179]\n"
    "s_mov_b32 m0, s54\n"
    "v_mfma_f32_16x16x32_bf16 v[176:179], a[204:207], a[20:23], v[176:179]\n"
    "ds_read_b64_tr_b16 a[224:225], v12 offset:384\n"
    "ds_read_b64_tr_b16 a[226:227], v12 offset:640\n"
    "v_mfma_f32_16x16x32_bf16 v[176:179], a[208:211], a[24:27], v[176:179]\n"
    "buffer_load_dwordx4 v6, s[16:19], s57 offen lds\n"
    "v_mfma_f32_16x16x32_bf16 v[180:183], a[184:187], a[4:7], v[180:183]\n"
    "v_mfma_f32_16x16x32_bf16 v[180:183], a[188:191], a[8:11], v[180:183]\n"
    "v_mfma_f32_16x16x32_bf16 v[180:183], a[192:195], a[12:15], v[180:183]\n"
    "v_mfma_f32_16x16x32_bf16 v[180:183], a[196:199], a[16:19], v[180:183]\n"
    "v_mfma_f32_16x16x32_bf16 v[180:183], a[200:203], a[20:23], v[180:183]\n"
    "v_mfma_f32_16x16x32_bf16 v[180:183], a[204:207], a[24:27], v[180:183]\n"
    "v_mfma_f32_16x16x32_bf16 v[180:183], a[208:211], a[28:31], v[180:183]\n"
    "s_waitcnt lgkmcnt(0)\n"
    "v_mfma_f32_32x32x16_bf16 v[48:63], a[224:227], a[232:235], v[48:63]\n"
    "s_mov_b32 m0, s55\n"
    "ds_write_b128 v33, v[64:67] offset:55616\n"
    "v_mfma_f32_16x16x32_bf16 v[184:187], a[184:187], a[8:11], v[184:187]\n"
    "buffer_load_dwordx4 v7, s[16:19], s57 offen lds\n"
    "v_mfma_f32_16x16x32_bf16 v[184:187], a[188:191], a[12:15], v[184:187]\n"
    "buffer_load_dwordx4 a[152:155], v5, s[28:31], s50 offen offset:2048 sc1\n"
    "v_mfma_f32_16x16x32_bf16 v[184:187], a[192:195], a[16:19], v[184:187]\n"
    "v_cvt_pk_bf16_f32 v48, v48, v49\n"
    "v_cvt_pk_bf16_f32 v49, v50, v51\n"
    "v_mfma_f32_16x16x32_bf16 v[184:187], a[196:199], a[20:23], v[184:187]\n"
    "v_cvt_pk_bf16_f32 v50, v52, v53\n"
    "v_cvt_pk_bf16_f32 v51, v54, v55\n"
    "v_mfma_f32_16x16x32_bf16 v[184:187], a[200:203], a[24:27], v[184:187]\n"
    "v_cvt_pk_bf16_f32 v52, v56, v57\n"
    "v_cvt_pk_bf16_f32 v53, v58, v59\n"
    "v_mfma_f32_16x16x32_bf16 v[184:187], a[204:207], a[28:31], v[184:187]\n"
    "v_cvt_pk_bf16_f32 v54, v60, v61\n"
    "v_cvt_pk_bf16_f32 v55, v62, v63\n"
    "v_mfma_f32_16x16x32_bf16 v[184:187], a[208:211], a[32:35], v[184:187]\n"
    "s_waitcnt vmcnt(13)\n"
    "ds_read_b64_tr_b16 a[220:221], v13\n"
    "ds_read_b64_tr_b16 a[222:223], v14\n"
    "v_mfma_f32_16x16x32_bf16 v[188:191], a[184:187], a[12:15], v[188:191]\n"
    "v_mfma_f32_16x16x32_bf16 v[188:191], a[188:191], a[16:19], v[188:191]\n"
    "v_mfma_f32_16x16x32_bf16 v[188:191], a[192:195], a[20:23], v[188:191]\n"
    "s_mov_b32 m0, s56\n"
    "v_mfma_f32_16x16x32_bf16 v[188:191], a[196:199], a[24:27], v[188:191]\n"
    "s_mov_b32 exec_hi, 0xffff\n"
    "buffer_load_dwordx4 v8, s[16:19], s57 offen lds\n"
    "s_mov_b32 exec_hi, -1\n"
    "v_mfma_f32_16x16x32_bf16 v[188:191], a[200:203], a[28:31], v[188:191]\n"
    "s_add_u32 s51, s51, 64\n"
    "s_add_u32 s57, s57, s59\n"
    "v_mfma_f32_16x16x32_bf16 v[188:191], a[204:207], a[32:35], v[188:191]\n"
    "s_waitcnt lgkmcnt(0)\n"
    "v_mfma_f32_32x32x16_bf16 v[80:95], a[220:223], a[236:239], 0\n"
    "ds_write_b128 v33, v[68:71] offset:56160\n"
    "v_mfma_f32_16x16x32_bf16 v[188:191], a[208:211], a[36:39], v[188:191]\n"
    "v_mfma_f32_16x16x32_bf16 v[192:195], a[184:187], a[16:19], v[192:195]\n"
    "v_mfma_f32_16x16x32_bf16 v[192:195], a[188:191], a[20:23], v[192:195]\n"
    "buffer_load_dwordx4 a[156:159], v5, s[28:31], s50 offen offset:3072 sc1\n"
    "s_add_u32 s50, s50, 4096\n"
    "v_mfma_f32_16x16x32_bf16 v[192:195], a[192:195], a[24:27], v[192:195]\n"
    "v_mfma_f32_16x16x32_bf16 v[192:195], a[196:199], a[28:31], v[192:195]\n"
    "v_mfma_f32_16x16x32_bf16 v[192:195], a[200:203], a[32:35], v[192:195]\n"
    "ds_read_b64_tr_b16 a[224:225], v12 offset:2816\n"
    "ds_read_b64_tr_b16 a[226:227], v12 offset:3072\n"
    "v_mfma_f32_16x16x32_bf16 v[192:195], a[204:207], a[36:39], v[192:195]\n"
    "v_mfma_f32_16x16x32_bf16 v[192:195], a[208:211], a[40:43], v[192:195]\n"
    "v_mfma_f32_16x16x32_bf16 v[196:199], a[184:187], a[20:23], v[196:199]\n"
    "v_mfma_f32_16x16x32_bf16 v[196:199], a[188:191], a[24:27], v[196:199]\n"
    "v_mfma_f32_16x16x32_bf16 v[196:199], a[192:195], a[28:31], v[196:199]\n"
    "v_mfma_f32_16x16x32_bf16 v[196:199], a[196:199], a[32:35], v[196:199]\n"
    "v_mfma_f32_16x16x32_bf16 v[196:199], a[200:203], a[36:39], v[196:199]\n"
    "v_mfma_f32_16x16x32_bf16 v[196:199], a[204:207], a[40:43], v[196:199]\n"
    "s_waitcnt lgkmcnt(0)\n"
    "v_mfma_f32_32x32x16_bf16 v[64:79], a[224:227], a[228:231], 0\n"
    "s_waitcnt lgkmcnt(0)\n"
    "s_barrier\n"
    "v_mfma_f32_16x16x32_bf16 v[196:199], a[208:211], a[44:47], v[196:199]\n"
    "ds_write_b128 v15, v[48:51] offset:0\n"
    "v_mfma_f32_16x16x32_bf16 v[200:203], a[184:187], a[24:27], v[200:203]\n"
    "v_mfma_f32_16x16x32_bf16 v[200:203], a[188:191], a[28:31], v[200:203]\n"
    "buffer_load_dwordx4 a[160:163], v5, s[28:31], s50 offen offset:0 sc1\n"
    "v_mfma_f32_16x16x32_bf16 v[200:203], a[192:195], a[32:35], v[200:203]\n"
    "ds_read_b128 a[0:3], v18 offset:50176\n"
    "v_mfma_f32_16x16x32_bf16 v[200:203], a[196:199], a[36:39], v[200:203]\n"
    "v_mfma_f32_16x16x32_bf16 v[200:203], a[200:203], a[40:43], v[200:203]\n"
    "ds_read_b128 a[4:7], v18 offset:50720\n"
    "v_mfma_f32_16x16x32_bf16 v[200:203], a[204:207], a[44:47], v[200:203]\n"
    "v_mfma_f32_16x16x32_bf16 v[200:203], a[208:211], a[48:51], v[200:203]\n"
    "ds_read_b128 a[8:11], v18 offset:51264\n"
    "v_mfma_f32_16x16x32_bf16 v[204:207], a[184:187], a[28:31], v[204:207]\n"
    "v_mfma_f32_16x16x32_bf16 v[204:207], a[188:191], a[32:35], v[204:207]\n"
    "ds_read_b128 a[12:15], v18 offset:51808\n"
    "v_mfma_f32_16x16x32_bf16 v[204:207], a[192:195], a[36:39], v[204:207]\n"
    "v_mfma_f32_16x16x32_bf16 v[204:207], a[196:199], a[40:43], v[204:207]\n"
    "ds_read_b128 a[16:19], v18 offset:52352\n"
    "v_mfma_f32_16x16x32_bf16 v[204:207], a[200:203], a[44:47], v[204:207]\n"
    "v_mfma_f32_16x16x32_bf16 v[204:207], a[204:207], a[48:51], v[204:207]\n"
    "ds_read_b128 a[20:23], v18 offset:52896\n"
    "v_mfma_f32_16x16x32_bf16 v[204:207], a[208:211], a[52:55], v[204:207]\n"
    "v_mfma_f32_16x16x32_bf16 v[208:211], a[184:187], a[32:35], v[208:211]\n"
    "ds_read_b128 a[24:27], v18 offset:53440\n"
    "v_mfma_f32_16x16x32_bf16 v[208:211], a[188:191], a[36:39], v[208:211]\n"
    "buffer_load_dwordx4 a[164:167], v5, s[28:31], s50 offen offset:1024 sc1\n"
    "v_mfma_f32_16x16x32_bf16 v[208:211], a[192:195], a[40:43], v[208:211]\n"
    "ds_read_b128 a[28:31], v18 offset:53984\n"
    "v_mfma_f32_16x16x32_bf16 v[208:211], a[196:199], a[44:47], v[208:211]\n"
    "v_mfma_f32_16x16x32_bf16 v[208:211], a[200:203], a[48:51], v[208:211]\n"
    "ds_read_b128 a[32:35], v18 offset:54528\n"
    "v_mfma_f32_16x16x32_bf16 v[208:211], a[204:207], a[52:55], v[208:211]\n"
    "v_mfma_f32_16x16x32_bf16 v[208:211], a[208:211], a[56:59], v[208:211]\n"
    "v_mfma_f32_16x16x32_bf16 v[212:215], a[184:187], a[36:39], v[212:215]\n"
    "v_mfma_f32_16x16x32_bf16 v[212:215], a[188:191], a[40:43], v[212:215]\n"
    "ds_read_b128 a[36:39], v18 offset:55072\n"
    "v_mfma_f32_16x16x32_bf16 v[212:215], a[192:195], a[44:47], v[212:215]\n"
    "v_mfma_f32_16x16x32_bf16 v[212:215], a[196:199], a[48:51], v[212:215]\n"
    "ds_read_b128 a[40:43], v18 offset:55616\n"
    "v_mfma_f32_16x16x32_bf16 v[212:215], a[200:203], a[52:55], v[212:215]\n"
    "v_mfma_f32_16x16x32_bf16 v[212:215], a[204:207], a[56:59], v[212:215]\n"
    "ds_read_b128 a[44:47], v18 offset:56160\n"
    "v_mfma_f32_16x16x32_bf16 v[212:215], a[208:211], a[60:63], v[212:215]\n"
    "v_mfma_f32_16x16x32_bf16 v[216:219], a[212:215], a[64:67], v[216:219]\n"
    "ds_read_b128 a[48:51], v18 offset:56704\n"
    "v_mfma_f32_16x16x32_bf16 v[216:219], a[216:219], a[72:75], v[216:219]\n"
    "buffer_load_dwordx4 a[168:171], v5, s[28:31], s50 offen offset:2048 sc1\n"
    "v_mfma_f32_16x16x32_bf16 v[220:223], a[212:215], a[68:71], v[220:223]\n"
    "ds_read_b128 a[52:55], v18 offset:57248\n"
    "v_mfma_f32_16x16x32_bf16 v[220:223], a[216:219], a[76:79], v[220:223]\n"
    "v_mfma_f32_16x16x32_bf16 v[224:227], a[212:215], a[72:75], v[224:227]\n"
    "ds_read_b128 a[56:59], v18 offset:57792\n"
    "v_mfma_f32_16x16x32_bf16 v[224:227], a[216:219], a[80:83], v[224:227]\n"
    "v_mfma_f32_16x16x32_bf16 v[228:231], a[212:215], a[76:79], v[228:231]\n"
    "ds_read_b128 a[60:63], v18 offset:58336\n"
    "v_mfma_f32_16x16x32_bf16 v[228:231], a[216:219], a[84:87], v[228:231]\n"
    "v_mfma_f32_16x16x32_bf16 v[232:235], a[212:215], a[80:83], v[232:235]\n"
    "v_mfma_f32_16x16x32_bf16 v[232:235], a[216:219], a[88:91], v[232:235]\n"
    "v_mfma_f32_16x16x32_bf16 v[236:239], a[212:215], a[84:87], v[236:239]\n"
    "v_mfma_f32_16x16x32_bf16 v[236:239], a[216:219], a[92:95], v[236:239]\n"
    "v_mfma_f32_16x16x32_bf16 v[240:243], a[212:215], a[88:91], v[240:243]\n"
    "v_mfma_f32_16x16x32_bf16 v[240:243], a[216:219], a[96:99], v[240:243]\n"
    "v_mfma_f32_16x16x32_bf16 v[244:247], a[212:215], a[92:95], v[244:247]\n"
    "v_mfma_f32_16x16x32_bf16 v[244:247], a[216:219], a[100:103], v[244:247]\n"
    "v_mfma_f32_16x16x32_bf16 v[248:251], a[212:215], a[96:99], v[248:251]\n"
    "v_mfma_f32_16x16x32_bf16 v[248:251], a[216:219], a[104:107], v[248:251]\n"
    "v_mfma_f32_16x16x32_bf16 v[252:255], a[212:215], a[100:103], v[252:255]\n"
    "v_mfma_f32_16x16x32_bf16 v[252:255], a[216:219], a[108:111], v[252:255]\n"
    "s_waitcnt vmcnt(5) lgkmcnt(0)\n"
    "s_mov_b32 s52, 1\n"
    "s_branch 21b\n"
    "22:\n"
    "v_mfma_f32_16x16x32_bf16 v[96:99], a[128:131], a[0:3], v[96:99]\n"
    "ds_read_b64_tr_b16 a[220:221], v12 offset:3200\n"
    "ds_read_b64_tr_b16 a[222:223], v12 offset:3456\n"
    "v_mfma_f32_16x16x32_bf16 v[96:99], a[132:135], a[4:7], v[96:99]\n"
    "buffer_load_dwordx4 a[192:195], v5, s[28:31], s50 offen offset:0 sc1\n"
    "ds_read_b128 a[64:67], v18 offset:80\n"
    "v_mfma_f32_16x16x32_bf16 v[96:99], a[136:139], a[8:11], v[96:99]\n"
    "v_mfma_f32_16x16x32_bf16 v[96:99], a[140:143], a[12:15], v[96:99]\n"
    "v_mfma_f32_16x16x32_bf16 v[96:99], a[144:147], a[16:19], v[96:99]\n"
    "ds_read_b128 a[68:71], v18 offset:624\n"
    "v_mfma_f32_16x16x32_bf16 v[96:99], a[148:151], a[20:23], v[96:99]\n"
    "v_mfma_f32_16x16x32_bf16 v[96:99], a[152:155], a[24:27], v[96:99]\n"
    "s_waitcnt lgkmcnt(2)\n"
    "v_mfma_f32_32x32x16_bf16 v[64:79], a[220:223], a[232:235], v[64:79]\n"
    "ds_write_b128 v15, v[52:55] offset:50720\n"
    "v_mfma_f32_16x16x32_bf16 v[100:103], a[128:131], a[4:7], v[100:103]\n"
    "v_mfma_f32_16x16x32_bf16 v[100:103], a[132:135], a[8:11], v[100:103]\n"
    "v_mfma_f32_16x16x32_bf16 v[100:103], a[136:139], a[12:15], v[100:103]\n"
    "v_cvt_pk_bf16_f32 v64, v64, v65\n"
    "v_cvt_pk_bf16_f32 v65, v66, v67\n"
    "v_mfma_f32_16x16x32_bf16 v[100:103], a[140:143], a[16:19], v[100:103]\n"
    "v_cvt_pk_bf16_f32 v66, v68, v69\n"
    "v_cvt_pk_bf16_f32 v67, v70, v71\n"
    "v_mfma_f32_16x16x32_bf16 v[100:103], a[144:147], a[20:23], v[100:103]\n"
    "v_cvt_pk_bf16_f32 v68, v72, v73\n"
    "v_cvt_pk_bf16_f32 v69, v74, v75\n"
    "v_mfma_f32_16x16x32_bf16 v[100:103], a[148:151], a[24:27], v[100:103]\n"
    "v_cvt_pk_bf16_f32 v70, v76, v77\n"
    "v_cvt_pk_bf16_f32 v71, v78, v79\n"
    "v_mfma_f32_16x16x32_bf16 v[100:103], a[152:155], a[28:31], v[100:103]\n"
    "buffer_load_dwordx4 a[196:199], v5, s[28:31], s50 offen offset:1024 sc1\n"
    "ds_read_b128 a[72:75], v18 offset:1168\n"
    "v_mfma_f32_16x16x32_bf16 v[104:107], a[128:131], a[8:11], v[104:107]\n"
    "ds_read_b64_tr_b16 a[224:225], v12 offset:5632\n"
    "ds_read_b64_tr_b16 a[226:227], v12 offset:5888\n"
    "v_mfma_f32_16x16x32_bf16 v[104:107], a[132:135], a[12:15], v[104:107]\n"
    "ds_read_b128 a[76:79], v18 offset:1712\n"
    "v_mfma_f32_16x16x32_bf16 v[104:107], a[136:139], a[16:19], v[104:107]\n"
    "ds_read_b128 a[80:83], v18 offset:2256\n"
    "v_mfma_f32_16x16x32_bf16 v[104:107], a[140:143], a[20:23], v[104:107]\n"
    "v_mfma_f32_16x16x32_bf16 v[104:107], a[144:147], a[24:27], v[104:107]\n"
    "buffer_load_dwordx4 a[200:203], v5, s[28:31], s50 offen offset:2048 sc1\n"
    "v_mfma_f32_16x16x32_bf16 v[104:107], a[148:151], a[28:31], v[104:107]\n"
    "ds_read_b128 a[84:87], v18 offset:2800\n"
    "v_mfma_f32_16x16x32_bf16 v[104:107], a[152:155], a[32:35], v[104:107]\n"
    "v_mfma_f32_16x16x32_bf16 v[108:111], a[128:131], a[12:15], v[108:111]\n"
    "v_mfma_f32_16x16x32_bf16 v[108:111], a[132:135], a[16:19], v[108:111]\n"
    "s_waitcnt lgkmcnt(3)\n"
    "v_mfma_f32_32x32x16_bf16 v[48:63], a[224:227], a[228:231], 0\n"
    "ds_write_b128 v15, v[64:67] offset:51264\n"
    "v_mfma_f32_16x16x32_bf16 v[108:111], a[136:139], a[20:23], v[108:111]\n"
    "v_mfma_f32_16x16x32_bf16 v[108:111], a[140:143], a[24:27], v[108:111]\n"
    "v_mfma_f32_16x16x32_bf16 v[108:111], a[144:147], a[28:31], v[108:111]\n"
    "ds_read_b128 a[88:91], v18 offset:3344\n"
    "v_mfma_f32_16x16x32_bf16 v[108:111], a[148:151], a[32:35], v[108:111]\n"
    "v_mfma_f32_16x16x32_bf16 v[108:111], a[152:155], a[36:39], v[108:111]\n"
    "buffer_load_dwordx4 a[204:207], v5, s[28:31], s50 offen offset:3072 sc1\n"
    "s_add_u32 s50, s50, 4096\n"
    "v_mfma_f32_16x16x32_bf16 v[112:115], a[128:131], a[16:19], v[112:115]\n"
    "ds_read_b128 a[92:95], v18 offset:3888\n"
    "v_mfma_f32_16x16x32_bf16 v[112:115], a[132:135], a[20:23], v[112:115]\n"
    "v_mfma_f32_16x16x32_bf16 v[112:115], a[136:139], a[24:27], v[112:115]\n"
    "v_mfma_f32_16x16x32_bf16 v[112:115], a[140:143], a[28:31], v[112:115]\n"
    "ds_read_b64_tr_b16 a[220:221], v12 offset:6016\n"
    "ds_read_b64_tr_b16 a[222:223], v12 offset:6272\n"
    "v_mfma_f32_16x16x32_bf16 v[112:115], a[144:147], a[32:35], v[112:115]\n"
    "ds_read_b128 a[96:99], v18 offset:4432\n"
    "v_mfma_f32_16x16x32_bf16 v[112:115], a[148:151], a[36:39], v[112:115]\n"
    "v_mfma_f32_16x16x32_bf16 v[112:115], a[152:155], a[40:43], v[112:115]\n"
    "ds_read_b128 a[100:103], v18 offset:4976\n"
    "v_mfma_f32_16x16x32_bf16 v[116:119], a[128:131], a[20:23], v[116:119]\n"
    "v_mfma_f32_16x16x32_bf16 v[116:119], a[132:135], a[24:27], v[116:119]\n"
    "buffer_load_dwordx4 a[208:211], v5, s[28:31], s50 offen offset:0 sc1\n"
    "v_mfma_f32_16x16x32_bf16 v[116:119], a[136:139], a[28:31], v[116:119]\n"
    "v_mfma_f32_16x16x32_bf16 v[116:119], a[140:143], a[32:35], v[116:119]\n"
    "ds_read_b128 a[104:107], v18 offset:5520\n"
    "v_mfma_f32_16x16x32_bf16 v[116:119], a[144:147], a[36:39], v[116:119]\n"
    "s_waitcnt lgkmcnt(3)\n"
    "v_mfma_f32_32x32x16_bf16 v[48:63], a[220:223], a[232:235], v[48:63]\n"
    "ds_write_b128 v15, v[68:71] offset:51808\n"
    "v_mfma_f32_16x16x32_bf16 v[116:119], a[148:151], a[40:43], v[116:119]\n"
    "v_mfma_f32_16x16x32_bf16 v[116:119], a[152:155], a[44:47], v[116:119]\n"
    "v_mfma_f32_16x16x32_bf16 v[120:123], a[128:131], a[24:27], v[120:123]\n"
    "v_cvt_pk_bf16_f32 v48, v48, v49\n"
    "v_cvt_pk_bf16_f32 v49, v50, v51\n"
    "v_mfma_f32_16x16x32_bf16 v[120:123], a[132:135], a[28:31], v[120:123]\n"
    "v_cvt_pk_bf16_f32 v50, v52, v53\n"
    "v_cvt_pk_bf16_f32 v51, v54, v55\n"
    "v_mfma_f32_16x16x32_bf16 v[120:123], a[136:139], a[32:35], v[120:123]\n"
    "v_cvt_pk_bf16_f32 v52, v56, v57\n"
    "v_cvt_pk_bf16_f32 v53, v58, v59\n"
    "v_mfma_f32_16x16x32_bf16 v[120:123], a[140:143], a[36:39], v[120:123]\n"
    "v_cvt_pk_bf16_f32 v54, v60, v61\n"
    "v_cvt_pk_bf16_f32 v55, v62, v63\n"
    "v_mfma_f32_16x16x32_bf16 v[120:123], a[144:147], a[40:43], v[120:123]\n"
    "buffer_load_dwordx4 a[212:215], v5, s[28:31], s50 offen offset:1024 sc1\n"
    "ds_read_b128 a[108:111], v18 offset:6064\n"
    "v_mfma_f32_16x16x32_bf16 v[120:123], a[148:151], a[44:47], v[120:123]\n"
    "ds_read_b128 a[112:115], v18 offset:6608\n"
    "v_mfma_f32_16x16x32_bf16 v[120:123], a[152:155], a[48:51], v[120:123]\n"
    "ds_read_b64_tr_b16 a[224:225], v31\n"
    "ds_read_b64_tr_b16 a[226:227], v32\n"
    "v_mfma_f32_16x16x32_bf16 v[124:127], a[128:131], a[28:31], v[124:127]\n"
    "ds_read_b128 a[116:119], v18 offset:7152\n"
    "v_mfma_f32_16x16x32_bf16 v[124:127], a[132:135], a[32:35], v[124:127]\n"
    "v_mfma_f32_16x16x32_bf16 v[124:127], a[136:139], a[36:39], v[124:127]\n"
    "v_mfma_f32_16x16x32_bf16 v[124:127], a[140:143], a[40:43], v[124:127]\n"
    "v_mfma_f32_16x16x32_bf16 v[124:127], a[144:147], a[44:47], v[124:127]\n"
    "ds_read_b128 a[120:123], v18 offset:7696\n"
    "v_mfma_f32_16x16x32_bf16 v[124:127], a[148:151], a[48:51], v[124:127]\n"
    "buffer_load_dwordx4 a[216:219], v5, s[28:31], s50 offen offset:2048 sc1\n"
    "s_add_u32 s50, s50, 3072\n"
    "v_mfma_f32_16x16x32_bf16 v[124:127], a[152:155], a[52:55], v[124:127]\n"
    "v_mfma_f32_16x16x32_bf16 v[128:131], a[128:131], a[32:35], v[128:131]\n"
    "s_waitcnt lgkmcnt(2)\n"
    "v_mfma_f32_32x32x16_bf16 v[80:95], a[224:227], a[240:243], v[80:95]\n"
    "ds_write_b128 v15, v[48:51] offset:52352\n"
    "v_mfma_f32_16x16x32_bf16 v[128:131], a[132:135], a[36:39], v[128:131]\n"
    "v_mfma_f32_16x16x32_bf16 v[128:131], a[136:139], a[40:43], v[128:131]\n"
    "v_mfma_f32_16x16x32_bf16 v[128:131], a[140:143], a[44:47], v[128:131]\n"
    "v_cvt_pk_bf16_f32 v80, v80, v81\n"
    "v_cvt_pk_bf16_f32 v81, v82, v83\n"
    "v_mfma_f32_16x16x32_bf16 v[128:131], a[144:147], a[48:51], v[128:131]\n"
    "v_cvt_pk_bf16_f32 v82, v84, v85\n"
    "v_cvt_pk_bf16_f32 v83, v86, v87\n"
    "v_mfma_f32_16x16x32_bf16 v[128:131], a[148:151], a[52:55], v[128:131]\n"
    "v_cvt_pk_bf16_f32 v84, v88, v89\n"
    "v_cvt_pk_bf16_f32 v85, v90, v91\n"
    "v_mfma_f32_16x16x32_bf16 v[128:131], a[152:155], a[56:59], v[128:131]\n"
    "v_cvt_pk_bf16_f32 v86, v92, v93\n"
    "v_cvt_pk_bf16_f32 v87, v94, v95\n"
    "v_mfma_f32_16x16x32_bf16 v[132:135], a[128:131], a[36:39], v[132:135]\n"
    "ds_read_b128 a[124:127], v18 offset:8240\n"
    "v_mfma_f32_16x16x32_bf16 v[132:135], a[132:135], a[40:43], v[132:135]\n"
    "v_mfma_f32_16x16x32_bf16 v[132:135], a[136:139], a[44:47], v[132:135]\n"
    "ds_read_b64_tr_b16 a[220:221], v12 offset:8448\n"
    "ds_read_b64_tr_b16 a[222:223], v12 offset:8704\n"
    "v_mfma_f32_16x16x32_bf16 v[132:135], a[140:143], a[48:51], v[132:135]\n"
    "v_mfma_f32_16x16x32_bf16 v[132:135], a[144:147], a[52:55], v[132:135]\n"
    "v_mfma_f32_16x16x32_bf16 v[132:135], a[148:151], a[56:59], v[132:135]\n"
    "v_mfma_f32_16x16x32_bf16 v[132:135], a[152:155], a[60:63], v[132:135]\n"
    "s_waitcnt vmcnt(9) lgkmcnt(3)\n"
    "buffer_load_dwordx4 a[128:131], v5, s[28:31], s50 offen offset:0 sc1\n"
    "v_mfma_f32_16x16x32_bf16 v[136:139], a[156:159], a[64:67], v[136:139]\n"
    "v_mfma_f32_16x16x32_bf16 v[136:139], a[160:163], a[68:71], v[136:139]\n"
    "ds_read_b128 a[64:67], v19 offset:0\n"
    "v_mfma_f32_16x16x32_bf16 v[136:139], a[164:167], a[72:75], v[136:139]\n"
    "ds_read_b128 a[0:3], v18 offset:160\n"
    "v_mfma_f32_16x16x32_bf16 v[136:139], a[168:171], a[76:79], v[136:139]\n"
    "s_waitcnt lgkmcnt(2)\n"
    "v_mfma_f32_32x32x16_bf16 v[64:79], a[220:223], a[228:231], 0\n"
    "ds_write_b128 v15, v[52:55] offset:52896\n"
    "v_mfma_f32_16x16x32_bf16 v[136:139], a[172:175], a[80:83], v[136:139]\n"
    "v_mfma_f32_16x16x32_bf16 v[136:139], a[176:179], a[84:87], v[136:139]\n"
    "v_mfma_f32_16x16x32_bf16 v[136:139], a[180:183], a[88:91], v[136:139]\n"
    "ds_read_b128 a[4:7], v18 offset:704\n"
    "v_mfma_f32_16x16x32_bf16 v[140:143], a[156:159], a[68:71], v[140:143]\n"
    "v_mfma_f32_16x16x32_bf16 v[140:143], a[160:163], a[72:75], v[140:143]\n"
    "ds_read_b128 a[68:71], v19 offset:544\n"
    "v_mfma_f32_16x16x32_bf16 v[140:143], a[164:167], a[76:79], v[140:143]\n"
    "buffer_load_dwordx4 a[132:135], v5, s[28:31], s50 offen offset:1024 sc1\n"
    "ds_read_b128 a[8:11], v18 offset:1248\n"
    "v_mfma_f32_16x16x32_bf16 v[140:143], a[168:171], a[80:83], v[140:143]\n"
    "v_mfma_f32_16x16x32_bf16 v[140:143], a[172:175], a[84:87], v[140:143]\n"
    "ds_read_b64_tr_b16 a[224:225], v12 offset:8832\n"
    "ds_read_b64_tr_b16 a[226:227], v12 offset:9088\n"
    "v_mfma_f32_16x16x32_bf16 v[140:143], a[176:179], a[88:91], v[140:143]\n"
    "ds_read_b128 a[12:15], v18 offset:1792\n"
    "v_mfma_f32_16x16x32_bf16 v[140:143], a[180:183], a[92:95], v[140:143]\n"
    "v_mfma_f32_16x16x32_bf16 v[144:147], a[156:159], a[72:75], v[144:147]\n"
    "v_mfma_f32_16x16x32_bf16 v[144:147], a[160:163], a[76:79], v[144:147]\n"
    "ds_read_b128 a[72:75], v19 offset:1088\n"
    "v_mfma_f32_16x16x32_bf16 v[144:147], a[164:167], a[80:83], v[144:147]\n"
    "ds_read_b128 a[16:19], v18 offset:2336\n"
    "v_mfma_f32_16x16x32_bf16 v[144:147], a[168:171], a[84:87], v[144:147]\n"
    "v_mfma_f32_16x16x32_bf16 v[144:147], a[172:175], a[88:91], v[144:147]\n"
    "v_mfma_f32_16x16x32_bf16 v[144:147], a[176:179], a[92:95], v[144:147]\n"
    "s_waitcnt lgkmcnt(3)\n"
    "buffer_load_dwordx4 a[136:139], v5, s[28:31], s50 offen offset:2048 sc1\n"
    "v_mfma_f32_32x32x16_bf16 v[64:79], a[224:227], a[232:235], v[64:79]\n"
    "ds_write_b128 v16, v[80:83] offset:50176\n"
    "v_mfma_f32_16x16x32_bf16 v[144:147], a[180:183], a[96:99], v[144:147]\n"
    "v_mfma_f32_16x16x32_bf16 v[148:151], a[156:159], a[76:79], v[148:151]\n"
    "v_mfma_f32_16x16x32_bf16 v[148:151], a[160:163], a[80:83], v[148:151]\n"
    "v_cvt_pk_bf16_f32 v64, v64, v65\n"
    "v_cvt_pk_bf16_f32 v65, v66, v67\n"
    "v_mfma_f32_16x16x32_bf16 v[148:151], a[164:167], a[84:87], v[148:151]\n"
    "v_cvt_pk_bf16_f32 v66, v68, v69\n"
    "v_cvt_pk_bf16_f32 v67, v70, v71\n"
    "v_mfma_f32_16x16x32_bf16 v[148:151], a[168:171], a[88:91], v[148:151]\n"
    "v_cvt_pk_bf16_f32 v68, v72, v73\n"
    "v_cvt_pk_bf16_f32 v69, v74, v75\n"
    "v_mfma_f32_16x16x32_bf16 v[148:151], a[172:175], a[92:95], v[148:151]\n"
    "v_cvt_pk_bf16_f32 v70, v76, v77\n"
    "v_cvt_pk_bf16_f32 v71, v78, v79\n"
    "v_mfma_f32_16x16x32_bf16 v[148:151], a[176:179], a[96:99], v[148:151]\n"
    "ds_read_b128 a[76:79], v19 offset:1632\n"
    "v_mfma_f32_16x16x32_bf16 v[148:151], a[180:183], a[100:103], v[148:151]\n"
    "ds_read_b128 a[20:23], v18 offset:2880\n"
    "v_mfma_f32_16x16x32_bf16 v[152:155], a[156:159], a[80:83], v[152:155]\n"
    "ds_read_b64_tr_b16 a[220:221], v12 offset:11264\n"
    "ds_read_b64_tr_b16 a[222:223], v12 offset:11520\n"
    "v_mfma_f32_16x16x32_bf16 v[152:155], a[160:163], a[84:87], v[152:155]\n"
    "buffer_load_dwordx4 a[140:143], v5, s[28:31], s50 offen offset:3072 sc1\n"
    "s_add_u32 s50, s50, 4096\n"
    "v_mfma_f32_16x16x32_bf16 v[152:155], a[164:167], a[88:91], v[152:155]\n"
    "ds_read_b128 a[80:83], v19 offset:2176\n"
    "v_mfma_f32_16x16x32_bf16 v[152:155], a[168:171], a[92:95], v[152:155]\n"
    "ds_read_b128 a[24:27], v18 offset:3424\n"
    "v_mfma_f32_16x16x32_bf16 v[152:155], a[172:175], a[96:99], v[152:155]\n"
    "ds_read_b128 a[28:31], v18 offset:3968\n"
    "v_mfma_f32_16x16x32_bf16 v[152:155], a[176:179], a[100:103], v[152:155]\n"
    "ds_read_b128 a[32:35], v18 offset:4512\n"
    "v_mfma_f32_16x16x32_bf16 v[152:155], a[180:183], a[104:107], v[152:155]\n"
    "ds_read_b128 a[36:39], v18 offset:5056\n"
    "v_mfma_f32_16x16x32_bf16 v[156:159], a[156:159], a[84:87], v[156:159]\n"
    "v_mfma_f32_16x16x32_bf16 v[156:159], a[160:163], a[88:91], v[156:159]\n"
    "s_waitcnt lgkmcnt(5)\n"
    "v_mfma_f32_32x32x16_bf16 v[48:63], a[220:223], a[228:231], 0\n"
    "ds_write_b128 v15, v[64:67] offset:53440\n"
    "v_mfma_f32_16x16x32_bf16 v[156:159], a[164:167], a[92:95], v[156:159]\n"
    "v_mfma_f32_16x16x32_bf16 v[156:159], a[168:171], a[96:99], v[156:159]\n"
    "v_mfma_f32_16x16x32_bf16 v[156:159], a[172:175], a[100:103], v[156:159]\n"
    "buffer_load_dwordx4 a[144:147], v5, s[28:31], s50 offen offset:0 sc1\n"
    "ds_read_b128 a[84:87], v19 offset:2720\n"
    "v_mfma_f32_16x16x32_bf16 v[156:159], a[176:179], a[104:107], v[156:159]\n"
    "ds_read_b128 a[40:43], v18 offset:5600\n"
    "v_mfma_f32_16x16x32_bf16 v[156:159], a[180:183], a[108:111], v[156:159]\n"
    "v_mfma_f32_16x16x32_bf16 v[160:163], a[156:159], a[88:91], v[160:163]\n"
    "ds_read_b128 a[44:47], v18 offset:6144\n"
    "v_mfma_f32_16x16x32_bf16 v[160:163], a[160:163], a[92:95], v[160:163]\n"
    "ds_read_b128 a[88:91], v19 offset:3264\n"
    "v_mfma_f32_16x16x32_bf16 v[160:163], a[164:167], a[96:99], v[160:163]\n"
    "v_mfma_f32_16x16x32_bf16 v[160:163], a[168:171], a[100:103], v[160:163]\n"
    "ds_read_b64_tr_b16 a[224:225], v12 offset:11648\n"
    "ds_read_b64_tr_b16 a[226:227], v12 offset:11904\n"
    "v_mfma_f32_16x16x32_bf16 v[160:163], a[172:175], a[104:107], v[160:163]\n"
    "ds_read_b128 a[48:51], v18 offset:6688\n"
    "v_mfma_f32_16x16x32_bf16 v[160:163], a[176:179], a[108:111], v[160:163]\n"
    "v_mfma_f32_16x16x32_bf16 v[160:163], a[180:183], a[112:115], v[160:163]\n"
    "v_mfma_f32_16x16x32_bf16 v[164:167], a[156:159], a[92:95], v[164:167]\n"
    "buffer_load_dwordx4 a[148:151], v5, s[28:31], s50 offen offset:1024 sc1\n"
    "ds_read_b128 a[52:55], v18 offset:7232\n"
    "v_mfma_f32_16x16x32_bf16 v[164:167], a[160:163], a[96:99], v[164:167]\n"
    "ds_read_b128 a[92:95], v19 offset:3808\n"
    "v_mfma_f32_16x16x32_bf16 v[164:167], a[164:167], a[100:103], v[164:167]\n"
    "v_mfma_f32_16x16x32_bf16 v[164:167], a[168:171], a[104:107], v[164:167]\n"
    "v_mfma_f32_16x16x32_bf16 v[164:167], a[172:175], a[108:111], v[164:167]\n"
    "s_waitcnt lgkmcnt(3)\n"
    "v_mfma_f32_32x32x16_bf16 v[48:63], a[224:227], a[232:235], v[48:63]\n"
    "ds_write_b128 v15, v[68:71] offset:53984\n"
    "v_mfma_f32_16x16x32_bf16 v[164:167], a[176:179], a[112:115], v[164:167]\n"
    "v_mfma_f32_16x16x32_bf16 v[164:167], a[180:183], a[116:119], v[164:167]\n"
    "v_mfma_f32_16x16x32_bf16 v[168:171], a[156:159], a[96:99], v[168:171]\n"
    "v_cvt_pk_bf16_f32 v48, v48, v49\n"
    "v_cvt_pk_bf16_f32 v49, v50, v51\n"
    "v_mfma_f32_16x16x32_bf16 v[168:171], a[160:163], a[100:103], v[168:171]\n"
    "v_cvt_pk_bf16_f32 v50, v52, v53\n"
    "v_cvt_pk_bf16_f32 v51, v54, v55\n"
    "v_mfma_f32_16x16x32_bf16 v[168:171], a[164:167], a[104:107], v[168:171]\n"
    "v_cvt_pk_bf16_f32 v52, v56, v57\n"
    "v_cvt_pk_bf16_f32 v53, v58, v59\n"
    "v_mfma_f32_16x16x32_bf16 v[168:171], a[168:171], a[108:111], v[168:171]\n"
    "v_cvt_pk_bf16_f32 v54, v60, v61\n"
    "v_cvt_pk_bf16_f32 v55, v62, v63\n"
    "v_mfma_f32_16x16x32_bf16 v[168:171], a[172:175], a[112:115], v[168:171]\n"
    "buffer_load_dwordx4 a[152:155], v5, s[28:31], s50 offen offset:2048 sc1\n"
    "ds_read_b128 a[96:99], v19 offset:4352\n"
    "v_mfma_f32_16x16x32_bf16 v[168:171], a[176:179], a[116:119], v[168:171]\n"
    "ds_read_b128 a[56:59], v18 offset:7776\n"
    "v_mfma_f32_16x16x32_bf16 v[168:171], a[180:183], a[120:123], v[168:171]\n"
    "ds_read_b64_tr_b16 a[220:221], v37 offset:14080\n"
    "ds_read_b64_tr_b16 a[222:223], v37 offset:14336\n"
    "v_mfma_f32_16x16x32_bf16 v[172:175], a[156:159], a[100:103], v[172:175]\n"
    "ds_read_b128 a[60:63], v18 offset:8320\n"
    "v_mfma_f32_16x16x32_bf16 v[172:175], a[160:163], a[104:107], v[172:175]\n"
    "ds_read_b128 a[100:103], v19 offset:4896\n"
    "v_mfma_f32_16x16x32_bf16 v[172:175], a[164:167], a[108:111], v[172:175]\n"
    "v_mfma_f32_16x16x32_bf16 v[172:175], a[168:171], a[112:115], v[172:175]\n"
    "ds_read_b128 a[104:107], v19 offset:5440\n"
    "v_mfma_f32_16x16x32_bf16 v[172:175], a[172:175], a[116:119], v[172:175]\n"
    "v_mfma_f32_16x16x32_bf16 v[172:175], a[176:179], a[120:123], v[172:175]\n"
    "ds_read_b128 a[108:111], v19 offset:5984\n"
    "v_mfma_f32_16x16x32_bf16 v[172:175], a[180:183], a[124:127], v[172:175]\n"
    "s_waitcnt vmcnt(9) lgkmcnt(8)\n"
    "buffer_load_dwordx4 a[156:159], v5, s[28:31], s50 offen offset:3072 sc1\n"
    "s_add_u32 s50, s50, 4096\n"
    "v_mfma_f32_16x16x32_bf16 v[176:179], a[184:187], a[0:3], v[176:179]\n"
    "s_waitcnt lgkmcnt(4)\n"
    "v_mfma_f32_32x32x16_bf16 v[64:79], a[220:223], a[228:231], 0\n"
    "ds_write_b128 v15, v[48:51] offset:54528\n"
    "v_mfma_f32_16x16x32_bf16 v[176:179], a[188:191], a[4:7], v[176:179]\n"
    "v_mfma_f32_16x16x32_bf16 v[176:179], a[192:195], a[8:11], v[176:179]\n"
    "v_mfma_f32_16x16x32_bf16 v[176:179], a[196:199], a[12:15], v[176:179]\n"
    "v_mfma_f32_16x16x32_bf16 v[176:179], a[200:203], a[16:19], v[176:179]\n"
    "v_mfma_f32_16x16x32_bf16 v[176:179], a[204:207], a[20:23], v[176:179]\n"
    "v_mfma_f32_16x16x32_bf16 v[176:179], a[208:211], a[24:27], v[176:179]\n"
    "v_mfma_f32_16x16x32_bf16 v[180:183], a[184:187], a[4:7], v[180:183]\n"
    "v_mfma_f32_16x16x32_bf16 v[180:183], a[188:191], a[8:11], v[180:183]\n"
    "v_mfma_f32_16x16x32_bf16 v[180:183], a[192:195], a[12:15], v[180:183]\n"
    "ds_read_b64_tr_b16 a[224:225], v37 offset:14464\n"
    "ds_read_b64_tr_b16 a[226:227], v37 offset:14720\n"
    "v_mfma_f32_16x16x32_bf16 v[180:183], a[196:199], a[16:19], v[180:183]\n"
    "buffer_load_dwordx4 a[160:163], v5, s[28:31], s50 offen offset:0 sc1\n"
    "v_mfma_f32_16x16x32_bf16 v[180:183], a[200:203], a[20:23], v[180:183]\n"
    "v_mfma_f32_16x16x32_bf16 v[180:183], a[204:207], a[24:27], v[180:183]\n"
    "v_mfma_f32_16x16x32_bf16 v[180:183], a[208:211], a[28:31], v[180:183]\n"
    "v_mfma_f32_16x16x32_bf16 v[184:187], a[184:187], a[8:11], v[184:187]\n"
    "v_mfma_f32_16x16x32_bf16 v[184:187], a[188:191], a[12:15], v[184:187]\n"
    "v_mfma_f32_16x16x32_bf16 v[184:187], a[192:195], a[16:19], v[184:187]\n"
    "v_mfma_f32_16x16x32_bf16 v[184:187], a[196:199], a[20:23], v[184:187]\n"
    "s_waitcnt lgkmcnt(0)\n"
    "v_mfma_f32_32x32x16_bf16 v[64:79], a[224:227], a[232:235], v[64:79]\n"
    "ds_write_b128 v15, v[52:55] offset:55072\n"
    "v_mfma_f32_16x16x32_bf16 v[184:187], a[200:203], a[24:27], v[184:187]\n"
    "v_mfma_f32_16x16x32_bf16 v[184:187], a[204:207], a[28:31], v[184:187]\n"
    "buffer_load_dwordx4 a[164:167], v5, s[28:31], s50 offen offset:1024 sc1\n"
    "v_mfma_f32_16x16x32_bf16 v[184:187], a[208:211], a[32:35], v[184:187]\n"
    "v_cvt_pk_bf16_f32 v64, v64, v65\n"
    "v_cvt_pk_bf16_f32 v65, v66, v67\n"
    "v_mfma_f32_16x16x32_bf16 v[188:191], a[184:187], a[12:15], v[188:191]\n"
    "v_cvt_pk_bf16_f32 v66, v68, v69\n"
    "v_cvt_pk_bf16_f32 v67, v70, v71\n"
    "v_mfma_f32_16x16x32_bf16 v[188:191], a[188:191], a[16:19], v[188:191]\n"
    "v_cvt_pk_bf16_f32 v68, v72, v73\n"
    "v_cvt_pk_bf16_f32 v69, v74, v75\n"
    "v_mfma_f32_16x16x32_bf16 v[188:191], a[192:195], a[20:23], v[188:191]\n"
    "v_cvt_pk_bf16_f32 v70, v76, v77\n"
    "v_cvt_pk_bf16_f32 v71, v78, v79\n"
    "v_mfma_f32_16x16x32_bf16 v[188:191], a[196:199], a[24:27], v[188:191]\n"
    "ds_write_b128 v16, v[84:87] offset:50720\n"
    "v_mfma_f32_16x16x32_bf16 v[188:191], a[200:203], a[28:31], v[188:191]\n"
    "v_mfma_f32_16x16x32_bf16 v[188:191], a[204:207], a[32:35], v[188:191]\n"
    "v_mfma_f32_16x16x32_bf16 v[188:191], a[208:211], a[36:39], v[188:191]\n"
    "ds_write_b128 v33, v[64:67] offset:55616\n"
    "v_mfma_f32_16x16x32_bf16 v[192:195], a[184:187], a[16:19], v[192:195]\n"
    "v_mfma_f32_16x16x32_bf16 v[192:195], a[188:191], a[20:23], v[192:195]\n"
    "buffer_load_dwordx4 a[168:171], v5, s[28:31], s50 offen offset:2048 sc1\n"
    "v_mfma_f32_16x16x32_bf16 v[192:195], a[192:195], a[24:27], v[192:195]\n"
    "ds_write_b128 v33, v[68:71] offset:56160\n"
    "v_mfma_f32_16x16x32_bf16 v[192:195], a[196:199], a[28:31], v[192:195]\n"
    "v_mfma_f32_16x16x32_bf16 v[192:195], a[200:203], a[32:35], v[192:195]\n"
    "v_mfma_f32_16x16x32_bf16 v[192:195], a[204:207], a[36:39], v[192:195]\n"
    "v_mfma_f32_16x16x32_bf16 v[192:195], a[208:211], a[40:43], v[192:195]\n"
    "v_mfma_f32_16x16x32_bf16 v[196:199], a[184:187], a[20:23], v[196:199]\n"
    "v_mfma_f32_16x16x32_bf16 v[196:199], a[188:191], a[24:27], v[196:199]\n"
    "v_mfma_f32_16x16x32_bf16 v[196:199], a[192:195], a[28:31], v[196:199]\n"
    "v_mfma_f32_16x16x32_bf16 v[196:199], a[196:199], a[32:35], v[196:199]\n"
    "v_mfma_f32_16x16x32_bf16 v[196:199], a[200:203], a[36:39], v[196:199]\n"
    "buffer_load_dwordx4 a[172:175], v5, s[28:31], s50 offen offset:3072 sc1\n"
    "s_add_u32 s50, s50, 4096\n"
    "v_mfma_f32_16x16x32_bf16 v[196:199], a[204:207], a[40:43], v[196:199]\n"
    "s_waitcnt lgkmcnt(0)\n"
    "s_barrier\n"
    "v_mfma_f32_16x16x32_bf16 v[196:199], a[208:211], a[44:47], v[196:199]\n"
    "v_mfma_f32_16x16x32_bf16 v[200:203], a[184:187], a[24:27], v[200:203]\n"
    "ds_read_b128 a[0:3], v18 offset:50176\n"
    "v_mfma_f32_16x16x32_bf16 v[200:203], a[188:191], a[28:31], v[200:203]\n"
    "v_mfma_f32_16x16x32_bf16 v[200:203], a[192:195], a[32:35], v[200:203]\n"
    "ds_read_b128 a[4:7], v18 offset:50720\n"
    "v_mfma_f32_16x16x32_bf16 v[200:203], a[196:199], a[36:39], v[200:203]\n"
    "v_mfma_f32_16x16x32_bf16 v[200:203], a[200:203], a[40:43], v[200:203]\n"
    "ds_read_b128 a[8:11], v18 offset:51264\n"
    "v_mfma_f32_16x16x32_bf16 v[200:203], a[204:207], a[44:47], v[200:203]\n"
    "v_mfma_f32_16x16x32_bf16 v[200:203], a[208:211], a[48:51], v[200:203]\n"
    "ds_read_b128 a[12:15], v18 offset:51808\n"
    "v_mfma_f32_16x16x32_bf16 v[204:207], a[184:187], a[28:31], v[204:207]\n"
    "buffer_load_dwordx4 a[176:179], v5, s[28:31], s50 offen offset:0 sc1\n"
    "v_mfma_f32_16x16x32_bf16 v[204:207], a[188:191], a[32:35], v[204:207]\n"
    "ds_read_b128 a[16:19], v18 offset:52352\n"
    "v_mfma_f32_16x16x32_bf16 v[204:207], a[192:195], a[36:39], v[204:207]\n"
    "v_mfma_f32_16x16x32_bf16 v[204:207], a[196:199], a[40:43], v[204:207]\n"
    "ds_read_b128 a[20:23], v18 offset:52896\n"
    "v_mfma_f32_16x16x32_bf16 v[204:207], a[200:203], a[44:47], v[204:207]\n"
    "v_mfma_f32_16x16x32_bf16 v[204:207], a[204:207], a[48:51], v[204:207]\n"
    "ds_read_b128 a[24:27], v18 offset:53440\n"
    "v_mfma_f32_16x16x32_bf16 v[204:207], a[208:211], a[52:55], v[204:207]\n"
    "v_mfma_f32_16x16x32_bf16 v[208:211], a[184:187], a[32:35], v[208:211]\n"
    "ds_read_b128 a[28:31], v18 offset:53984\n"
    "v_mfma_f32_16x16x32_bf16 v[208:211], a[188:191], a[36:39], v[208:211]\n"
    "v_mfma_f32_16x16x32_bf16 v[208:211], a[192:195], a[40:43], v[208:211]\n"
    "ds_read_b128 a[32:35], v18 offset:54528\n"
    "v_mfma_f32_16x16x32_bf16 v[208:211], a[196:199], a[44:47], v[208:211]\n"
    "buffer_load_dwordx4 a[180:183], v5, s[28:31], s50 offen offset:1024 sc1\n"
    "v_mfma_f32_16x16x32_bf16 v[208:211], a[200:203], a[48:51], v[208:211]\n"
    "v_mfma_f32_16x16x32_bf16 v[208:211], a[204:207], a[52:55], v[208:211]\n"
    "v_mfma_f32_16x16x32_bf16 v[208:211], a[208:211], a[56:59], v[208:211]\n"
    "v_mfma_f32_16x16x32_bf16 v[212:215], a[184:187], a[36:39], v[212:215]\n"
    "v_mfma_f32_16x16x32_bf16 v[212:215], a[188:191], a[40:43], v[212:215]\n"
    "ds_read_b128 a[36:39], v18 offset:55072\n"
    "v_mfma_f32_16x16x32_bf16 v[212:215], a[192:195], a[44:47], v[212:215]\n"
    "v_mfma_f32_16x16x32_bf16 v[212:215], a[196:199], a[48:51], v[212:215]\n"
    "ds_read_b128 a[40:43], v18 offset:55616\n"
    "v_mfma_f32_16x16x32_bf16 v[212:215], a[200:203], a[52:55], v[212:215]\n"
    "v_mfma_f32_16x16x32_bf16 v[212:215], a[204:207], a[56:59], v[212:215]\n"
    "ds_read_b128 a[44:47], v18 offset:56160\n"
    "v_mfma_f32_16x16x32_bf16 v[212:215], a[208:211], a[60:63], v[212:215]\n"
    "s_waitcnt vmcnt(14) lgkmcnt(3)\n"
    "buffer_load_dwordx4 a[184:187], v5, s[28:31], s50 offen offset:2048 sc1\n"
    "v_mfma_f32_16x16x32_bf16 v[216:219], a[212:215], a[64:67], v[216:219]\n"
    "ds_read_b128 a[48:51], v18 offset:56704\n"
    "v_mfma_f32_16x16x32_bf16 v[216:219], a[216:219], a[72:75], v[216:219]\n"
    "buffer_load_dwordx4 a[188:191], v5, s[28:31], s50 offen offset:3072 sc1\n"
    "s_add_u32 s50, s50, 4096\n"
    "v_mfma_f32_16x16x32_bf16 v[220:223], a[212:215], a[68:71], v[220:223]\n"
    "ds_read_b128 a[52:55], v18 offset:57248\n"
    "v_mfma_f32_16x16x32_bf16 v[220:223], a[216:219], a[76:79], v[220:223]\n"
    "buffer_load_dwordx4 a[192:195], v5, s[28:31], s50 offen offset:0 sc1\n"
    "v_mfma_f32_16x16x32_bf16 v[224:227], a[212:215], a[72:75], v[224:227]\n"
    "ds_read_b128 a[56:59], v18 offset:57792\n"
    "v_mfma_f32_16x16x32_bf16 v[224:227], a[216:219], a[80:83], v[224:227]\n"
    "buffer_load_dwordx4 a[196:199], v5, s[28:31], s50 offen offset:1024 sc1\n"
    "v_mfma_f32_16x16x32_bf16 v[228:231], a[212:215], a[76:79], v[228:231]\n"
    "ds_read_b128 a[60:63], v18 offset:58336\n"
    "v_mfma_f32_16x16x32_bf16 v[228:231], a[216:219], a[84:87], v[228:231]\n"
    "buffer_load_dwordx4 a[200:203], v5, s[28:31], s50 offen offset:2048 sc1\n"
    "v_mfma_f32_16x16x32_bf16 v[232:235], a[212:215], a[80:83], v[232:235]\n"
    "v_mfma_f32_16x16x32_bf16 v[232:235], a[216:219], a[88:91], v[232:235]\n"
    "buffer_load_dwordx4 a[204:207], v5, s[28:31], s50 offen offset:3072 sc1\n"
    "s_add_u32 s50, s50, 4096\n"
    "v_mfma_f32_16x16x32_bf16 v[236:239], a[212:215], a[84:87], v[236:239]\n"
    "v_mfma_f32_16x16x32_bf16 v[236:239], a[216:219], a[92:95], v[236:239]\n"
    "buffer_load_dwordx4 a[208:211], v5, s[28:31], s50 offen offset:0 sc1\n"
    "v_mfma_f32_16x16x32_bf16 v[240:243], a[212:215], a[88:91], v[240:243]\n"
    "v_mfma_f32_16x16x32_bf16 v[240:243], a[216:219], a[96:99], v[240:243]\n"
    "v_mfma_f32_16x16x32_bf16 v[244:247], a[212:215], a[92:95], v[244:247]\n"
    "v_mfma_f32_16x16x32_bf16 v[244:247], a[216:219], a[100:103], v[244:247]\n"
    "v_mfma_f32_16x16x32_bf16 v[248:251], a[212:215], a[96:99], v[248:251]\n"
    "v_mfma_f32_16x16x32_bf16 v[248:251], a[216:219], a[104:107], v[248:251]\n"
    "v_mfma_f32_16x16x32_bf16 v[252:255], a[212:215], a[100:103], v[252:255]\n"
    "v_mfma_f32_16x16x32_bf16 v[252:255], a[216:219], a[108:111], v[252:255]\n"
    "s_waitcnt vmcnt(14) lgkmcnt(0)\n"
    "s_cmp_eq_u32 s44, 1\n"
    "s_cbranch_scc1 23f\n"
    "v_mfma_f32_16x16x32_bf16 v[100:103], a[128:131], a[4:7], v[100:103]\n"
    "buffer_load_dwordx4 a[212:215], v5, s[28:31], s50 offen offset:1024 sc1\n"
    "ds_read_b128 a[64:67], v18 offset:50800\n"
    "v_mfma_f32_16x16x32_bf16 v[100:103], a[132:135], a[8:11], v[100:103]\n"
    "v_add_u32 v46, s48, v5\n"
    "v_add_u32 v47, s49, v5\n"
    "v_mfma_f32_16x16x32_bf16 v[100:103], a[136:139], a[12:15], v[100:103]\n"
    "s_mov_b32 s76, 0x3f360b61\n"
    "buffer_load_dwordx4 a[216:219], v5, s[28:31], s50 offen offset:2048 sc1\n"
    "s_mov_b32 s78, 0x3eb60b61\n"
    "v_mfma_f32_16x16x32_bf16 v[100:103], a[140:143], a[16:19], v[100:103]\n"
    "s_mov_b32 s80, 0x3e360b61\n"
    "s_mov_b32 s82, 0x3db60b61\n"
    "s_mov_b32 s84, 0x41000000\n"
    "v_mfma_f32_16x16x32_bf16 v[100:103], a[144:147], a[20:23], v[100:103]\n"
    "s_lshl_b32 s72, s43, 5\n"
    "v_xor_b32 v40, s72, v40\n"
    "s_lshl_b32 s72, s42, 10\n"
    "v_mfma_f32_16x16x32_bf16 v[100:103], a[148:151], a[24:27], v[100:103]\n"
    "v_add_u32 v41, s72, v41\n"
    "ds_read_b128 a[68:71], v18 offset:51344\n"
    "v_mfma_f32_16x16x32_bf16 v[100:103], a[152:155], a[28:31], v[100:103]\n"
    "ds_read_b128 a[72:75], v18 offset:51888\n"
    "v_mfma_f32_16x16x32_bf16 v[96:99], a[128:131], a[0:3], v[96:99]\n"
    "ds_read_b128 a[76:79], v18 offset:52432\n"
    "v_mfma_f32_16x16x32_bf16 v[96:99], a[132:135], a[4:7], v[96:99]\n"
    "ds_read_b128 a[80:83], v18 offset:52976\n"
    "v_mfma_f32_16x16x32_bf16 v[96:99], a[136:139], a[8:11], v[96:99]\n"
    "ds_read_b128 a[84:87], v18 offset:53520\n"
    "v_mfma_f32_16x16x32_bf16 v[96:99], a[140:143], a[12:15], v[96:99]\n"
    "ds_read_b128 a[88:91], v18 offset:54064\n"
    "v_mfma_f32_16x16x32_bf16 v[96:99], a[144:147], a[16:19], v[96:99]\n"
    "ds_read_b128 a[92:95], v18 offset:50256\n"
    "v_mfma_f32_16x16x32_bf16 v[96:99], a[148:151], a[20:23], v[96:99]\n"
    "ds_read_b128 a[96:99], v19 offset:50720\n"
    "v_mfma_f32_16x16x32_bf16 v[96:99], a[152:155], a[24:27], v[96:99]\n"
    "ds_read_b128 a[100:103], v19 offset:51808\n"
    "v_mfma_f32_16x16x32_bf16 v[108:111], a[128:131], a[12:15], v[108:111]\n"
    "ds_read_b128 a[104:107], v18 offset:50880\n"
    "v_mfma_f32_16x16x32_bf16 v[108:111], a[132:135], a[16:19], v[108:111]\n"
    "ds_read_b128 a[108:111], v18 offset:51424\n"
    "v_mfma_f32_16x16x32_bf16 v[108:111], a[136:139], a[20:23], v[108:111]\n"
    "ds_read_b128 a[112:115], v18 offset:51968\n"
    "v_mfma_f32_16x16x32_bf16 v[108:111], a[140:143], a[24:27], v[108:111]\n"
    "ds_read_b128 a[116:119], v18 offset:52512\n"
    "v_mfma_f32_16x16x32_bf16 v[108:111], a[144:147], a[28:31], v[108:111]\n"
    "ds_read_b128 a[120:123], v18 offset:53056\n"
    "v_mfma_f32_16x16x32_bf16 v[108:111], a[148:151], a[32:35], v[108:111]\n"
    "ds_read_b128 a[124:127], v18 offset:53600\n"
    "v_mfma_f32_16x16x32_bf16 v[108:111], a[152:155], a[36:39], v[108:111]\n"
    "ds_read_b128 a[220:223], v18 offset:54144\n"
    "v_mfma_f32_16x16x32_bf16 v[104:107], a[128:131], a[8:11], v[104:107]\n"
    "ds_read_b128 a[224:227], v19 offset:50176\n"
    "v_mfma_f32_16x16x32_bf16 v[104:107], a[132:135], a[12:15], v[104:107]\n"
    "ds_read_b128 a[228:231], v19 offset:51264\n"
    "v_mfma_f32_16x16x32_bf16 v[104:107], a[136:139], a[16:19], v[104:107]\n"
    "ds_read_b128 a[232:235], v18 offset:50336\n"
    "v_mfma_f32_16x16x32_bf16 v[104:107], a[140:143], a[20:23], v[104:107]\n"
    "ds_read_b128 a[236:239], v18 offset:54608\n"
    "v_mfma_f32_16x16x32_bf16 v[104:107], a[144:147], a[24:27], v[104:107]\n"
    "ds_read_b128 a[240:243], v18 offset:55152\n"
    "v_mfma_f32_16x16x32_bf16 v[104:107], a[148:151], a[28:31], v[104:107]\n"
    "ds_read_b128 a[244:247], v19 offset:52896\n"
    "v_mfma_f32_16x16x32_bf16 v[104:107], a[152:155], a[32:35], v[104:107]\n"
    "s_waitcnt vmcnt(9) lgkmcnt(9)\n"
    "ds_read_b128 a[248:251], v18 offset:54688\n"
    "v_mfma_f32_16x16x32_bf16 v[140:143], a[156:159], a[64:67], v[140:143]\n"
    "ds_read_b128 a[252:255], v18 offset:55232\n"
    "v_mfma_f32_16x16x32_bf16 v[140:143], a[160:163], a[68:71], v[140:143]\n"
    "v_mfma_f32_16x16x32_bf16 v[140:143], a[164:167], a[72:75], v[140:143]\n"
    "v_mfma_f32_16x16x32_bf16 v[140:143], a[168:171], a[76:79], v[140:143]\n"
    "v_mfma_f32_16x16x32_bf16 v[140:143], a[172:175], a[80:83], v[140:143]\n"
    "v_mfma_f32_16x16x32_bf16 v[140:143], a[176:179], a[84:87], v[140:143]\n"
    "v_mfma_f32_16x16x32_bf16 v[140:143], a[180:183], a[88:91], v[140:143]\n"
    "v_mfma_f32_16x16x32_bf16 v[136:139], a[156:159], a[92:95], v[136:139]\n"
    "v_mfma_f32_16x16x32_bf16 v[136:139], a[160:163], a[64:67], v[136:139]\n"
    "v_mfma_f32_16x16x32_bf16 v[136:139], a[164:167], a[68:71], v[136:139]\n"
    "v_mul_f32 v100, s76, v100\n"
    "v_mul_f32 v101, s76, v101\n"
    "v_mfma_f32_16x16x32_bf16 v[136:139], a[168:171], a[72:75], v[136:139]\n"
    "v_mul_f32 v102, s76, v102\n"
    "v_mul_f32 v103, s76, v103\n"
    "v_mfma_f32_16x16x32_bf16 v[136:139], a[172:175], a[76:79], v[136:139]\n"
    "v_fma_f32 v140, -v140, s76, v100\n"
    "v_fma_f32 v141, -v141, s76, v101\n"
    "v_mfma_f32_16x16x32_bf16 v[136:139], a[176:179], a[80:83], v[136:139]\n"
    "v_fma_f32 v142, -v142, s76, v102\n"
    "v_fma_f32 v143, -v143, s76, v103\n"
    "v_mfma_f32_16x16x32_bf16 v[136:139], a[180:183], a[84:87], v[136:139]\n"
    "s_waitcnt vmcnt(0) lgkmcnt(0)\n"
    "v_fma_f32 v100, v100, 2.0, -v140\n"
    "v_fma_f32 v101, v101, 2.0, -v141\n"
    "v_mfma_f32_16x16x32_bf16 v[220:223], a[212:215], a[96:99], v[220:223]\n"
    "v_fma_f32 v102, v102, 2.0, -v142\n"
    "v_fma_f32 v103, v103, 2.0, -v143\n"
    "v_mfma_f32_16x16x32_bf16 v[220:223], a[216:219], a[100:103], v[220:223]\n"
    "v_mfma_f32_16x16x32_bf16 v[180:183], a[184:187], a[104:107], v[180:183]\n"
    "ds_write_b128 v46, v[140:143] offset:1024\n"
    "v_mfma_f32_16x16x32_bf16 v[180:183], a[188:191], a[108:111], v[180:183]\n"
    "v_mul_f32 v96, s76, v96\n"
    "v_mul_f32 v97, s76, v97\n"
    "v_mfma_f32_16x16x32_bf16 v[180:183], a[192:195], a[112:115], v[180:183]\n"
    "v_fma_f32 v220, v220, s76, v140\n"
    "v_fma_f32 v221, v221, s76, v141\n"
    "v_mfma_f32_16x16x32_bf16 v[180:183], a[196:199], a[116:119], v[180:183]\n"
    "ds_write_b128 v46, v[100:103] offset:0\n"
    "v_mfma_f32_16x16x32_bf16 v[180:183], a[200:203], a[120:123], v[180:183]\n"
    "v_fma_f32 v222, v222, s76, v142\n"
    "v_fma_f32 v223, v223, s76, v143\n"
    "v_mfma_f32_16x16x32_bf16 v[180:183], a[204:207], a[124:127], v[180:183]\n"
    "v_mul_f32 v98, s76, v98\n"
    "v_mul_f32 v99, s76, v99\n"
    "v_mfma_f32_16x16x32_bf16 v[180:183], a[208:211], a[220:223], v[180:183]\n"
    "ds_write_b128 v46, v[220:223] offset:3072\n"
    "v_mfma_f32_16x16x32_bf16 v[216:219], a[212:215], a[224:227], v[216:219]\n"
    "v_fma_f32 v136, -v136, s76, v96\n"
    "v_fma_f32 v137, -v137, s76, v97\n"
    "v_mfma_f32_16x16x32_bf16 v[216:219], a[216:219], a[228:231], v[216:219]\n"
    "v_fma_f32 v138, -v138, s76, v98\n"
    "v_fma_f32 v139, -v139, s76, v99\n"
    "v_mfma_f32_16x16x32_bf16 v[176:179], a[184:187], a[232:235], v[176:179]\n"
    "v_fma_f32 v96, v96, 2.0, -v136\n"
    "v_fma_f32 v97, v97, 2.0, -v137\n"
    "v_mfma_f32_16x16x32_bf16 v[176:179], a[188:191], a[104:107], v[176:179]\n"
    "v_fma_f32 v98, v98, 2.0, -v138\n"
    "v_fma_f32 v99, v99, 2.0, -v139\n"
    "v_mfma_f32_16x16x32_bf16 v[176:179], a[192:195], a[108:111], v[176:179]\n"
    "v_fma_f32 v180, v180, s76, v100\n"
    "v_fma_f32 v181, v181, s76, v101\n"
    "v_mfma_f32_16x16x32_bf16 v[176:179], a[196:199], a[112:115], v[176:179]\n"
    "v_fma_f32 v182, v182, s76, v102\n"
    "v_fma_f32 v183, v183, s76, v103\n"
    "v_mfma_f32_16x16x32_bf16 v[176:179], a[200:203], a[116:119], v[176:179]\n"
    "v_fma_f32 v216, v216, s76, v136\n"
    "v_fma_f32 v217, v217, s76, v137\n"
    "v_mfma_f32_16x16x32_bf16 v[176:179], a[204:207], a[120:123], v[176:179]\n"
    "ds_write_b128 v46, v[180:183] offset:2048\n"
    "v_mfma_f32_16x16x32_bf16 v[176:179], a[208:211], a[124:127], v[176:179]\n"
    "v_fma_f32 v218, v218, s76, v138\n"
    "v_fma_f32 v219, v219, s76, v139\n"
    "v_mfma_f32_16x16x32_bf16 v[148:151], a[156:159], a[72:75], v[148:151]\n"
    "v_mfma_f32_16x16x32_bf16 v[148:151], a[160:163], a[76:79], v[148:151]\n"
    "v_mfma_f32_16x16x32_bf16 v[148:151], a[164:167], a[80:83], v[148:151]\n"
    "v_mfma_f32_16x16x32_bf16 v[148:151], a[168:171], a[84:87], v[148:151]\n"
    "s_waitcnt lgkmcnt(0)\n"
    "s_barrier\n"
    "v_mfma_f32_16x16x32_bf16 v[148:151], a[172:175], a[88:91], v[148:151]\n"
    "ds_read_b128 v[100:103], v47 offset:0\n"
    "ds_read_b128 v[140:143], v47 offset:1024\n"
    "v_mfma_f32_16x16x32_bf16 v[148:151], a[176:179], a[236:239], v[148:151]\n"
    "ds_read_b128 v[180:183], v47 offset:2048\n"
    "ds_read_b128 v[220:223], v47 offset:3072\n"
    "v_mfma_f32_16x16x32_bf16 v[148:151], a[180:183], a[240:243], v[148:151]\n"
    "ds_read_b128 a[0:3], v19 offset:52352\n"
    "v_mfma_f32_16x16x32_bf16 v[228:231], a[212:215], a[100:103], v[228:231]\n"
    "ds_read_b128 a[4:7], v18 offset:55696\n"
    "v_mfma_f32_16x16x32_bf16 v[228:231], a[216:219], a[244:247], v[228:231]\n"
    "ds_read_b128 a[8:11], v18 offset:56240\n"
    "v_mfma_f32_16x16x32_bf16 v[188:191], a[184:187], a[112:115], v[188:191]\n"
    "ds_read_b128 a[12:15], v19 offset:53984\n"
    "v_pk_mul_f32 v[108:109], v[108:109], s[76:77] op_sel_hi:[1,0]\n"
    "v_pk_mul_f32 v[110:111], v[110:111], s[76:77] op_sel_hi:[1,0]\n"
    "v_pk_fma_f32 v[148:149], v[148:149], s[76:77], v[108:109] op_sel_hi:[1,0,1] neg_lo:[1,0,0] neg_hi:[1,0,0]\n"
    "v_pk_fma_f32 v[150:151], v[150:151], s[76:77], v[110:111] op_sel_hi:[1,0,1] neg_lo:[1,0,0] neg_hi:[1,0,0]\n"
    "v_pk_fma_f32 v[108:109], v[108:109], 2.0, v[148:149] op_sel_hi:[1,0,1] neg_lo:[0,0,1] neg_hi:[0,0,1]\n"
    "v_pk_fma_f32 v[110:111], v[110:111], 2.0, v[150:151] op_sel_hi:[1,0,1] neg_lo:[0,0,1] neg_hi:[0,0,1]\n"
    "v_mfma_f32_16x16x32_bf16 v[188:191], a[188:191], a[116:119], v[188:191]\n"
    "ds_read_b128 a[92:95], v18 offset:55776\n"
    "v_mfma_f32_16x16x32_bf16 v[188:191], a[192:195], a[120:123], v[188:191]\n"
    "s_waitcnt lgkmcnt(5)\n"
    "ds_write_b128 v46, v[148:151] offset:5120\n"
    "v_pk_fma_f32 v[216:217], v[220:221], s[84:85], v[216:217] op_sel_hi:[1,0,1]\n"
    "v_pk_fma_f32 v[218:219], v[222:223], s[84:85], v[218:219] op_sel_hi:[1,0,1]\n"
    "v_pk_fma_f32 v[136:137], v[140:141], 2.0, v[136:137] op_sel_hi:[1,0,1]\n"
    "v_pk_fma_f32 v[138:139], v[142:143], 2.0, v[138:139] op_sel_hi:[1,0,1]\n"
    "v_pk_fma_f32 v[140:141], v[100:101], 4.0, v[96:97] op_sel_hi:[1,0,1]\n"
    "v_pk_fma_f32 v[142:143], v[102:103], 4.0, v[98:99] op_sel_hi:[1,0,1]\n"
    "v_pk_add_f32 v[96:97], v[96:97], v[100:101]\n"
    "v_pk_add_f32 v[98:99], v[98:99], v[102:103]\n"
    "v_pk_fma_f32 v[216:217], v[180:181], s[82:83], v[216:217] op_sel_hi:[1,0,1]\n"
    "v_pk_fma_f32 v[218:219], v[182:183], s[82:83], v[218:219] op_sel_hi:[1,0,1]\n"
    "v_pk_fma_f32 v[136:137], v[180:181], s[78:79], v[136:137] op_sel_hi:[1,0,1]\n"
    "v_pk_fma_f32 v[138:139], v[182:183], s[78:79], v[138:139] op_sel_hi:[1,0,1]\n"
    "v_pk_fma_f32 v[140:141], v[180:181], s[80:81], v[140:141] op_sel_hi:[1,0,1]\n"
    "v_pk_fma_f32 v[142:143], v[182:183], s[80:81], v[142:143] op_sel_hi:[1,0,1]\n"
    "v_pk_fma_f32 v[96:97], v[180:181], s[76:77], v[96:97] op_sel_hi:[1,0,1]\n"
    "v_pk_fma_f32 v[98:99], v[182:183], s[76:77], v[98:99] op_sel_hi:[1,0,1]\n"
    "v_pk_fma_f32 v[96:97], v[176:177], s[76:77], v[96:97] op_sel_hi:[1,0,1]\n"
    "v_pk_fma_f32 v[98:99], v[178:179], s[76:77], v[98:99] op_sel_hi:[1,0,1]\n"
    "v_mfma_f32_16x16x32_bf16 v[188:191], a[196:199], a[124:127], v[188:191]\n"
    "v_fma_f32 v228, v228, s76, v148\n"
    "v_fma_f32 v229, v229, s76, v149\n"
    "v_mfma_f32_16x16x32_bf16 v[188:191], a[200:203], a[220:223], v[188:191]\n"
    "v_fma_f32 v230, v230, s76, v150\n"
    "v_fma_f32 v231, v231, s76, v151\n"
    "v_mfma_f32_16x16x32_bf16 v[188:191], a[204:207], a[248:251], v[188:191]\n"
    "ds_write_b128 v46, v[108:111] offset:4096\n"
    "v_mfma_f32_16x16x32_bf16 v[188:191], a[208:211], a[252:255], v[188:191]\n"
    "v_cvt_pk_bf16_f32 v136, v136, v137\n"
    "v_cvt_pk_bf16_f32 v137, v138, v139\n"
    "v_mfma_f32_16x16x32_bf16 v[144:147], a[156:159], a[68:71], v[144:147]\n"
    "v_cvt_pk_bf16_f32 v140, v140, v141\n"
    "v_cvt_pk_bf16_f32 v141, v142, v143\n"
    "v_mfma_f32_16x16x32_bf16 v[144:147], a[160:163], a[72:75], v[144:147]\n"
    "ds_write_b128 v46, v[228:231] offset:7168\n"
    "v_mfma_f32_16x16x32_bf16 v[144:147], a[164:167], a[76:79], v[144:147]\n"
    "v_fma_f32 v188, v188, s76, v108\n"
    "v_fma_f32 v189, v189, s76, v109\n"
    "v_mfma_f32_16x16x32_bf16 v[144:147], a[168:171], a[80:83], v[144:147]\n"
    "v_fma_f32 v190, v190, s76, v110\n"
    "v_fma_f32 v191, v191, s76, v111\n"
    "v_mfma_f32_16x16x32_bf16 v[144:147], a[172:175], a[84:87], v[144:147]\n"
    "ds_write_b64 v40, v[136:137] offset:1024\n"
    "v_mfma_f32_16x16x32_bf16 v[144:147], a[176:179], a[88:91], v[144:147]\n"
    "ds_write_b128 v46, v[188:191] offset:6144\n"
    "v_mfma_f32_16x16x32_bf16 v[144:147], a[180:183], a[236:239], v[144:147]\n"
    "v_cvt_pk_bf16_f32 v216, v216, v217\n"
    "v_cvt_pk_bf16_f32 v217, v218, v219\n"
    "v_mfma_f32_16x16x32_bf16 v[224:227], a[212:215], a[228:231], v[224:227]\n"
    "s_waitcnt lgkmcnt(4)\n"
    "v_cvt_pk_bf16_f32 v96, v96, v97\n"
    "v_cvt_pk_bf16_f32 v97, v98, v99\n"
    "v_mfma_f32_16x16x32_bf16 v[224:227], a[216:219], a[0:3], v[224:227]\n"
    "ds_write_b64 v40, v[140:141] offset:2048\n"
    "v_mfma_f32_16x16x32_bf16 v[184:187], a[184:187], a[108:111], v[184:187]\n"
    "v_mul_f32 v104, s76, v104\n"
    "v_mul_f32 v105, s76, v105\n"
    "v_mfma_f32_16x16x32_bf16 v[184:187], a[188:191], a[112:115], v[184:187]\n"
    "ds_write_b64 v40, v[216:217] offset:3072\n"
    "v_mfma_f32_16x16x32_bf16 v[184:187], a[192:195], a[116:119], v[184:187]\n"
    "v_mul_f32 v106, s76, v106\n"
    "v_mul_f32 v107, s76, v107\n"
    "v_mfma_f32_16x16x32_bf16 v[184:187], a[196:199], a[120:123], v[184:187]\n"
    "ds_write_b64 v40, v[96:97] offset:0\n"
    "v_mfma_f32_16x16x32_bf16 v[184:187], a[200:203], a[124:127], v[184:187]\n"
    "v_fma_f32 v144, -v144, s76, v104\n"
    "v_fma_f32 v145, -v145, s76, v105\n"
    "v_mfma_f32_16x16x32_bf16 v[184:187], a[204:207], a[220:223], v[184:187]\n"
    "v_fma_f32 v146, -v146, s76, v106\n"
    "v_fma_f32 v147, -v147, s76, v107\n"
    "v_mfma_f32_16x16x32_bf16 v[184:187], a[208:211], a[248:251], v[184:187]\n"
    "v_fma_f32 v104, v104, 2.0, -v144\n"
    "v_fma_f32 v105, v105, 2.0, -v145\n"
    "v_mfma_f32_16x16x32_bf16 v[116:119], a[128:131], a[20:23], v[116:119]\n"
    "v_fma_f32 v106, v106, 2.0, -v146\n"
    "v_fma_f32 v107, v107, 2.0, -v147\n"
    "v_mfma_f32_16x16x32_bf16 v[116:119], a[132:135], a[24:27], v[116:119]\n"
    "s_waitcnt lgkmcnt(0)\n"
    "s_barrier\n"
    "v_mfma_f32_16x16x32_bf16 v[116:119], a[136:139], a[28:31], v[116:119]\n"
    "ds_read_b128 v[108:111], v47 offset:4096\n"
    "ds_read_b128 v[148:151], v47 offset:5120\n"
    "v_mfma_f32_16x16x32_bf16 v[116:119], a[140:143], a[32:35], v[116:119]\n"
    "ds_read_b128 v[188:191], v47 offset:6144\n"
    "ds_read_b128 v[228:231], v47 offset:7168\n"
    "v_mfma_f32_16x16x32_bf16 v[116:119], a[144:147], a[36:39], v[116:119]\n"
    "ds_read_b128 v[48:51], v41 offset:0\n"
    "ds_read_b128 v[52:55], v41 offset:4096\n"
    "v_mfma_f32_16x16x32_bf16 v[116:119], a[148:151], a[40:43], v[116:119]\n"
    "ds_read_b128 a[64:67], v18 offset:56320\n"
    "v_fma_f32 v224, v224, s76, v144\n"
    "v_mfma_f32_16x16x32_bf16 v[116:119], a[152:155], a[44:47], v[116:119]\n"
    "ds_read_b128 a[96:99], v19 offset:53440\n"
    "v_fma_f32 v225, v225, s76, v145\n"
    "v_mfma_f32_16x16x32_bf16 v[156:159], a[156:159], a[80:83], v[156:159]\n"
    "v_fma_f32 v226, v226, s76, v146\n"
    "v_fma_f32 v227, v227, s76, v147\n"
    "v_mfma_f32_16x16x32_bf16 v[156:159], a[160:163], a[84:87], v[156:159]\n"
    "v_mfma_f32_16x16x32_bf16 v[156:159], a[164:167], a[88:91], v[156:159]\n"
    "s_waitcnt lgkmcnt(3)\n"
    "s_add_u32 s58, s53, 0\n"
    "buffer_store_dwordx4 v[48:51], v42, s[24:27], s58 offen sc0 sc1\n"
    "v_pk_fma_f32 v[224:225], v[228:229], s[84:85], v[224:225] op_sel_hi:[1,0,1]\n"
    "v_pk_fma_f32 v[226:227], v[230:231], s[84:85], v[226:227] op_sel_hi:[1,0,1]\n"
    "v_pk_fma_f32 v[144:145], v[148:149], 2.0, v[144:145] op_sel_hi:[1,0,1]\n"
    "v_pk_fma_f32 v[146:147], v[150:151], 2.0, v[146:147] op_sel_hi:[1,0,1]\n"
    "v_pk_fma_f32 v[148:149], v[108:109], 4.0, v[104:105] op_sel_hi:[1,0,1]\n"
    "v_pk_fma_f32 v[150:151], v[110:111], 4.0, v[106:107] op_sel_hi:[1,0,1]\n"
    "v_pk_add_f32 v[104:105], v[104:105], v[108:109]\n"
    "v_pk_add_f32 v[106:107], v[106:107], v[110:111]\n"
    "v_pk_fma_f32 v[224:225], v[188:189], s[82:83], v[224:225] op_sel_hi:[1,0,1]\n"
    "v_pk_fma_f32 v[226:227], v[190:191], s[82:83], v[226:227] op_sel_hi:[1,0,1]\n"
    "v_pk_fma_f32 v[144:145], v[188:189], s[78:79], v[144:145] op_sel_hi:[1,0,1]\n"
    "v_pk_fma_f32 v[146:147], v[190:191], s[78:79], v[146:147] op_sel_hi:[1,0,1]\n"
    "v_pk_fma_f32 v[148:149], v[188:189], s[80:81], v[148:149] op_sel_hi:[1,0,1]\n"
    "v_pk_fma_f32 v[150:151], v[190:191], s[80:81], v[150:151] op_sel_hi:[1,0,1]\n"
    "v_pk_fma_f32 v[104:105], v[188:189], s[76:77], v[104:105] op_sel_hi:[1,0,1]\n"
    "v_pk_fma_f32 v[106:107], v[190:191], s[76:77], v[106:107] op_sel_hi:[1,0,1]\n"
    "v_pk_fma_f32 v[104:105], v[184:185], s[76:77], v[104:105] op_sel_hi:[1,0,1]\n"
    "v_pk_fma_f32 v[106:107], v[186:187], s[76:77], v[106:107] op_sel_hi:[1,0,1]\n"
    "v_mfma_f32_16x16x32_bf16 v[156:159], a[168:171], a[236:239], v[156:159]\n"
    "v_cvt_pk_bf16_f32 v144, v144, v145\n"
    "v_cvt_pk_bf16_f32 v145, v146, v147\n"
    "v_mfma_f32_16x16x32_bf16 v[156:159], a[172:175], a[240:243], v[156:159]\n"
    "s_waitcnt lgkmcnt(2)\n"
    "s_add_u32 s58, s53, 86016\n"
    "buffer_store_dwordx4 v[52:55], v42, s[24:27], s58 offen sc0 sc1\n"
    "v_cvt_pk_bf16_f32 v148, v148, v149\n"
    "v_mfma_f32_16x16x32_bf16 v[156:159], a[176:179], a[4:7], v[156:159]\n"
    "ds_write_b64 v40, v[144:145] offset:9216\n"
    "v_mfma_f32_16x16x32_bf16 v[156:159], a[180:183], a[8:11], v[156:159]\n"
    "v_cvt_pk_bf16_f32 v149, v150, v151\n"
    "v_cvt_pk_bf16_f32 v224, v224, v225\n"
    "v_mfma_f32_16x16x32_bf16 v[236:239], a[212:215], a[244:247], v[236:239]\n"
    "v_cvt_pk_bf16_f32 v225, v226, v227\n"
    "v_cvt_pk_bf16_f32 v104, v104, v105\n"
    "v_mfma_f32_16x16x32_bf16 v[236:239], a[216:219], a[12:15], v[236:239]\n"
    "ds_write_b64 v40, v[148:149] offset:10240\n"
    "v_mfma_f32_16x16x32_bf16 v[196:199], a[184:187], a[120:123], v[196:199]\n"
    "v_mul_f32 v116, s76, v116\n"
    "v_mul_f32 v117, s76, v117\n"
    "v_mfma_f32_16x16x32_bf16 v[196:199], a[188:191], a[124:127], v[196:199]\n"
    "v_mul_f32 v118, s76, v118\n"
    "v_mul_f32 v119, s76, v119\n"
    "v_mfma_f32_16x16x32_bf16 v[196:199], a[192:195], a[220:223], v[196:199]\n"
    "v_fma_f32 v156, -v156, s76, v116\n"
    "v_fma_f32 v157, -v157, s76, v117\n"
    "v_mfma_f32_16x16x32_bf16 v[196:199], a[196:199], a[248:251], v[196:199]\n"
    "v_fma_f32 v158, -v158, s76, v118\n"
    "v_fma_f32 v159, -v159, s76, v119\n"
    "v_mfma_f32_16x16x32_bf16 v[196:199], a[200:203], a[252:255], v[196:199]\n"
    "v_fma_f32 v116, v116, 2.0, -v156\n"
    "v_fma_f32 v117, v117, 2.0, -v157\n"
    "v_mfma_f32_16x16x32_bf16 v[196:199], a[204:207], a[92:95], v[196:199]\n"
    "s_waitcnt lgkmcnt(2)\n"
    "v_fma_f32 v118, v118, 2.0, -v158\n"
    "v_fma_f32 v119, v119, 2.0, -v159\n"
    "v_mfma_f32_16x16x32_bf16 v[196:199], a[208:211], a[64:67], v[196:199]\n"
    "ds_write_b64 v40, v[224:225] offset:11264\n"
    "v_mfma_f32_16x16x32_bf16 v[112:115], a[128:131], a[16:19], v[112:115]\n"
    "ds_write_b128 v46, v[156:159] offset:9216\n"
    "v_mfma_f32_16x16x32_bf16 v[112:115], a[132:135], a[20:23], v[112:115]\n"
    "v_cvt_pk_bf16_f32 v105, v106, v107\n"
    "v_fma_f32 v236, v236, s76, v156\n"
    "v_mfma_f32_16x16x32_bf16 v[112:115], a[136:139], a[24:27], v[112:115]\n"
    "v_fma_f32 v237, v237, s76, v157\n"
    "v_fma_f32 v238, v238, s76, v158\n"
    "v_mfma_f32_16x16x32_bf16 v[112:115], a[140:143], a[28:31], v[112:115]\n"
    "ds_write_b128 v46, v[116:119] offset:8192\n"
    "v_mfma_f32_16x16x32_bf16 v[112:115], a[144:147], a[32:35], v[112:115]\n"
    "v_fma_f32 v239, v239, s76, v159\n"
    "v_fma_f32 v196, v196, s76, v116\n"
    "v_mfma_f32_16x16x32_bf16 v[112:115], a[148:151], a[36:39], v[112:115]\n"
    "v_fma_f32 v197, v197, s76, v117\n"
    "v_fma_f32 v198, v198, s76, v118\n"
    "v_mfma_f32_16x16x32_bf16 v[112:115], a[152:155], a[40:43], v[112:115]\n"
    "ds_write_b64 v40, v[104:105] offset:8192\n"
    "v_mfma_f32_16x16x32_bf16 v[152:155], a[156:159], a[76:79], v[152:155]\n"
    "ds_write_b128 v46, v[236:239] offset:11264\n"
    "v_mfma_f32_16x16x32_bf16 v[152:155], a[160:163], a[80:83], v[152:155]\n"
    "v_fma_f32 v199, v199, s76, v119\n"
    "v_mfma_f32_16x16x32_bf16 v[152:155], a[164:167], a[84:87], v[152:155]\n"
    "v_mfma_f32_16x16x32_bf16 v[152:155], a[168:171], a[88:91], v[152:155]\n"
    "ds_write_b128 v46, v[196:199] offset:10240\n"
    "v_mfma_f32_16x16x32_bf16 v[152:155], a[172:175], a[236:239], v[152:155]\n"
    "v_mfma_f32_16x16x32_bf16 v[152:155], a[176:179], a[240:243], v[152:155]\n"
    "v_mfma_f32_16x16x32_bf16 v[152:155], a[180:183], a[4:7], v[152:155]\n"
    "v_mfma_f32_16x16x32_bf16 v[232:235], a[212:215], a[0:3], v[232:235]\n"
    "s_waitcnt lgkmcnt(0)\n"
    "s_barrier\n"
    "v_mfma_f32_16x16x32_bf16 v[232:235], a[216:219], a[96:99], v[232:235]\n"
    "ds_read_b128 v[116:119], v47 offset:8192\n"
    "ds_read_b128 v[156:159], v47 offset:9216\n"
    "v_mfma_f32_16x16x32_bf16 v[192:195], a[184:187], a[116:119], v[192:195]\n"
    "ds_read_b128 v[196:199], v47 offset:10240\n"
    "ds_read_b128 v[236:239], v47 offset:11264\n"
    "v_mfma_f32_16x16x32_bf16 v[192:195], a[188:191], a[120:123], v[192:195]\n"
    "ds_read_b128 v[56:59], v41 offset:8192\n"
    "ds_read_b128 v[60:63], v41 offset:12288\n"
    "v_mfma_f32_16x16x32_bf16 v[192:195], a[192:195], a[124:127], v[192:195]\n"
    "ds_read_b128 a[224:227], v18 offset:56784\n"
    "v_mul_f32 v112, s76, v112\n"
    "v_mfma_f32_16x16x32_bf16 v[192:195], a[196:199], a[220:223], v[192:195]\n"
    "ds_read_b128 a[232:235], v18 offset:57328\n"
    "v_mul_f32 v113, s76, v113\n"
    "v_mfma_f32_16x16x32_bf16 v[192:195], a[200:203], a[248:251], v[192:195]\n"
    "ds_read_b128 a[104:107], v19 offset:55072\n"
    "v_mul_f32 v114, s76, v114\n"
    "v_mfma_f32_16x16x32_bf16 v[192:195], a[204:207], a[252:255], v[192:195]\n"
    "ds_read_b128 a[100:103], v18 offset:56864\n"
    "v_mul_f32 v115, s76, v115\n"
    "v_mfma_f32_16x16x32_bf16 v[192:195], a[208:211], a[92:95], v[192:195]\n"
    "s_waitcnt lgkmcnt(5)\n"
    "s_add_u32 s58, s53, 172032\n"
    "buffer_store_dwordx4 v[56:59], v42, s[24:27], s58 offen sc0 sc1\n"
    "ds_read_b128 a[68:71], v18 offset:57408\n"
    "v_mfma_f32_16x16x32_bf16 v[124:127], a[128:131], a[28:31], v[124:127]\n"
    "ds_read_b128 a[72:75], v19 offset:54528\n"
    "v_fma_f32 v152, -v152, s76, v112\n"
    "v_mfma_f32_16x16x32_bf16 v[124:127], a[132:135], a[32:35], v[124:127]\n"
    "s_waitcnt lgkmcnt(6)\n"
    "s_add_u32 s58, s53, 258048\n"
    "buffer_store_dwordx4 v[60:63], v42, s[24:27], s58 offen sc0 sc1\n"
    "v_fma_f32 v153, -v153, s76, v113\n"
    "v_mfma_f32_16x16x32_bf16 v[124:127], a[136:139], a[36:39], v[124:127]\n"
    "v_fma_f32 v154, -v154, s76, v114\n"
    "v_fma_f32 v155, -v155, s76, v115\n"
    "v_mfma_f32_16x16x32_bf16 v[124:127], a[140:143], a[40:43], v[124:127]\n"
    "v_fma_f32 v112, v112, 2.0, -v152\n"
    "v_fma_f32 v113, v113, 2.0, -v153\n"
    "v_mfma_f32_16x16x32_bf16 v[124:127], a[144:147], a[44:47], v[124:127]\n"
    "v_fma_f32 v114, v114, 2.0, -v154\n"
    "v_fma_f32 v115, v115, 2.0, -v155\n"
    "v_mfma_f32_16x16x32_bf16 v[124:127], a[148:151], a[48:51], v[124:127]\n"
    "v_fma_f32 v232, v232, s76, v152\n"
    "v_fma_f32 v233, v233, s76, v153\n"
    "v_mfma_f32_16x16x32_bf16 v[124:127], a[152:155], a[52:55], v[124:127]\n"
    "v_fma_f32 v234, v234, s76, v154\n"
    "v_fma_f32 v235, v235, s76, v155\n"
    "v_pk_fma_f32 v[232:233], v[236:237], s[84:85], v[232:233] op_sel_hi:[1,0,1]\n"
    "v_pk_fma_f32 v[234:235], v[238:239], s[84:85], v[234:235] op_sel_hi:[1,0,1]\n"
    "v_pk_fma_f32 v[152:153], v[156:157], 2.0, v[152:153] op_sel_hi:[1,0,1]\n"
    "v_pk_fma_f32 v[154:155], v[158:159], 2.0, v[154:155] op_sel_hi:[1,0,1]\n"
    "v_pk_fma_f32 v[156:157], v[116:117], 4.0, v[112:113] op_sel_hi:[1,0,1]\n"
    "v_pk_fma_f32 v[158:159], v[118:119], 4.0, v[114:115] op_sel_hi:[1,0,1]\n"
    "v_pk_add_f32 v[112:113], v[112:113], v[116:117]\n"
    "v_pk_add_f32 v[114:115], v[114:115], v[118:119]\n"
    "v_pk_fma_f32 v[232:233], v[196:197], s[82:83], v[232:233] op_sel_hi:[1,0,1]\n"
    "v_pk_fma_f32 v[234:235], v[198:199], s[82:83], v[234:235] op_sel_hi:[1,0,1]\n"
    "v_pk_fma_f32 v[152:153], v[196:197], s[78:79], v[152:153] op_sel_hi:[1,0,1]\n"
    "v_pk_fma_f32 v[154:155], v[198:199], s[78:79], v[154:155] op_sel_hi:[1,0,1]\n"
    "v_pk_fma_f32 v[156:157], v[196:197], s[80:81], v[156:157] op_sel_hi:[1,0,1]\n"
    "v_pk_fma_f32 v[158:159], v[198:199], s[80:81], v[158:159] op_sel_hi:[1,0,1]\n"
    "v_pk_fma_f32 v[112:113], v[196:197], s[76:77], v[112:113] op_sel_hi:[1,0,1]\n"
    "v_pk_fma_f32 v[114:115], v[198:199], s[76:77], v[114:115] op_sel_hi:[1,0,1]\n"
    "v_mfma_f32_16x16x32_bf16 v[164:167], a[156:159], a[88:91], v[164:167]\n"
    "v_fmac_f32 v112, s76, v192\n"
    "v_fmac_f32 v113, s76, v193\n"
    "v_mfma_f32_16x16x32_bf16 v[164:167], a[160:163], a[236:239], v[164:167]\n"
    "v_fmac_f32 v114, s76, v194\n"
    "v_fmac_f32 v115, s76, v195\n"
    "v_mfma_f32_16x16x32_bf16 v[164:167], a[164:167], a[240:243], v[164:167]\n"
    "v_cvt_pk_bf16_f32 v152, v152, v153\n"
    "v_cvt_pk_bf16_f32 v153, v154, v155\n"
    "v_mfma_f32_16x16x32_bf16 v[164:167], a[168:171], a[4:7], v[164:167]\n"
    "v_cvt_pk_bf16_f32 v156, v156, v157\n"
    "v_cvt_pk_bf16_f32 v157, v158, v159\n"
    "v_mfma_f32_16x16x32_bf16 v[164:167], a[172:175], a[8:11], v[164:167]\n"
    "s_waitcnt lgkmcnt(0)\n"
    "ds_write_b64 v40, v[152:153] offset:17408\n"
    "v_mfma_f32_16x16x32_bf16 v[164:167], a[176:179], a[224:227], v[164:167]\n"
    "v_cvt_pk_bf16_f32 v232, v232, v233\n"
    "v_cvt_pk_bf16_f32 v233, v234, v235\n"
    "v_mfma_f32_16x16x32_bf16 v[164:167], a[180:183], a[232:235], v[164:167]\n"
    "ds_write_b64 v40, v[156:157] offset:18432\n"
    "v_mfma_f32_16x16x32_bf16 v[244:247], a[212:215], a[12:15], v[244:247]\n"
    "v_cvt_pk_bf16_f32 v112, v112, v113\n"
    "v_cvt_pk_bf16_f32 v113, v114, v115\n"
    "v_mfma_f32_16x16x32_bf16 v[244:247], a[216:219], a[104:107], v[244:247]\n"
    "ds_write_b64 v40, v[232:233] offset:19456\n"
    "v_mfma_f32_16x16x32_bf16 v[204:207], a[184:187], a[220:223], v[204:207]\n"
    "v_mul_f32 v124, s76, v124\n"
    "v_mul_f32 v125, s76, v125\n"
    "v_mfma_f32_16x16x32_bf16 v[204:207], a[188:191], a[248:251], v[204:207]\n"
    "v_mul_f32 v126, s76, v126\n"
    "v_mul_f32 v127, s76, v127\n"
    "v_mfma_f32_16x16x32_bf16 v[204:207], a[192:195], a[252:255], v[204:207]\n"
    "v_fma_f32 v164, -v164, s76, v124\n"
    "v_fma_f32 v165, -v165, s76, v125\n"
    "v_mfma_f32_16x16x32_bf16 v[204:207], a[196:199], a[92:95], v[204:207]\n"
    "v_fma_f32 v166, -v166, s76, v126\n"
    "v_fma_f32 v167, -v167, s76, v127\n"
    "v_mfma_f32_16x16x32_bf16 v[204:207], a[200:203], a[64:67], v[204:207]\n"
    "v_fma_f32 v124, v124, 2.0, -v164\n"
    "v_fma_f32 v125, v125, 2.0, -v165\n"
    "v_mfma_f32_16x16x32_bf16 v[204:207], a[204:207], a[100:103], v[204:207]\n"
    "v_fma_f32 v126, v126, 2.0, -v166\n"
    "v_fma_f32 v127, v127, 2.0, -v167\n"
    "v_mfma_f32_16x16x32_bf16 v[204:207], a[208:211], a[68:71], v[204:207]\n"
    "ds_write_b64 v40, v[112:113] offset:16384\n"
    "v_mfma_f32_16x16x32_bf16 v[120:123], a[128:131], a[24:27], v[120:123]\n"
    "ds_write_b128 v46, v[164:167] offset:1024\n"
    "v_mfma_f32_16x16x32_bf16 v[120:123], a[132:135], a[28:31], v[120:123]\n"
    "v_fma_f32 v244, v244, s76, v164\n"
    "v_fma_f32 v245, v245, s76, v165\n"
    "v_mfma_f32_16x16x32_bf16 v[120:123], a[136:139], a[32:35], v[120:123]\n"
    "v_fma_f32 v246, v246, s76, v166\n"
    "v_fma_f32 v247, v247, s76, v167\n"
    "v_mfma_f32_16x16x32_bf16 v[120:123], a[140:143], a[36:39], v[120:123]\n"
    "ds_write_b128 v46, v[124:127] offset:0\n"
    "v_mfma_f32_16x16x32_bf16 v[120:123], a[144:147], a[40:43], v[120:123]\n"
    "v_fma_f32 v204, v204, s76, v124\n"
    "v_fma_f32 v205, v205, s76, v125\n"
    "v_mfma_f32_16x16x32_bf16 v[120:123], a[148:151], a[44:47], v[120:123]\n"
    "v_fma_f32 v206, v206, s76, v126\n"
    "v_fma_f32 v207, v207, s76, v127\n"
    "v_mfma_f32_16x16x32_bf16 v[120:123], a[152:155], a[48:51], v[120:123]\n"
    "ds_write_b128 v46, v[244:247] offset:3072\n"
    "v_mfma_f32_16x16x32_bf16 v[160:163], a[156:159], a[84:87], v[160:163]\n"
    "v_mfma_f32_16x16x32_bf16 v[160:163], a[160:163], a[88:91], v[160:163]\n"
    "v_mfma_f32_16x16x32_bf16 v[160:163], a[164:167], a[236:239], v[160:163]\n"
    "ds_write_b128 v46, v[204:207] offset:2048\n"
    "v_mfma_f32_16x16x32_bf16 v[160:163], a[168:171], a[240:243], v[160:163]\n"
    "v_mfma_f32_16x16x32_bf16 v[160:163], a[172:175], a[4:7], v[160:163]\n"
    "v_mfma_f32_16x16x32_bf16 v[160:163], a[176:179], a[8:11], v[160:163]\n"
    "v_mfma_f32_16x16x32_bf16 v[160:163], a[180:183], a[224:227], v[160:163]\n"
    "v_mfma_f32_16x16x32_bf16 v[240:243], a[212:215], a[96:99], v[240:243]\n"
    "s_waitcnt lgkmcnt(0)\n"
    "s_barrier\n"
    "v_mfma_f32_16x16x32_bf16 v[240:243], a[216:219], a[72:75], v[240:243]\n"
    "ds_read_b128 v[124:127], v47 offset:0\n"
    "ds_read_b128 v[164:167], v47 offset:1024\n"
    "v_mfma_f32_16x16x32_bf16 v[200:203], a[184:187], a[124:127], v[200:203]\n"
    "ds_read_b128 v[204:207], v47 offset:2048\n"
    "ds_read_b128 v[244:247], v47 offset:3072\n"
    "v_mfma_f32_16x16x32_bf16 v[200:203], a[188:191], a[220:223], v[200:203]\n"
    "ds_read_b128 v[64:67], v41 offset:16384\n"
    "ds_read_b128 v[68:71], v41 offset:20480\n"
    "v_mfma_f32_16x16x32_bf16 v[200:203], a[192:195], a[248:251], v[200:203]\n"
    "ds_read_b128 a[228:231], v18 offset:57872\n"
    "v_mul_f32 v120, s76, v120\n"
    "v_mfma_f32_16x16x32_bf16 v[200:203], a[196:199], a[252:255], v[200:203]\n"
    "ds_read_b128 a[108:111], v18 offset:58416\n"
    "v_mul_f32 v121, s76, v121\n"
    "v_mfma_f32_16x16x32_bf16 v[200:203], a[200:203], a[92:95], v[200:203]\n"
    "ds_read_b128 a[112:115], v19 offset:56160\n"
    "v_mul_f32 v122, s76, v122\n"
    "v_mfma_f32_16x16x32_bf16 v[200:203], a[204:207], a[64:67], v[200:203]\n"
    "ds_read_b128 a[244:247], v18 offset:57952\n"
    "v_mul_f32 v123, s76, v123\n"
    "v_mfma_f32_16x16x32_bf16 v[200:203], a[208:211], a[100:103], v[200:203]\n"
    "s_waitcnt lgkmcnt(5)\n"
    "s_add_u32 s58, s53, 344064\n"
    "buffer_store_dwordx4 v[64:67], v42, s[24:27], s58 offen sc0 sc1\n"
    "ds_read_b128 a[16:19], v18 offset:58496\n"
    "v_mfma_f32_16x16x32_bf16 v[132:135], a[128:131], a[36:39], v[132:135]\n"
    "ds_read_b128 a[20:23], v19 offset:55616\n"
    "v_fma_f32 v160, -v160, s76, v120\n"
    "v_mfma_f32_16x16x32_bf16 v[132:135], a[132:135], a[40:43], v[132:135]\n"
    "s_waitcnt lgkmcnt(6)\n"
    "s_add_u32 s58, s53, 430080\n"
    "buffer_store_dwordx4 v[68:71], v42, s[24:27], s58 offen sc0 sc1\n"
    "v_fma_f32 v161, -v161, s76, v121\n"
    "v_mfma_f32_16x16x32_bf16 v[132:135], a[136:139], a[44:47], v[132:135]\n"
    "v_fma_f32 v162, -v162, s76, v122\n"
    "v_fma_f32 v163, -v163, s76, v123\n"
    "v_mfma_f32_16x16x32_bf16 v[132:135], a[140:143], a[48:51], v[132:135]\n"
    "v_fma_f32 v120, v120, 2.0, -v160\n"
    "v_fma_f32 v121, v121, 2.0, -v161\n"
    "v_mfma_f32_16x16x32_bf16 v[132:135], a[144:147], a[52:55], v[132:135]\n"
    "v_fma_f32 v122, v122, 2.0, -v162\n"
    "v_fma_f32 v123, v123, 2.0, -v163\n"
    "v_mfma_f32_16x16x32_bf16 v[132:135], a[148:151], a[56:59], v[132:135]\n"
    "v_fma_f32 v240, v240, s76, v160\n"
    "v_fma_f32 v241, v241, s76, v161\n"
    "v_mfma_f32_16x16x32_bf16 v[132:135], a[152:155], a[60:63], v[132:135]\n"
    "v_fma_f32 v242, v242, s76, v162\n"
    "v_fma_f32 v243, v243, s76, v163\n"
    "v_pk_fma_f32 v[240:241], v[244:245], s[84:85], v[240:241] op_sel_hi:[1,0,1]\n"
    "v_pk_fma_f32 v[242:243], v[246:247], s[84:85], v[242:243] op_sel_hi:[1,0,1]\n"
    "v_pk_fma_f32 v[160:161], v[164:165], 2.0, v[160:161] op_sel_hi:[1,0,1]\n"
    "v_pk_fma_f32 v[162:163], v[166:167], 2.0, v[162:163] op_sel_hi:[1,0,1]\n"
    "v_pk_fma_f32 v[164:165], v[124:125], 4.0, v[120:121] op_sel_hi:[1,0,1]\n"
    "v_pk_fma_f32 v[166:167], v[126:127], 4.0, v[122:123] op_sel_hi:[1,0,1]\n"
    "v_pk_add_f32 v[120:121], v[120:121], v[124:125]\n"
    "v_pk_add_f32 v[122:123], v[122:123], v[126:127]\n"
    "v_pk_fma_f32 v[240:241], v[204:205], s[82:83], v[240:241] op_sel_hi:[1,0,1]\n"
    "v_pk_fma_f32 v[242:243], v[206:207], s[82:83], v[242:243] op_sel_hi:[1,0,1]\n"
    "v_pk_fma_f32 v[160:161], v[204:205], s[78:79], v[160:161] op_sel_hi:[1,0,1]\n"
    "v_pk_fma_f32 v[162:163], v[206:207], s[78:79], v[162:163] op_sel_hi:[1,0,1]\n"
    "v_pk_fma_f32 v[164:165], v[204:205], s[80:81], v[164:165] op_sel_hi:[1,0,1]\n"
    "v_pk_fma_f32 v[166:167], v[206:207], s[80:81], v[166:167] op_sel_hi:[1,0,1]\n"
    "v_pk_fma_f32 v[120:121], v[204:205], s[76:77], v[120:121] op_sel_hi:[1,0,1]\n"
    "v_pk_fma_f32 v[122:123], v[206:207], s[76:77], v[122:123] op_sel_hi:[1,0,1]\n"
    "v_pk_fma_f32 v[120:121], v[200:201], s[76:77], v[120:121] op_sel_hi:[1,0,1]\n"
    "v_pk_fma_f32 v[122:123], v[202:203], s[76:77], v[122:123] op_sel_hi:[1,0,1]\n"
    "v_mfma_f32_16x16x32_bf16 v[172:175], a[156:159], a[240:243], v[172:175]\n"
    "v_cvt_pk_bf16_f32 v160, v160, v161\n"
    "v_cvt_pk_bf16_f32 v161, v162, v163\n"
    "v_mfma_f32_16x16x32_bf16 v[172:175], a[160:163], a[4:7], v[172:175]\n"
    "v_cvt_pk_bf16_f32 v164, v164, v165\n"
    "v_cvt_pk_bf16_f32 v165, v166, v167\n"
    "v_mfma_f32_16x16x32_bf16 v[172:175], a[164:167], a[8:11], v[172:175]\n"
    "ds_write_b64 v40, v[160:161] offset:25600\n"
    "v_mfma_f32_16x16x32_bf16 v[172:175], a[168:171], a[224:227], v[172:175]\n"
    "v_cvt_pk_bf16_f32 v240, v240, v241\n"
    "v_cvt_pk_bf16_f32 v241, v242, v243\n"
    "v_mfma_f32_16x16x32_bf16 v[172:175], a[172:175], a[232:235], v[172:175]\n"
    "s_waitcnt lgkmcnt(1)\n"
    "ds_write_b64 v40, v[164:165] offset:26624\n"
    "v_mfma_f32_16x16x32_bf16 v[172:175], a[176:179], a[228:231], v[172:175]\n"
    "v_cvt_pk_bf16_f32 v120, v120, v121\n"
    "v_cvt_pk_bf16_f32 v121, v122, v123\n"
    "v_mfma_f32_16x16x32_bf16 v[172:175], a[180:183], a[108:111], v[172:175]\n"
    "ds_write_b64 v40, v[240:241] offset:27648\n"
    "v_mfma_f32_16x16x32_bf16 v[252:255], a[212:215], a[104:107], v[252:255]\n"
    "v_mfma_f32_16x16x32_bf16 v[252:255], a[216:219], a[112:115], v[252:255]\n"
    "ds_write_b64 v40, v[120:121] offset:24576\n"
    "v_mfma_f32_16x16x32_bf16 v[212:215], a[184:187], a[252:255], v[212:215]\n"
    "v_pk_mul_f32 v[132:133], v[132:133], s[76:77] op_sel_hi:[1,0]\n"
    "v_pk_mul_f32 v[134:135], v[134:135], s[76:77] op_sel_hi:[1,0]\n"
    "v_pk_fma_f32 v[172:173], v[172:173], s[76:77], v[132:133] op_sel_hi:[1,0,1] neg_lo:[1,0,0] neg_hi:[1,0,0]\n"
    "v_pk_fma_f32 v[174:175], v[174:175], s[76:77], v[134:135] op_sel_hi:[1,0,1] neg_lo:[1,0,0] neg_hi:[1,0,0]\n"
    "v_pk_fma_f32 v[132:133], v[132:133], 2.0, v[172:173] op_sel_hi:[1,0,1] neg_lo:[0,0,1] neg_hi:[0,0,1]\n"
    "v_pk_fma_f32 v[134:135], v[134:135], 2.0, v[174:175] op_sel_hi:[1,0,1] neg_lo:[0,0,1] neg_hi:[0,0,1]\n"
    "v_mfma_f32_16x16x32_bf16 v[212:215], a[188:191], a[92:95], v[212:215]\n"
    "v_mfma_f32_16x16x32_bf16 v[212:215], a[192:195], a[64:67], v[212:215]\n"
    "v_fma_f32 v252, v252, s76, v172\n"
    "v_fma_f32 v253, v253, s76, v173\n"
    "v_mfma_f32_16x16x32_bf16 v[212:215], a[196:199], a[100:103], v[212:215]\n"
    "s_waitcnt lgkmcnt(0)\n"
    "s_barrier\n"
    "v_mfma_f32_16x16x32_bf16 v[212:215], a[200:203], a[68:71], v[212:215]\n"
    "ds_read_b128 v[72:75], v41 offset:24576\n"
    "ds_read_b128 v[76:79], v41 offset:28672\n"
    "v_mfma_f32_16x16x32_bf16 v[212:215], a[204:207], a[244:247], v[212:215]\n"
    "ds_write_b128 v46, v[172:175] offset:5120\n"
    "v_mfma_f32_16x16x32_bf16 v[212:215], a[208:211], a[16:19], v[212:215]\n"
    "v_fma_f32 v254, v254, s76, v174\n"
    "v_fma_f32 v255, v255, s76, v175\n"
    "v_mfma_f32_16x16x32_bf16 v[128:131], a[128:131], a[32:35], v[128:131]\n"
    "v_mfma_f32_16x16x32_bf16 v[128:131], a[132:135], a[36:39], v[128:131]\n"
    "ds_write_b128 v46, v[132:135] offset:4096\n"
    "v_mfma_f32_16x16x32_bf16 v[128:131], a[136:139], a[40:43], v[128:131]\n"
    "s_waitcnt lgkmcnt(3)\n"
    "s_add_u32 s58, s53, 516096\n"
    "buffer_store_dwordx4 v[72:75], v42, s[24:27], s58 offen sc0 sc1\n"
    "v_fma_f32 v212, v212, s76, v132\n"
    "v_mfma_f32_16x16x32_bf16 v[128:131], a[140:143], a[44:47], v[128:131]\n"
    "v_fma_f32 v213, v213, s76, v133\n"
    "v_fma_f32 v214, v214, s76, v134\n"
    "v_mfma_f32_16x16x32_bf16 v[128:131], a[144:147], a[48:51], v[128:131]\n"
    "s_waitcnt lgkmcnt(2)\n"
    "s_add_u32 s58, s53, 602112\n"
    "buffer_store_dwordx4 v[76:79], v42, s[24:27], s58 offen sc0 sc1\n"
    "v_fma_f32 v215, v215, s76, v135\n"
    "v_mfma_f32_16x16x32_bf16 v[128:131], a[148:151], a[52:55], v[128:131]\n"
    "ds_write_b128 v46, v[252:255] offset:7168\n"
    "v_mfma_f32_16x16x32_bf16 v[128:131], a[152:155], a[56:59], v[128:131]\n"
    "v_mfma_f32_16x16x32_bf16 v[168:171], a[156:159], a[236:239], v[168:171]\n"
    "v_mfma_f32_16x16x32_bf16 v[168:171], a[160:163], a[240:243], v[168:171]\n"
    "ds_write_b128 v46, v[212:215] offset:6144\n"
    "v_mfma_f32_16x16x32_bf16 v[168:171], a[164:167], a[4:7], v[168:171]\n"
    "v_mfma_f32_16x16x32_bf16 v[168:171], a[168:171], a[8:11], v[168:171]\n"
    "v_mfma_f32_16x16x32_bf16 v[168:171], a[172:175], a[224:227], v[168:171]\n"
    "v_mfma_f32_16x16x32_bf16 v[168:171], a[176:179], a[232:235], v[168:171]\n"
    "s_waitcnt lgkmcnt(0)\n"
    "s_barrier\n"
    "v_mfma_f32_16x16x32_bf16 v[168:171], a[180:183], a[228:231], v[168:171]\n"
    "ds_read_b128 v[132:135], v47 offset:4096\n"
    "ds_read_b128 v[172:175], v47 offset:5120\n"
    "v_mfma_f32_16x16x32_bf16 v[248:251], a[212:215], a[72:75], v[248:251]\n"
    "ds_read_b128 v[212:215], v47 offset:6144\n"
    "ds_read_b128 v[252:255], v47 offset:7168\n"
    "v_mfma_f32_16x16x32_bf16 v[248:251], a[216:219], a[20:23], v[248:251]\n"
    "v_mfma_f32_16x16x32_bf16 v[208:211], a[184:187], a[248:251], v[208:211]\n"
    "v_pk_mul_f32 v[128:129], v[128:129], s[76:77] op_sel_hi:[1,0]\n"
    "v_pk_mul_f32 v[130:131], v[130:131], s[76:77] op_sel_hi:[1,0]\n"
    "v_pk_fma_f32 v[168:169], v[168:169], s[76:77], v[128:129] op_sel_hi:[1,0,1] neg_lo:[1,0,0] neg_hi:[1,0,0]\n"
    "v_pk_fma_f32 v[170:171], v[170:171], s[76:77], v[130:131] op_sel_hi:[1,0,1] neg_lo:[1,0,0] neg_hi:[1,0,0]\n"
    "v_pk_fma_f32 v[128:129], v[128:129], 2.0, v[168:169] op_sel_hi:[1,0,1] neg_lo:[0,0,1] neg_hi:[0,0,1]\n"
    "v_pk_fma_f32 v[130:131], v[130:131], 2.0, v[170:171] op_sel_hi:[1,0,1] neg_lo:[0,0,1] neg_hi:[0,0,1]\n"
    "v_mfma_f32_16x16x32_bf16 v[208:211], a[188:191], a[252:255], v[208:211]\n"
    "v_mfma_f32_16x16x32_bf16 v[208:211], a[192:195], a[92:95], v[208:211]\n"
    "v_fma_f32 v248, v248, s76, v168\n"
    "v_fma_f32 v249, v249, s76, v169\n"
    "v_mfma_f32_16x16x32_bf16 v[208:211], a[196:199], a[64:67], v[208:211]\n"
    "v_fma_f32 v250, v250, s76, v170\n"
    "v_fma_f32 v251, v251, s76, v171\n"
    "v_mfma_f32_16x16x32_bf16 v[208:211], a[200:203], a[100:103], v[208:211]\n"
    "s_waitcnt lgkmcnt(0)\n"
    "v_pk_fma_f32 v[248:249], v[252:253], s[84:85], v[248:249] op_sel_hi:[1,0,1]\n"
    "v_pk_fma_f32 v[250:251], v[254:255], s[84:85], v[250:251] op_sel_hi:[1,0,1]\n"
    "v_pk_fma_f32 v[168:169], v[172:173], 2.0, v[168:169] op_sel_hi:[1,0,1]\n"
    "v_pk_fma_f32 v[170:171], v[174:175], 2.0, v[170:171] op_sel_hi:[1,0,1]\n"
    "v_pk_fma_f32 v[172:173], v[132:133], 4.0, v[128:129] op_sel_hi:[1,0,1]\n"
    "v_pk_fma_f32 v[174:175], v[134:135], 4.0, v[130:131] op_sel_hi:[1,0,1]\n"
    "v_pk_add_f32 v[128:129], v[128:129], v[132:133]\n"
    "v_pk_add_f32 v[130:131], v[130:131], v[134:135]\n"
    "v_pk_fma_f32 v[248:249], v[212:213], s[82:83], v[248:249] op_sel_hi:[1,0,1]\n"
    "v_pk_fma_f32 v[250:251], v[214:215], s[82:83], v[250:251] op_sel_hi:[1,0,1]\n"
    "v_pk_fma_f32 v[168:169], v[212:213], s[78:79], v[168:169] op_sel_hi:[1,0,1]\n"
    "v_pk_fma_f32 v[170:171], v[214:215], s[78:79], v[170:171] op_sel_hi:[1,0,1]\n"
    "v_pk_fma_f32 v[172:173], v[212:213], s[80:81], v[172:173] op_sel_hi:[1,0,1]\n"
    "v_pk_fma_f32 v[174:175], v[214:215], s[80:81], v[174:175] op_sel_hi:[1,0,1]\n"
    "v_pk_fma_f32 v[128:129], v[212:213], s[76:77], v[128:129] op_sel_hi:[1,0,1]\n"
    "v_pk_fma_f32 v[130:131], v[214:215], s[76:77], v[130:131] op_sel_hi:[1,0,1]\n"
    "v_mfma_f32_16x16x32_bf16 v[208:211], a[204:207], a[68:71], v[208:211]\n"
    "v_mfma_f32_16x16x32_bf16 v[208:211], a[208:211], a[244:247], v[208:211]\n"
    "s_nop 7\n"
    "v_pk_fma_f32 v[128:129], v[208:209], s[76:77], v[128:129] op_sel_hi:[1,0,1]\n"
    "v_pk_fma_f32 v[130:131], v[210:211], s[76:77], v[130:131] op_sel_hi:[1,0,1]\n"
    "v_cvt_pk_bf16_f32 v168, v168, v169\n"
    "v_cvt_pk_bf16_f32 v169, v170, v171\n"
    "v_cvt_pk_bf16_f32 v172, v172, v173\n"
    "v_cvt_pk_bf16_f32 v173, v174, v175\n"
    "v_cvt_pk_bf16_f32 v248, v248, v249\n"
    "v_cvt_pk_bf16_f32 v249, v250, v251\n"
    "v_cvt_pk_bf16_f32 v128, v128, v129\n"
    "v_cvt_pk_bf16_f32 v129, v130, v131\n"
    "ds_write_b64 v40, v[168:169] offset:33792\n"
    "ds_write_b64 v40, v[172:173] offset:34816\n"
    "ds_write_b64 v40, v[248:249] offset:35840\n"
    "ds_write_b64 v40, v[128:129] offset:32768\n"
    "s_waitcnt lgkmcnt(0)\n"
    "s_barrier\n"
    "ds_read_b128 v[96:99], v41 offset:32768\n"
    "ds_read_b128 v[100:103], v41 offset:36864\n"
    "s_add_u32 s58, s53, 688128\n"
    "s_waitcnt lgkmcnt(1)\n"
    "buffer_store_dwordx4 v[96:99], v42, s[24:27], s58 offen sc0 sc1\n"
    "s_add_u32 s58, s53, 774144\n"
    "s_waitcnt lgkmcnt(0)\n"
    "buffer_store_dwordx4 v[100:103], v42, s[24:27], s58 offen sc0 sc1\n"
    "s_endpgm\n"
    "23:\n"
    "v_mfma_f32_16x16x32_bf16 v[96:99], a[128:131], a[0:3], v[96:99]\n"
    "buffer_load_dwordx4 a[212:215], v5, s[28:31], s50 offen offset:1024 sc1\n"
    "ds_read_b128 a[64:67], v18 offset:50256\n"
    "v_mfma_f32_16x16x32_bf16 v[96:99], a[132:135], a[4:7], v[96:99]\n"
    "v_add_u32 v46, s48, v5\n"
    "v_add_u32 v47, s49, v5\n"
    "v_mfma_f32_16x16x32_bf16 v[96:99], a[136:139], a[8:11], v[96:99]\n"
    "s_mov_b32 s76, 0x3f360b61\n"
    "buffer_load_dwordx4 a[216:219], v5, s[28:31], s50 offen offset:2048 sc1\n"
    "s_mov_b32 s78, 0x3eb60b61\n"
    "v_mfma_f32_16x16x32_bf16 v[96:99], a[140:143], a[12:15], v[96:99]\n"
    "s_mov_b32 s80, 0x3e360b61\n"
    "s_mov_b32 s82, 0x3db60b61\n"
    "s_mov_b32 s84, 0x41000000\n"
    "v_mfma_f32_16x16x32_bf16 v[96:99], a[144:147], a[16:19], v[96:99]\n"
    "s_lshl_b32 s72, s43, 5\n"
    "v_xor_b32 v40, s72, v40\n"
    "s_lshl_b32 s72, s42, 10\n"
    "v_mfma_f32_16x16x32_bf16 v[96:99], a[148:151], a[20:23], v[96:99]\n"
    "v_add_u32 v41, s72, v41\n"
    "ds_read_b128 a[68:71], v18 offset:50800\n"
    "v_mfma_f32_16x16x32_bf16 v[96:99], a[152:155], a[24:27], v[96:99]\n"
    "ds_read_b128 a[72:75], v18 offset:51344\n"
    "v_mfma_f32_16x16x32_bf16 v[100:103], a[128:131], a[4:7], v[100:103]\n"
    "ds_read_b128 a[76:79], v18 offset:51888\n"
    "v_mfma_f32_16x16x32_bf16 v[100:103], a[132:135], a[8:11], v[100:103]\n"
    "ds_read_b128 a[80:83], v18 offset:52432\n"
    "v_mfma_f32_16x16x32_bf16 v[100:103], a[136:139], a[12:15], v[100:103]\n"
    "ds_read_b128 a[84:87], v18 offset:52976\n"
    "v_mfma_f32_16x16x32_bf16 v[100:103], a[140:143], a[16:19], v[100:103]\n"
    "ds_read_b128 a[88:91], v18 offset:53520\n"
    "v_mfma_f32_16x16x32_bf16 v[100:103], a[144:147], a[20:23], v[100:103]\n"
    "ds_read_b128 a[92:95], v18 offset:54064\n"
    "v_mfma_f32_16x16x32_bf16 v[100:103], a[148:151], a[24:27], v[100:103]\n"
    "ds_read_b128 a[96:99], v19 offset:50176\n"
    "v_mfma_f32_16x16x32_bf16 v[100:103], a[152:155], a[28:31], v[100:103]\n"
    "ds_read_b128 a[100:103], v19 offset:51264\n"
    "v_mfma_f32_16x16x32_bf16 v[104:107], a[128:131], a[8:11], v[104:107]\n"
    "ds_read_b128 a[104:107], v18 offset:50336\n"
    "v_mfma_f32_16x16x32_bf16 v[104:107], a[132:135], a[12:15], v[104:107]\n"
    "ds_read_b128 a[108:111], v18 offset:50880\n"
    "v_mfma_f32_16x16x32_bf16 v[104:107], a[136:139], a[16:19], v[104:107]\n"
    "ds_read_b128 a[112:115], v18 offset:51424\n"
    "v_mfma_f32_16x16x32_bf16 v[104:107], a[140:143], a[20:23], v[104:107]\n"
    "ds_read_b128 a[116:119], v18 offset:51968\n"
    "v_mfma_f32_16x16x32_bf16 v[104:107], a[144:147], a[24:27], v[104:107]\n"
    "ds_read_b128 a[120:123], v18 offset:52512\n"
    "v_mfma_f32_16x16x32_bf16 v[104:107], a[148:151], a[28:31], v[104:107]\n"
    "ds_read_b128 a[124:127], v18 offset:53056\n"
    "v_mfma_f32_16x16x32_bf16 v[104:107], a[152:155], a[32:35], v[104:107]\n"
    "ds_read_b128 a[220:223], v18 offset:53600\n"
    "v_mfma_f32_16x16x32_bf16 v[108:111], a[128:131], a[12:15], v[108:111]\n"
    "ds_read_b128 a[224:227], v19 offset:50720\n"
    "v_mfma_f32_16x16x32_bf16 v[108:111], a[132:135], a[16:19], v[108:111]\n"
    "ds_read_b128 a[228:231], v19 offset:51808\n"
    "v_mfma_f32_16x16x32_bf16 v[108:111], a[136:139], a[20:23], v[108:111]\n"
    "ds_read_b128 a[232:235], v18 offset:54144\n"
    "v_mfma_f32_16x16x32_bf16 v[108:111], a[140:143], a[24:27], v[108:111]\n"
    "ds_read_b128 a[236:239], v18 offset:54608\n"
    "v_mfma_f32_16x16x32_bf16 v[108:111], a[144:147], a[28:31], v[108:111]\n"
    "ds_read_b128 a[240:243], v19 offset:52352\n"
    "v_mfma_f32_16x16x32_bf16 v[108:111], a[148:151], a[32:35], v[108:111]\n"
    "ds_read_b128 a[244:247], v18 offset:54688\n"
    "v_mfma_f32_16x16x32_bf16 v[108:111], a[152:155], a[36:39], v[108:111]\n"
    "s_waitcnt vmcnt(9) lgkmcnt(9)\n"
    "ds_read_b128 a[248:251], v18 offset:55152\n"
    "v_mfma_f32_16x16x32_bf16 v[136:139], a[156:159], a[64:67], v[136:139]\n"
    "v_mfma_f32_16x16x32_bf16 v[136:139], a[160:163], a[68:71], v[136:139]\n"
    "v_mfma_f32_16x16x32_bf16 v[136:139], a[164:167], a[72:75], v[136:139]\n"
    "v_mfma_f32_16x16x32_bf16 v[136:139], a[168:171], a[76:79], v[136:139]\n"
    "v_mfma_f32_16x16x32_bf16 v[136:139], a[172:175], a[80:83], v[136:139]\n"
    "v_mfma_f32_16x16x32_bf16 v[136:139], a[176:179], a[84:87], v[136:139]\n"
    "v_mfma_f32_16x16x32_bf16 v[136:139], a[180:183], a[88:91], v[136:139]\n"
    "v_mfma_f32_16x16x32_bf16 v[140:143], a[156:159], a[68:71], v[140:143]\n"
    "v_mfma_f32_16x16x32_bf16 v[140:143], a[160:163], a[72:75], v[140:143]\n"
    "v_mfma_f32_16x16x32_bf16 v[140:143], a[164:167], a[76:79], v[140:143]\n"
    "v_mul_f32 v96, s76, v96\n"
    "v_mul_f32 v97, s76, v97\n"
    "v_mfma_f32_16x16x32_bf16 v[140:143], a[168:171], a[80:83], v[140:143]\n"
    "v_mul_f32 v98, s76, v98\n"
    "v_mul_f32 v99, s76, v99\n"
    "v_mfma_f32_16x16x32_bf16 v[140:143], a[172:175], a[84:87], v[140:143]\n"
    "v_fma_f32 v136, -v136, s76, v96\n"
    "v_fma_f32 v137, -v137, s76, v97\n"
    "v_mfma_f32_16x16x32_bf16 v[140:143], a[176:179], a[88:91], v[140:143]\n"
    "v_fma_f32 v138, -v138, s76, v98\n"
    "v_fma_f32 v139, -v139, s76, v99\n"
    "v_mfma_f32_16x16x32_bf16 v[140:143], a[180:183], a[92:95], v[140:143]\n"
    "s_waitcnt vmcnt(0) lgkmcnt(0)\n"
    "v_fma_f32 v96, v96, 2.0, -v136\n"
    "v_fma_f32 v97, v97, 2.0, -v137\n"
    "v_mfma_f32_16x16x32_bf16 v[216:219], a[212:215], a[96:99], v[216:219]\n"
    "v_fma_f32 v98, v98, 2.0, -v138\n"
    "v_fma_f32 v99, v99, 2.0, -v139\n"
    "v_mfma_f32_16x16x32_bf16 v[216:219], a[216:219], a[100:103], v[216:219]\n"
    "v_mfma_f32_16x16x32_bf16 v[176:179], a[184:187], a[104:107], v[176:179]\n"
    "ds_write_b128 v46, v[136:139] offset:1024\n"
    "v_mfma_f32_16x16x32_bf16 v[176:179], a[188:191], a[108:111], v[176:179]\n"
    "v_mul_f32 v100, s76, v100\n"
    "v_mul_f32 v101, s76, v101\n"
    "v_mfma_f32_16x16x32_bf16 v[176:179], a[192:195], a[112:115], v[176:179]\n"
    "v_fma_f32 v216, v216, s82, v136\n"
    "v_fma_f32 v217, v217, s82, v137\n"
    "v_mfma_f32_16x16x32_bf16 v[176:179], a[196:199], a[116:119], v[176:179]\n"
    "ds_write_b128 v46, v[96:99] offset:0\n"
    "v_mfma_f32_16x16x32_bf16 v[176:179], a[200:203], a[120:123], v[176:179]\n"
    "v_fma_f32 v218, v218, s82, v138\n"
    "v_fma_f32 v219, v219, s82, v139\n"
    "v_mfma_f32_16x16x32_bf16 v[176:179], a[204:207], a[124:127], v[176:179]\n"
    "v_mul_f32 v102, s76, v102\n"
    "v_mul_f32 v103, s76, v103\n"
    "v_mfma_f32_16x16x32_bf16 v[176:179], a[208:211], a[220:223], v[176:179]\n"
    "ds_write_b128 v46, v[216:219] offset:3072\n"
    "v_mfma_f32_16x16x32_bf16 v[220:223], a[212:215], a[224:227], v[220:223]\n"
    "v_fma_f32 v140, -v140, s76, v100\n"
    "v_fma_f32 v141, -v141, s76, v101\n"
    "v_mfma_f32_16x16x32_bf16 v[220:223], a[216:219], a[228:231], v[220:223]\n"
    "v_fma_f32 v142, -v142, s76, v102\n"
    "v_fma_f32 v143, -v143, s76, v103\n"
    "v_mfma_f32_16x16x32_bf16 v[180:183], a[184:187], a[108:111], v[180:183]\n"
    "v_fma_f32 v100, v100, 2.0, -v140\n"
    "v_fma_f32 v101, v101, 2.0, -v141\n"
    "v_mfma_f32_16x16x32_bf16 v[180:183], a[188:191], a[112:115], v[180:183]\n"
    "v_fma_f32 v102, v102, 2.0, -v142\n"
    "v_fma_f32 v103, v103, 2.0, -v143\n"
    "v_mfma_f32_16x16x32_bf16 v[180:183], a[192:195], a[116:119], v[180:183]\n"
    "ds_write_b128 v46, v[176:179] offset:2048\n"
    "v_mfma_f32_16x16x32_bf16 v[180:183], a[196:199], a[120:123], v[180:183]\n"
    "v_mfma_f32_16x16x32_bf16 v[180:183], a[200:203], a[124:127], v[180:183]\n"
    "v_mfma_f32_16x16x32_bf16 v[180:183], a[204:207], a[220:223], v[180:183]\n"
    "v_mfma_f32_16x16x32_bf16 v[180:183], a[208:211], a[232:235], v[180:183]\n"
    "v_mfma_f32_16x16x32_bf16 v[144:147], a[156:159], a[72:75], v[144:147]\n"
    "v_mfma_f32_16x16x32_bf16 v[144:147], a[160:163], a[76:79], v[144:147]\n"
    "v_mfma_f32_16x16x32_bf16 v[144:147], a[164:167], a[80:83], v[144:147]\n"
    "v_mfma_f32_16x16x32_bf16 v[144:147], a[168:171], a[84:87], v[144:147]\n"
    "s_waitcnt lgkmcnt(0)\n"
    "s_barrier\n"
    "v_mfma_f32_16x16x32_bf16 v[144:147], a[172:175], a[88:91], v[144:147]\n"
    "ds_read_b128 v[96:99], v47 offset:0\n"
    "ds_read_b128 v[136:139], v47 offset:1024\n"
    "v_mfma_f32_16x16x32_bf16 v[144:147], a[176:179], a[92:95], v[144:147]\n"
    "ds_read_b128 v[176:179], v47 offset:2048\n"
    "ds_read_b128 v[216:219], v47 offset:3072\n"
    "v_mfma_f32_16x16x32_bf16 v[144:147], a[180:183], a[236:239], v[144:147]\n"
    "ds_read_b128 a[252:255], v19 offset:52896\n"
    "v_mfma_f32_16x16x32_bf16 v[224:227], a[212:215], a[100:103], v[224:227]\n"
    "ds_read_b128 a[0:3], v18 offset:55232\n"
    "v_mfma_f32_16x16x32_bf16 v[224:227], a[216:219], a[240:243], v[224:227]\n"
    "ds_read_b128 a[4:7], v18 offset:55696\n"
    "v_mfma_f32_16x16x32_bf16 v[184:187], a[184:187], a[112:115], v[184:187]\n"
    "ds_read_b128 a[8:11], v19 offset:53440\n"
    "v_pk_mul_f32 v[104:105], v[104:105], s[76:77] op_sel_hi:[1,0]\n"
    "v_pk_mul_f32 v[106:107], v[106:107], s[76:77] op_sel_hi:[1,0]\n"
    "v_pk_fma_f32 v[144:145], v[144:145], s[76:77], v[104:105] op_sel_hi:[1,0,1] neg_lo:[1,0,0] neg_hi:[1,0,0]\n"
    "v_pk_fma_f32 v[146:147], v[146:147], s[76:77], v[106:107] op_sel_hi:[1,0,1] neg_lo:[1,0,0] neg_hi:[1,0,0]\n"
    "v_pk_fma_f32 v[104:105], v[104:105], 2.0, v[144:145] op_sel_hi:[1,0,1] neg_lo:[0,0,1] neg_hi:[0,0,1]\n"
    "v_pk_fma_f32 v[106:107], v[106:107], 2.0, v[146:147] op_sel_hi:[1,0,1] neg_lo:[0,0,1] neg_hi:[0,0,1]\n"
    "v_mfma_f32_16x16x32_bf16 v[184:187], a[188:191], a[116:119], v[184:187]\n"
    "v_mfma_f32_16x16x32_bf16 v[184:187], a[192:195], a[120:123], v[184:187]\n"
    "s_waitcnt lgkmcnt(4)\n"
    "ds_write_b128 v46, v[144:147] offset:5120\n"
    "v_pk_fma_f32 v[216:217], v[220:221], s[76:77], v[216:217] op_sel_hi:[1,0,1]\n"
    "v_pk_fma_f32 v[218:219], v[222:223], s[76:77], v[218:219] op_sel_hi:[1,0,1]\n"
    "v_pk_fma_f32 v[216:217], v[140:141], s[84:85], v[216:217] op_sel_hi:[1,0,1]\n"
    "v_pk_fma_f32 v[218:219], v[142:143], s[84:85], v[218:219] op_sel_hi:[1,0,1]\n"
    "v_pk_fma_f32 v[136:137], v[140:141], 2.0, v[136:137] op_sel_hi:[1,0,1]\n"
    "v_pk_fma_f32 v[138:139], v[142:143], 2.0, v[138:139] op_sel_hi:[1,0,1]\n"
    "v_pk_fma_f32 v[96:97], v[100:101], 4.0, v[96:97] op_sel_hi:[1,0,1]\n"
    "v_pk_fma_f32 v[98:99], v[102:103], 4.0, v[98:99] op_sel_hi:[1,0,1]\n"
    "v_pk_add_f32 v[176:177], v[176:177], v[100:101]\n"
    "v_pk_add_f32 v[178:179], v[178:179], v[102:103]\n"
    "v_pk_fma_f32 v[176:177], v[180:181], s[76:77], v[176:177] op_sel_hi:[1,0,1]\n"
    "v_pk_fma_f32 v[178:179], v[182:183], s[76:77], v[178:179] op_sel_hi:[1,0,1]\n"
    "v_pk_fma_f32 v[136:137], v[180:181], s[78:79], v[136:137] op_sel_hi:[1,0,1]\n"
    "v_pk_fma_f32 v[138:139], v[182:183], s[78:79], v[138:139] op_sel_hi:[1,0,1]\n"
    "v_pk_fma_f32 v[96:97], v[180:181], s[80:81], v[96:97] op_sel_hi:[1,0,1]\n"
    "v_pk_fma_f32 v[98:99], v[182:183], s[80:81], v[98:99] op_sel_hi:[1,0,1]\n"
    "v_pk_fma_f32 v[216:217], v[180:181], s[82:83], v[216:217] op_sel_hi:[1,0,1]\n"
    "v_pk_fma_f32 v[218:219], v[182:183], s[82:83], v[218:219] op_sel_hi:[1,0,1]\n"
    "v_mfma_f32_16x16x32_bf16 v[184:187], a[196:199], a[124:127], v[184:187]\n"
    "v_fma_f32 v224, v224, s82, v144\n"
    "v_fma_f32 v225, v225, s82, v145\n"
    "v_mfma_f32_16x16x32_bf16 v[184:187], a[200:203], a[220:223], v[184:187]\n"
    "v_fma_f32 v226, v226, s82, v146\n"
    "v_fma_f32 v227, v227, s82, v147\n"
    "v_mfma_f32_16x16x32_bf16 v[184:187], a[204:207], a[232:235], v[184:187]\n"
    "ds_write_b128 v46, v[104:107] offset:4096\n"
    "v_mfma_f32_16x16x32_bf16 v[184:187], a[208:211], a[244:247], v[184:187]\n"
    "v_cvt_pk_bf16_f32 v136, v136, v137\n"
    "v_cvt_pk_bf16_f32 v137, v138, v139\n"
    "v_mfma_f32_16x16x32_bf16 v[148:151], a[156:159], a[76:79], v[148:151]\n"
    "v_cvt_pk_bf16_f32 v96, v96, v97\n"
    "v_cvt_pk_bf16_f32 v97, v98, v99\n"
    "v_mfma_f32_16x16x32_bf16 v[148:151], a[160:163], a[80:83], v[148:151]\n"
    "ds_write_b128 v46, v[224:227] offset:7168\n"
    "v_mfma_f32_16x16x32_bf16 v[148:151], a[164:167], a[84:87], v[148:151]\n"
    "v_cvt_pk_bf16_f32 v216, v216, v217\n"
    "v_cvt_pk_bf16_f32 v217, v218, v219\n"
    "v_mfma_f32_16x16x32_bf16 v[148:151], a[168:171], a[88:91], v[148:151]\n"
    "v_cvt_pk_bf16_f32 v176, v176, v177\n"
    "v_cvt_pk_bf16_f32 v177, v178, v179\n"
    "v_mfma_f32_16x16x32_bf16 v[148:151], a[172:175], a[92:95], v[148:151]\n"
    "ds_write_b128 v46, v[184:187] offset:6144\n"
    "v_mfma_f32_16x16x32_bf16 v[148:151], a[176:179], a[236:239], v[148:151]\n"
    "v_mfma_f32_16x16x32_bf16 v[148:151], a[180:183], a[248:251], v[148:151]\n"
    "v_mfma_f32_16x16x32_bf16 v[228:231], a[212:215], a[228:231], v[228:231]\n"
    "s_waitcnt lgkmcnt(3)\n"
    "ds_write_b64 v40, v[136:137] offset:5120\n"
    "v_mfma_f32_16x16x32_bf16 v[228:231], a[216:219], a[252:255], v[228:231]\n"
    "v_mfma_f32_16x16x32_bf16 v[188:191], a[184:187], a[116:119], v[188:191]\n"
    "ds_write_b64 v40, v[96:97] offset:6144\n"
    "v_mfma_f32_16x16x32_bf16 v[188:191], a[188:191], a[120:123], v[188:191]\n"
    "v_mul_f32 v108, s76, v108\n"
    "v_mul_f32 v109, s76, v109\n"
    "v_mfma_f32_16x16x32_bf16 v[188:191], a[192:195], a[124:127], v[188:191]\n"
    "ds_write_b64 v40, v[216:217] offset:7168\n"
    "v_mfma_f32_16x16x32_bf16 v[188:191], a[196:199], a[220:223], v[188:191]\n"
    "v_mul_f32 v110, s76, v110\n"
    "v_mul_f32 v111, s76, v111\n"
    "v_mfma_f32_16x16x32_bf16 v[188:191], a[200:203], a[232:235], v[188:191]\n"
    "ds_write_b64 v40, v[176:177] offset:4096\n"
    "v_mfma_f32_16x16x32_bf16 v[188:191], a[204:207], a[244:247], v[188:191]\n"
    "v_fma_f32 v148, -v148, s76, v108\n"
    "v_fma_f32 v149, -v149, s76, v109\n"
    "v_mfma_f32_16x16x32_bf16 v[188:191], a[208:211], a[0:3], v[188:191]\n"
    "v_fma_f32 v150, -v150, s76, v110\n"
    "v_fma_f32 v151, -v151, s76, v111\n"
    "v_mfma_f32_16x16x32_bf16 v[112:115], a[128:131], a[16:19], v[112:115]\n"
    "v_fma_f32 v108, v108, 2.0, -v148\n"
    "v_fma_f32 v109, v109, 2.0, -v149\n"
    "v_mfma_f32_16x16x32_bf16 v[112:115], a[132:135], a[20:23], v[112:115]\n"
    "s_waitcnt lgkmcnt(0)\n"
    "s_barrier\n"
    "v_mfma_f32_16x16x32_bf16 v[112:115], a[136:139], a[24:27], v[112:115]\n"
    "ds_read_b128 v[104:107], v47 offset:4096\n"
    "ds_read_b128 v[144:147], v47 offset:5120\n"
    "v_mfma_f32_16x16x32_bf16 v[112:115], a[140:143], a[28:31], v[112:115]\n"
    "ds_read_b128 v[184:187], v47 offset:6144\n"
    "ds_read_b128 v[224:227], v47 offset:7168\n"
    "v_mfma_f32_16x16x32_bf16 v[112:115], a[144:147], a[32:35], v[112:115]\n"
    "ds_read_b128 v[48:51], v41 offset:0\n"
    "ds_read_b128 v[52:55], v41 offset:4096\n"
    "v_mfma_f32_16x16x32_bf16 v[112:115], a[148:151], a[36:39], v[112:115]\n"
    "ds_read_b128 a[12:15], v18 offset:55776\n"
    "v_fma_f32 v110, v110, 2.0, -v150\n"
    "v_mfma_f32_16x16x32_bf16 v[112:115], a[152:155], a[40:43], v[112:115]\n"
    "ds_read_b128 a[64:67], v18 offset:56240\n"
    "v_fma_f32 v111, v111, 2.0, -v151\n"
    "v_mfma_f32_16x16x32_bf16 v[152:155], a[156:159], a[80:83], v[152:155]\n"
    "ds_read_b128 a[68:71], v19 offset:53984\n"
    "v_mfma_f32_16x16x32_bf16 v[152:155], a[160:163], a[84:87], v[152:155]\n"
    "ds_read_b128 a[96:99], v18 offset:56320\n"
    "v_mfma_f32_16x16x32_bf16 v[152:155], a[164:167], a[88:91], v[152:155]\n"
    "s_waitcnt lgkmcnt(5)\n"
    "s_add_u32 s58, s53, 0\n"
    "buffer_store_dwordx4 v[48:51], v42, s[24:27], s58 offen sc0 sc1\n"
    "v_pk_fma_f32 v[224:225], v[228:229], s[76:77], v[224:225] op_sel_hi:[1,0,1]\n"
    "v_pk_fma_f32 v[226:227], v[230:231], s[76:77], v[226:227] op_sel_hi:[1,0,1]\n"
    "v_pk_fma_f32 v[224:225], v[148:149], s[84:85], v[224:225] op_sel_hi:[1,0,1]\n"
    "v_pk_fma_f32 v[226:227], v[150:151], s[84:85], v[226:227] op_sel_hi:[1,0,1]\n"
    "v_pk_fma_f32 v[144:145], v[148:149], 2.0, v[144:145] op_sel_hi:[1,0,1]\n"
    "v_pk_fma_f32 v[146:147], v[150:151], 2.0, v[146:147] op_sel_hi:[1,0,1]\n"
    "v_pk_fma_f32 v[104:105], v[108:109], 4.0, v[104:105] op_sel_hi:[1,0,1]\n"
    "v_pk_fma_f32 v[106:107], v[110:111], 4.0, v[106:107] op_sel_hi:[1,0,1]\n"
    "v_pk_add_f32 v[184:185], v[184:185], v[108:109]\n"
    "v_pk_add_f32 v[186:187], v[186:187], v[110:111]\n"
    "v_pk_fma_f32 v[184:185], v[188:189], s[76:77], v[184:185] op_sel_hi:[1,0,1]\n"
    "v_pk_fma_f32 v[186:187], v[190:191], s[76:77], v[186:187] op_sel_hi:[1,0,1]\n"
    "v_pk_fma_f32 v[144:145], v[188:189], s[78:79], v[144:145] op_sel_hi:[1,0,1]\n"
    "v_pk_fma_f32 v[146:147], v[190:191], s[78:79], v[146:147] op_sel_hi:[1,0,1]\n"
    "v_pk_fma_f32 v[104:105], v[188:189], s[80:81], v[104:105] op_sel_hi:[1,0,1]\n"
    "v_pk_fma_f32 v[106:107], v[190:191], s[80:81], v[106:107] op_sel_hi:[1,0,1]\n"
    "v_pk_fma_f32 v[224:225], v[188:189], s[82:83], v[224:225] op_sel_hi:[1,0,1]\n"
    "v_pk_fma_f32 v[226:227], v[190:191], s[82:83], v[226:227] op_sel_hi:[1,0,1]\n"
    "v_mfma_f32_16x16x32_bf16 v[152:155], a[168:171], a[92:95], v[152:155]\n"
    "v_cvt_pk_bf16_f32 v144, v144, v145\n"
    "v_cvt_pk_bf16_f32 v145, v146, v147\n"
    "v_mfma_f32_16x16x32_bf16 v[152:155], a[172:175], a[236:239], v[152:155]\n"
    "s_waitcnt lgkmcnt(4)\n"
    "s_add_u32 s58, s53, 86016\n"
    "buffer_store_dwordx4 v[52:55], v42, s[24:27], s58 offen sc0 sc1\n"
    "v_cvt_pk_bf16_f32 v104, v104, v105\n"
    "v_mfma_f32_16x16x32_bf16 v[152:155], a[176:179], a[248:251], v[152:155]\n"
    "ds_write_b64 v40, v[144:145] offset:13312\n"
    "v_mfma_f32_16x16x32_bf16 v[152:155], a[180:183], a[4:7], v[152:155]\n"
    "v_cvt_pk_bf16_f32 v105, v106, v107\n"
    "v_cvt_pk_bf16_f32 v224, v224, v225\n"
    "v_mfma_f32_16x16x32_bf16 v[232:235], a[212:215], a[240:243], v[232:235]\n"
    "v_cvt_pk_bf16_f32 v225, v226, v227\n"
    "v_cvt_pk_bf16_f32 v184, v184, v185\n"
    "v_mfma_f32_16x16x32_bf16 v[232:235], a[216:219], a[8:11], v[232:235]\n"
    "ds_write_b64 v40, v[104:105] offset:14336\n"
    "v_mfma_f32_16x16x32_bf16 v[192:195], a[184:187], a[120:123], v[192:195]\n"
    "v_mul_f32 v112, s76, v112\n"
    "v_mul_f32 v113, s76, v113\n"
    "v_mfma_f32_16x16x32_bf16 v[192:195], a[188:191], a[124:127], v[192:195]\n"
    "v_mul_f32 v114, s76, v114\n"
    "v_mul_f32 v115, s76, v115\n"
    "v_mfma_f32_16x16x32_bf16 v[192:195], a[192:195], a[220:223], v[192:195]\n"
    "v_fma_f32 v152, -v152, s76, v112\n"
    "v_fma_f32 v153, -v153, s76, v113\n"
    "v_mfma_f32_16x16x32_bf16 v[192:195], a[196:199], a[232:235], v[192:195]\n"
    "v_fma_f32 v154, -v154, s76, v114\n"
    "v_fma_f32 v155, -v155, s76, v115\n"
    "v_mfma_f32_16x16x32_bf16 v[192:195], a[200:203], a[244:247], v[192:195]\n"
    "v_fma_f32 v112, v112, 2.0, -v152\n"
    "v_fma_f32 v113, v113, 2.0, -v153\n"
    "v_mfma_f32_16x16x32_bf16 v[192:195], a[204:207], a[0:3], v[192:195]\n"
    "s_waitcnt lgkmcnt(2)\n"
    "v_fma_f32 v114, v114, 2.0, -v154\n"
    "v_fma_f32 v115, v115, 2.0, -v155\n"
    "v_mfma_f32_16x16x32_bf16 v[192:195], a[208:211], a[12:15], v[192:195]\n"
    "ds_write_b64 v40, v[224:225] offset:15360\n"
    "v_mfma_f32_16x16x32_bf16 v[116:119], a[128:131], a[20:23], v[116:119]\n"
    "ds_write_b128 v46, v[152:155] offset:9216\n"
    "v_mfma_f32_16x16x32_bf16 v[116:119], a[132:135], a[24:27], v[116:119]\n"
    "v_cvt_pk_bf16_f32 v185, v186, v187\n"
    "v_fma_f32 v232, v232, s82, v152\n"
    "v_mfma_f32_16x16x32_bf16 v[116:119], a[136:139], a[28:31], v[116:119]\n"
    "v_fma_f32 v233, v233, s82, v153\n"
    "v_fma_f32 v234, v234, s82, v154\n"
    "v_mfma_f32_16x16x32_bf16 v[116:119], a[140:143], a[32:35], v[116:119]\n"
    "ds_write_b128 v46, v[112:115] offset:8192\n"
    "v_mfma_f32_16x16x32_bf16 v[116:119], a[144:147], a[36:39], v[116:119]\n"
    "v_fma_f32 v235, v235, s82, v155\n"
    "v_mfma_f32_16x16x32_bf16 v[116:119], a[148:151], a[40:43], v[116:119]\n"
    "v_mfma_f32_16x16x32_bf16 v[116:119], a[152:155], a[44:47], v[116:119]\n"
    "ds_write_b64 v40, v[184:185] offset:12288\n"
    "v_mfma_f32_16x16x32_bf16 v[156:159], a[156:159], a[84:87], v[156:159]\n"
    "ds_write_b128 v46, v[232:235] offset:11264\n"
    "v_mfma_f32_16x16x32_bf16 v[156:159], a[160:163], a[88:91], v[156:159]\n"
    "v_mfma_f32_16x16x32_bf16 v[156:159], a[164:167], a[92:95], v[156:159]\n"
    "v_mfma_f32_16x16x32_bf16 v[156:159], a[168:171], a[236:239], v[156:159]\n"
    "ds_write_b128 v46, v[192:195] offset:10240\n"
    "v_mfma_f32_16x16x32_bf16 v[156:159], a[172:175], a[248:251], v[156:159]\n"
    "v_mfma_f32_16x16x32_bf16 v[156:159], a[176:179], a[4:7], v[156:159]\n"
    "v_mfma_f32_16x16x32_bf16 v[156:159], a[180:183], a[64:67], v[156:159]\n"
    "v_mfma_f32_16x16x32_bf16 v[236:239], a[212:215], a[252:255], v[236:239]\n"
    "s_waitcnt lgkmcnt(0)\n"
    "s_barrier\n"
    "v_mfma_f32_16x16x32_bf16 v[236:239], a[216:219], a[68:71], v[236:239]\n"
    "ds_read_b128 v[112:115], v47 offset:8192\n"
    "ds_read_b128 v[152:155], v47 offset:9216\n"
    "v_mfma_f32_16x16x32_bf16 v[196:199], a[184:187], a[124:127], v[196:199]\n"
    "ds_read_b128 v[192:195], v47 offset:10240\n"
    "ds_read_b128 v[232:235], v47 offset:11264\n"
    "v_mfma_f32_16x16x32_bf16 v[196:199], a[188:191], a[220:223], v[196:199]\n"
    "ds_read_b128 v[56:59], v41 offset:8192\n"
    "ds_read_b128 v[60:63], v41 offset:12288\n"
    "v_mfma_f32_16x16x32_bf16 v[196:199], a[192:195], a[232:235], v[196:199]\n"
    "ds_read_b128 a[104:107], v18 offset:56784\n"
    "v_mul_f32 v116, s76, v116\n"
    "v_mfma_f32_16x16x32_bf16 v[196:199], a[196:199], a[244:247], v[196:199]\n"
    "ds_read_b128 a[224:227], v19 offset:54528\n"
    "v_mul_f32 v117, s76, v117\n"
    "v_mfma_f32_16x16x32_bf16 v[196:199], a[200:203], a[0:3], v[196:199]\n"
    "ds_read_b128 a[108:111], v18 offset:56864\n"
    "v_mul_f32 v118, s76, v118\n"
    "v_mfma_f32_16x16x32_bf16 v[196:199], a[204:207], a[12:15], v[196:199]\n"
    "ds_read_b128 a[72:75], v18 offset:57328\n"
    "v_mul_f32 v119, s76, v119\n"
    "v_mfma_f32_16x16x32_bf16 v[196:199], a[208:211], a[96:99], v[196:199]\n"
    "s_waitcnt lgkmcnt(5)\n"
    "s_add_u32 s58, s53, 172032\n"
    "buffer_store_dwordx4 v[56:59], v42, s[24:27], s58 offen sc0 sc1\n"
    "ds_read_b128 a[100:103], v19 offset:55072\n"
    "v_mfma_f32_16x16x32_bf16 v[120:123], a[128:131], a[24:27], v[120:123]\n"
    "ds_read_b128 a[112:115], v18 offset:57408\n"
    "v_fma_f32 v156, -v156, s76, v116\n"
    "v_mfma_f32_16x16x32_bf16 v[120:123], a[132:135], a[28:31], v[120:123]\n"
    "s_waitcnt lgkmcnt(6)\n"
    "s_add_u32 s58, s53, 258048\n"
    "buffer_store_dwordx4 v[60:63], v42, s[24:27], s58 offen sc0 sc1\n"
    "v_fma_f32 v157, -v157, s76, v117\n"
    "v_mfma_f32_16x16x32_bf16 v[120:123], a[136:139], a[32:35], v[120:123]\n"
    "v_fma_f32 v158, -v158, s76, v118\n"
    "v_fma_f32 v159, -v159, s76, v119\n"
    "v_mfma_f32_16x16x32_bf16 v[120:123], a[140:143], a[36:39], v[120:123]\n"
    "v_fma_f32 v116, v116, 2.0, -v156\n"
    "v_fma_f32 v117, v117, 2.0, -v157\n"
    "v_mfma_f32_16x16x32_bf16 v[120:123], a[144:147], a[40:43], v[120:123]\n"
    "v_fma_f32 v118, v118, 2.0, -v158\n"
    "v_fma_f32 v119, v119, 2.0, -v159\n"
    "v_pk_fma_f32 v[232:233], v[236:237], s[76:77], v[232:233] op_sel_hi:[1,0,1]\n"
    "v_pk_fma_f32 v[234:235], v[238:239], s[76:77], v[234:235] op_sel_hi:[1,0,1]\n"
    "v_pk_fma_f32 v[232:233], v[156:157], s[84:85], v[232:233] op_sel_hi:[1,0,1]\n"
    "v_pk_fma_f32 v[234:235], v[158:159], s[84:85], v[234:235] op_sel_hi:[1,0,1]\n"
    "v_pk_fma_f32 v[152:153], v[156:157], 2.0, v[152:153] op_sel_hi:[1,0,1]\n"
    "v_pk_fma_f32 v[154:155], v[158:159], 2.0, v[154:155] op_sel_hi:[1,0,1]\n"
    "v_pk_fma_f32 v[112:113], v[116:117], 4.0, v[112:113] op_sel_hi:[1,0,1]\n"
    "v_pk_fma_f32 v[114:115], v[118:119], 4.0, v[114:115] op_sel_hi:[1,0,1]\n"
    "v_pk_add_f32 v[192:193], v[192:193], v[116:117]\n"
    "v_pk_add_f32 v[194:195], v[194:195], v[118:119]\n"
    "v_mfma_f32_16x16x32_bf16 v[120:123], a[148:151], a[44:47], v[120:123]\n"
    "v_fmac_f32 v192, s76, v196\n"
    "v_fmac_f32 v193, s76, v197\n"
    "v_mfma_f32_16x16x32_bf16 v[120:123], a[152:155], a[48:51], v[120:123]\n"
    "v_fmac_f32 v194, s76, v198\n"
    "v_fmac_f32 v195, s76, v199\n"
    "v_mfma_f32_16x16x32_bf16 v[160:163], a[156:159], a[88:91], v[160:163]\n"
    "v_fmac_f32 v152, s78, v196\n"
    "v_fmac_f32 v153, s78, v197\n"
    "v_mfma_f32_16x16x32_bf16 v[160:163], a[160:163], a[92:95], v[160:163]\n"
    "v_fmac_f32 v154, s78, v198\n"
    "v_fmac_f32 v155, s78, v199\n"
    "v_mfma_f32_16x16x32_bf16 v[160:163], a[164:167], a[236:239], v[160:163]\n"
    "v_fmac_f32 v112, s80, v196\n"
    "v_fmac_f32 v113, s80, v197\n"
    "v_mfma_f32_16x16x32_bf16 v[160:163], a[168:171], a[248:251], v[160:163]\n"
    "v_fmac_f32 v114, s80, v198\n"
    "v_fmac_f32 v115, s80, v199\n"
    "v_mfma_f32_16x16x32_bf16 v[160:163], a[172:175], a[4:7], v[160:163]\n"
    "v_fmac_f32 v232, s82, v196\n"
    "v_fmac_f32 v233, s82, v197\n"
    "v_mfma_f32_16x16x32_bf16 v[160:163], a[176:179], a[64:67], v[160:163]\n"
    "s_waitcnt lgkmcnt(0)\n"
    "v_fmac_f32 v234, s82, v198\n"
    "v_fmac_f32 v235, s82, v199\n"
    "v_mfma_f32_16x16x32_bf16 v[160:163], a[180:183], a[104:107], v[160:163]\n"
    "v_cvt_pk_bf16_f32 v152, v152, v153\n"
    "v_cvt_pk_bf16_f32 v153, v154, v155\n"
    "v_mfma_f32_16x16x32_bf16 v[240:243], a[212:215], a[8:11], v[240:243]\n"
    "v_cvt_pk_bf16_f32 v112, v112, v113\n"
    "v_cvt_pk_bf16_f32 v113, v114, v115\n"
    "v_mfma_f32_16x16x32_bf16 v[240:243], a[216:219], a[224:227], v[240:243]\n"
    "ds_write_b64 v40, v[152:153] offset:21504\n"
    "v_mfma_f32_16x16x32_bf16 v[200:203], a[184:187], a[220:223], v[200:203]\n"
    "v_mul_f32 v120, s76, v120\n"
    "v_mul_f32 v121, s76, v121\n"
    "v_mfma_f32_16x16x32_bf16 v[200:203], a[188:191], a[232:235], v[200:203]\n"
    "v_mul_f32 v122, s76, v122\n"
    "v_mul_f32 v123, s76, v123\n"
    "v_mfma_f32_16x16x32_bf16 v[200:203], a[192:195], a[244:247], v[200:203]\n"
    "v_fma_f32 v160, -v160, s76, v120\n"
    "v_fma_f32 v161, -v161, s76, v121\n"
    "v_mfma_f32_16x16x32_bf16 v[200:203], a[196:199], a[0:3], v[200:203]\n"
    "v_fma_f32 v162, -v162, s76, v122\n"
    "v_fma_f32 v163, -v163, s76, v123\n"
    "v_mfma_f32_16x16x32_bf16 v[200:203], a[200:203], a[12:15], v[200:203]\n"
    "v_fma_f32 v120, v120, 2.0, -v160\n"
    "v_fma_f32 v121, v121, 2.0, -v161\n"
    "v_mfma_f32_16x16x32_bf16 v[200:203], a[204:207], a[96:99], v[200:203]\n"
    "v_fma_f32 v122, v122, 2.0, -v162\n"
    "v_fma_f32 v123, v123, 2.0, -v163\n"
    "v_mfma_f32_16x16x32_bf16 v[200:203], a[208:211], a[108:111], v[200:203]\n"
    "ds_write_b64 v40, v[112:113] offset:22528\n"
    "v_mfma_f32_16x16x32_bf16 v[124:127], a[128:131], a[28:31], v[124:127]\n"
    "ds_write_b128 v46, v[160:163] offset:1024\n"
    "v_mfma_f32_16x16x32_bf16 v[124:127], a[132:135], a[32:35], v[124:127]\n"
    "v_cvt_pk_bf16_f32 v232, v232, v233\n"
    "v_cvt_pk_bf16_f32 v233, v234, v235\n"
    "v_mfma_f32_16x16x32_bf16 v[124:127], a[136:139], a[36:39], v[124:127]\n"
    "v_cvt_pk_bf16_f32 v192, v192, v193\n"
    "v_cvt_pk_bf16_f32 v193, v194, v195\n"
    "v_mfma_f32_16x16x32_bf16 v[124:127], a[140:143], a[40:43], v[124:127]\n"
    "ds_write_b128 v46, v[120:123] offset:0\n"
    "v_mfma_f32_16x16x32_bf16 v[124:127], a[144:147], a[44:47], v[124:127]\n"
    "v_fma_f32 v240, v240, s82, v160\n"
    "v_fma_f32 v241, v241, s82, v161\n"
    "v_mfma_f32_16x16x32_bf16 v[124:127], a[148:151], a[48:51], v[124:127]\n"
    "v_fma_f32 v242, v242, s82, v162\n"
    "v_fma_f32 v243, v243, s82, v163\n"
    "v_mfma_f32_16x16x32_bf16 v[124:127], a[152:155], a[52:55], v[124:127]\n"
    "ds_write_b64 v40, v[232:233] offset:23552\n"
    "v_mfma_f32_16x16x32_bf16 v[164:167], a[156:159], a[92:95], v[164:167]\n"
    "ds_write_b128 v46, v[240:243] offset:3072\n"
    "v_mfma_f32_16x16x32_bf16 v[164:167], a[160:163], a[236:239], v[164:167]\n"
    "ds_write_b64 v40, v[192:193] offset:20480\n"
    "v_mfma_f32_16x16x32_bf16 v[164:167], a[164:167], a[248:251], v[164:167]\n"
    "v_mfma_f32_16x16x32_bf16 v[164:167], a[168:171], a[4:7], v[164:167]\n"
    "ds_write_b128 v46, v[200:203] offset:2048\n"
    "v_mfma_f32_16x16x32_bf16 v[164:167], a[172:175], a[64:67], v[164:167]\n"
    "v_mfma_f32_16x16x32_bf16 v[164:167], a[176:179], a[104:107], v[164:167]\n"
    "v_mfma_f32_16x16x32_bf16 v[164:167], a[180:183], a[72:75], v[164:167]\n"
    "v_mfma_f32_16x16x32_bf16 v[244:247], a[212:215], a[68:71], v[244:247]\n"
    "s_waitcnt lgkmcnt(0)\n"
    "s_barrier\n"
    "v_mfma_f32_16x16x32_bf16 v[244:247], a[216:219], a[100:103], v[244:247]\n"
    "ds_read_b128 v[120:123], v47 offset:0\n"
    "ds_read_b128 v[160:163], v47 offset:1024\n"
    "v_mfma_f32_16x16x32_bf16 v[204:207], a[184:187], a[232:235], v[204:207]\n"
    "ds_read_b128 v[200:203], v47 offset:2048\n"
    "ds_read_b128 v[240:243], v47 offset:3072\n"
    "v_mfma_f32_16x16x32_bf16 v[204:207], a[188:191], a[244:247], v[204:207]\n"
    "ds_read_b128 v[64:67], v41 offset:16384\n"
    "ds_read_b128 v[68:71], v41 offset:20480\n"
    "v_mfma_f32_16x16x32_bf16 v[204:207], a[192:195], a[0:3], v[204:207]\n"
    "ds_read_b128 a[76:79], v18 offset:57872\n"
    "v_mul_f32 v124, s76, v124\n"
    "v_mfma_f32_16x16x32_bf16 v[204:207], a[196:199], a[12:15], v[204:207]\n"
    "ds_read_b128 a[228:231], v19 offset:55616\n"
    "v_mul_f32 v125, s76, v125\n"
    "v_mfma_f32_16x16x32_bf16 v[204:207], a[200:203], a[96:99], v[204:207]\n"
    "ds_read_b128 a[116:119], v18 offset:57952\n"
    "v_mul_f32 v126, s76, v126\n"
    "v_mfma_f32_16x16x32_bf16 v[204:207], a[204:207], a[108:111], v[204:207]\n"
    "ds_read_b128 a[16:19], v18 offset:58416\n"
    "v_mul_f32 v127, s76, v127\n"
    "v_mfma_f32_16x16x32_bf16 v[204:207], a[208:211], a[112:115], v[204:207]\n"
    "s_waitcnt lgkmcnt(5)\n"
    "s_add_u32 s58, s53, 344064\n"
    "buffer_store_dwordx4 v[64:67], v42, s[24:27], s58 offen sc0 sc1\n"
    "ds_read_b128 a[80:83], v19 offset:56160\n"
    "v_mfma_f32_16x16x32_bf16 v[128:131], a[128:131], a[32:35], v[128:131]\n"
    "ds_read_b128 a[240:243], v18 offset:58496\n"
    "v_fma_f32 v164, -v164, s76, v124\n"
    "v_mfma_f32_16x16x32_bf16 v[128:131], a[132:135], a[36:39], v[128:131]\n"
    "s_waitcnt lgkmcnt(6)\n"
    "s_add_u32 s58, s53, 430080\n"
    "buffer_store_dwordx4 v[68:71], v42, s[24:27], s58 offen sc0 sc1\n"
    "v_fma_f32 v165, -v165, s76, v125\n"
    "v_mfma_f32_16x16x32_bf16 v[128:131], a[136:139], a[40:43], v[128:131]\n"
    "v_fma_f32 v166, -v166, s76, v126\n"
    "v_fma_f32 v167, -v167, s76, v127\n"
    "v_mfma_f32_16x16x32_bf16 v[128:131], a[140:143], a[44:47], v[128:131]\n"
    "v_fma_f32 v124, v124, 2.0, -v164\n"
    "v_fma_f32 v125, v125, 2.0, -v165\n"
    "v_mfma_f32_16x16x32_bf16 v[128:131], a[144:147], a[48:51], v[128:131]\n"
    "v_fma_f32 v126, v126, 2.0, -v166\n"
    "v_fma_f32 v127, v127, 2.0, -v167\n"
    "v_pk_fma_f32 v[240:241], v[244:245], s[76:77], v[240:241] op_sel_hi:[1,0,1]\n"
    "v_pk_fma_f32 v[242:243], v[246:247], s[76:77], v[242:243] op_sel_hi:[1,0,1]\n"
    "v_pk_fma_f32 v[240:241], v[164:165], s[84:85], v[240:241] op_sel_hi:[1,0,1]\n"
    "v_pk_fma_f32 v[242:243], v[166:167], s[84:85], v[242:243] op_sel_hi:[1,0,1]\n"
    "v_pk_fma_f32 v[160:161], v[164:165], 2.0, v[160:161] op_sel_hi:[1,0,1]\n"
    "v_pk_fma_f32 v[162:163], v[166:167], 2.0, v[162:163] op_sel_hi:[1,0,1]\n"
    "v_pk_fma_f32 v[120:121], v[124:125], 4.0, v[120:121] op_sel_hi:[1,0,1]\n"
    "v_pk_fma_f32 v[122:123], v[126:127], 4.0, v[122:123] op_sel_hi:[1,0,1]\n"
    "v_pk_add_f32 v[200:201], v[200:201], v[124:125]\n"
    "v_pk_add_f32 v[202:203], v[202:203], v[126:127]\n"
    "v_pk_fma_f32 v[200:201], v[204:205], s[76:77], v[200:201] op_sel_hi:[1,0,1]\n"
    "v_pk_fma_f32 v[202:203], v[206:207], s[76:77], v[202:203] op_sel_hi:[1,0,1]\n"
    "v_pk_fma_f32 v[160:161], v[204:205], s[78:79], v[160:161] op_sel_hi:[1,0,1]\n"
    "v_pk_fma_f32 v[162:163], v[206:207], s[78:79], v[162:163] op_sel_hi:[1,0,1]\n"
    "v_pk_fma_f32 v[120:121], v[204:205], s[80:81], v[120:121] op_sel_hi:[1,0,1]\n"
    "v_pk_fma_f32 v[122:123], v[206:207], s[80:81], v[122:123] op_sel_hi:[1,0,1]\n"
    "v_pk_fma_f32 v[240:241], v[204:205], s[82:83], v[240:241] op_sel_hi:[1,0,1]\n"
    "v_pk_fma_f32 v[242:243], v[206:207], s[82:83], v[242:243] op_sel_hi:[1,0,1]\n"
    "v_mfma_f32_16x16x32_bf16 v[128:131], a[148:151], a[52:55], v[128:131]\n"
    "v_cvt_pk_bf16_f32 v160, v160, v161\n"
    "v_cvt_pk_bf16_f32 v161, v162, v163\n"
    "v_mfma_f32_16x16x32_bf16 v[128:131], a[152:155], a[56:59], v[128:131]\n"
    "v_cvt_pk_bf16_f32 v120, v120, v121\n"
    "v_cvt_pk_bf16_f32 v121, v122, v123\n"
    "v_mfma_f32_16x16x32_bf16 v[168:171], a[156:159], a[236:239], v[168:171]\n"
    "ds_write_b64 v40, v[160:161] offset:29696\n"
    "v_mfma_f32_16x16x32_bf16 v[168:171], a[160:163], a[248:251], v[168:171]\n"
    "v_cvt_pk_bf16_f32 v240, v240, v241\n"
    "v_cvt_pk_bf16_f32 v241, v242, v243\n"
    "v_mfma_f32_16x16x32_bf16 v[168:171], a[164:167], a[4:7], v[168:171]\n"
    "ds_write_b64 v40, v[120:121] offset:30720\n"
    "v_mfma_f32_16x16x32_bf16 v[168:171], a[168:171], a[64:67], v[168:171]\n"
    "v_cvt_pk_bf16_f32 v200, v200, v201\n"
    "v_cvt_pk_bf16_f32 v201, v202, v203\n"
    "v_mfma_f32_16x16x32_bf16 v[168:171], a[172:175], a[104:107], v[168:171]\n"
    "ds_write_b64 v40, v[240:241] offset:31744\n"
    "v_mfma_f32_16x16x32_bf16 v[168:171], a[176:179], a[72:75], v[168:171]\n"
    "s_waitcnt lgkmcnt(3)\n"
    "v_mfma_f32_16x16x32_bf16 v[168:171], a[180:183], a[76:79], v[168:171]\n"
    "ds_write_b64 v40, v[200:201] offset:28672\n"
    "v_mfma_f32_16x16x32_bf16 v[248:251], a[212:215], a[224:227], v[248:251]\n"
    "v_mfma_f32_16x16x32_bf16 v[248:251], a[216:219], a[228:231], v[248:251]\n"
    "v_mfma_f32_16x16x32_bf16 v[208:211], a[184:187], a[244:247], v[208:211]\n"
    "v_pk_mul_f32 v[128:129], v[128:129], s[76:77] op_sel_hi:[1,0]\n"
    "v_pk_mul_f32 v[130:131], v[130:131], s[76:77] op_sel_hi:[1,0]\n"
    "v_pk_fma_f32 v[168:169], v[168:169], s[76:77], v[128:129] op_sel_hi:[1,0,1] neg_lo:[1,0,0] neg_hi:[1,0,0]\n"
    "v_pk_fma_f32 v[170:171], v[170:171], s[76:77], v[130:131] op_sel_hi:[1,0,1] neg_lo:[1,0,0] neg_hi:[1,0,0]\n"
    "v_pk_fma_f32 v[128:129], v[128:129], 2.0, v[168:169] op_sel_hi:[1,0,1] neg_lo:[0,0,1] neg_hi:[0,0,1]\n"
    "v_pk_fma_f32 v[130:131], v[130:131], 2.0, v[170:171] op_sel_hi:[1,0,1] neg_lo:[0,0,1] neg_hi:[0,0,1]\n"
    "v_mfma_f32_16x16x32_bf16 v[208:211], a[188:191], a[0:3], v[208:211]\n"
    "v_mfma_f32_16x16x32_bf16 v[208:211], a[192:195], a[12:15], v[208:211]\n"
    "v_fma_f32 v248, v248, s82, v168\n"
    "v_fma_f32 v249, v249, s82, v169\n"
    "v_mfma_f32_16x16x32_bf16 v[208:211], a[196:199], a[96:99], v[208:211]\n"
    "s_waitcnt lgkmcnt(0)\n"
    "s_barrier\n"
    "v_mfma_f32_16x16x32_bf16 v[208:211], a[200:203], a[108:111], v[208:211]\n"
    "ds_read_b128 v[72:75], v41 offset:24576\n"
    "ds_read_b128 v[76:79], v41 offset:28672\n"
    "v_mfma_f32_16x16x32_bf16 v[208:211], a[204:207], a[112:115], v[208:211]\n"
    "ds_write_b128 v46, v[168:171] offset:5120\n"
    "v_mfma_f32_16x16x32_bf16 v[208:211], a[208:211], a[116:119], v[208:211]\n"
    "v_fma_f32 v250, v250, s82, v170\n"
    "v_fma_f32 v251, v251, s82, v171\n"
    "v_mfma_f32_16x16x32_bf16 v[132:135], a[128:131], a[36:39], v[132:135]\n"
    "v_mfma_f32_16x16x32_bf16 v[132:135], a[132:135], a[40:43], v[132:135]\n"
    "ds_write_b128 v46, v[128:131] offset:4096\n"
    "v_mfma_f32_16x16x32_bf16 v[132:135], a[136:139], a[44:47], v[132:135]\n"
    "s_waitcnt lgkmcnt(3)\n"
    "s_add_u32 s58, s53, 516096\n"
    "buffer_store_dwordx4 v[72:75], v42, s[24:27], s58 offen sc0 sc1\n"
    "v_mfma_f32_16x16x32_bf16 v[132:135], a[140:143], a[48:51], v[132:135]\n"
    "v_mfma_f32_16x16x32_bf16 v[132:135], a[144:147], a[52:55], v[132:135]\n"
    "s_waitcnt lgkmcnt(2)\n"
    "s_add_u32 s58, s53, 602112\n"
    "buffer_store_dwordx4 v[76:79], v42, s[24:27], s58 offen sc0 sc1\n"
    "v_mfma_f32_16x16x32_bf16 v[132:135], a[148:151], a[56:59], v[132:135]\n"
    "ds_write_b128 v46, v[248:251] offset:7168\n"
    "v_mfma_f32_16x16x32_bf16 v[132:135], a[152:155], a[60:63], v[132:135]\n"
    "v_mfma_f32_16x16x32_bf16 v[172:175], a[156:159], a[248:251], v[172:175]\n"
    "v_mfma_f32_16x16x32_bf16 v[172:175], a[160:163], a[4:7], v[172:175]\n"
    "ds_write_b128 v46, v[208:211] offset:6144\n"
    "v_mfma_f32_16x16x32_bf16 v[172:175], a[164:167], a[64:67], v[172:175]\n"
    "v_mfma_f32_16x16x32_bf16 v[172:175], a[168:171], a[104:107], v[172:175]\n"
    "v_mfma_f32_16x16x32_bf16 v[172:175], a[172:175], a[72:75], v[172:175]\n"
    "v_mfma_f32_16x16x32_bf16 v[172:175], a[176:179], a[76:79], v[172:175]\n"
    "s_waitcnt lgkmcnt(0)\n"
    "s_barrier\n"
    "v_mfma_f32_16x16x32_bf16 v[172:175], a[180:183], a[16:19], v[172:175]\n"
    "ds_read_b128 v[128:131], v47 offset:4096\n"
    "ds_read_b128 v[168:171], v47 offset:5120\n"
    "v_mfma_f32_16x16x32_bf16 v[252:255], a[212:215], a[100:103], v[252:255]\n"
    "ds_read_b128 v[208:211], v47 offset:6144\n"
    "ds_read_b128 v[248:251], v47 offset:7168\n"
    "v_mfma_f32_16x16x32_bf16 v[252:255], a[216:219], a[80:83], v[252:255]\n"
    "v_mfma_f32_16x16x32_bf16 v[212:215], a[184:187], a[0:3], v[212:215]\n"
    "v_pk_mul_f32 v[132:133], v[132:133], s[76:77] op_sel_hi:[1,0]\n"
    "v_pk_mul_f32 v[134:135], v[134:135], s[76:77] op_sel_hi:[1,0]\n"
    "v_pk_fma_f32 v[172:173], v[172:173], s[76:77], v[132:133] op_sel_hi:[1,0,1] neg_lo:[1,0,0] neg_hi:[1,0,0]\n"
    "v_pk_fma_f32 v[174:175], v[174:175], s[76:77], v[134:135] op_sel_hi:[1,0,1] neg_lo:[1,0,0] neg_hi:[1,0,0]\n"
    "v_pk_fma_f32 v[132:133], v[132:133], 2.0, v[172:173] op_sel_hi:[1,0,1] neg_lo:[0,0,1] neg_hi:[0,0,1]\n"
    "v_pk_fma_f32 v[134:135], v[134:135], 2.0, v[174:175] op_sel_hi:[1,0,1] neg_lo:[0,0,1] neg_hi:[0,0,1]\n"
    "v_mfma_f32_16x16x32_bf16 v[212:215], a[188:191], a[12:15], v[212:215]\n"
    "v_mfma_f32_16x16x32_bf16 v[212:215], a[192:195], a[96:99], v[212:215]\n"
    "v_mfma_f32_16x16x32_bf16 v[212:215], a[196:199], a[108:111], v[212:215]\n"
    "v_mfma_f32_16x16x32_bf16 v[212:215], a[200:203], a[112:115], v[212:215]\n"
    "s_waitcnt lgkmcnt(0)\n"
    "v_pk_fma_f32 v[248:249], v[252:253], s[76:77], v[248:249] op_sel_hi:[1,0,1]\n"
    "v_pk_fma_f32 v[250:251], v[254:255], s[76:77], v[250:251] op_sel_hi:[1,0,1]\n"
    "v_pk_fma_f32 v[248:249], v[172:173], s[84:85], v[248:249] op_sel_hi:[1,0,1]\n"
    "v_pk_fma_f32 v[250:251], v[174:175], s[84:85], v[250:251] op_sel_hi:[1,0,1]\n"
    "v_pk_fma_f32 v[168:169], v[172:173], 2.0, v[168:169] op_sel_hi:[1,0,1]\n"
    "v_pk_fma_f32 v[170:171], v[174:175], 2.0, v[170:171] op_sel_hi:[1,0,1]\n"
    "v_pk_fma_f32 v[128:129], v[132:133], 4.0, v[128:129] op_sel_hi:[1,0,1]\n"
    "v_pk_fma_f32 v[130:131], v[134:135], 4.0, v[130:131] op_sel_hi:[1,0,1]\n"
    "v_pk_add_f32 v[208:209], v[208:209], v[132:133]\n"
    "v_pk_add_f32 v[210:211], v[210:211], v[134:135]\n"
    "v_mfma_f32_16x16x32_bf16 v[212:215], a[204:207], a[116:119], v[212:215]\n"
    "v_mfma_f32_16x16x32_bf16 v[212:215], a[208:211], a[240:243], v[212:215]\n"
    "s_nop 7\n"
    "v_pk_fma_f32 v[208:209], v[212:213], s[76:77], v[208:209] op_sel_hi:[1,0,1]\n"
    "v_pk_fma_f32 v[210:211], v[214:215], s[76:77], v[210:211] op_sel_hi:[1,0,1]\n"
    "v_pk_fma_f32 v[168:169], v[212:213], s[78:79], v[168:169] op_sel_hi:[1,0,1]\n"
    "v_pk_fma_f32 v[170:171], v[214:215], s[78:79], v[170:171] op_sel_hi:[1,0,1]\n"
    "v_pk_fma_f32 v[128:129], v[212:213], s[80:81], v[128:129] op_sel_hi:[1,0,1]\n"
    "v_pk_fma_f32 v[130:131], v[214:215], s[80:81], v[130:131] op_sel_hi:[1,0,1]\n"
    "v_pk_fma_f32 v[248:249], v[212:213], s[82:83], v[248:249] op_sel_hi:[1,0,1]\n"
    "v_pk_fma_f32 v[250:251], v[214:215], s[82:83], v[250:251] op_sel_hi:[1,0,1]\n"
    "v_cvt_pk_bf16_f32 v168, v168, v169\n"
    "v_cvt_pk_bf16_f32 v169, v170, v171\n"
    "v_cvt_pk_bf16_f32 v128, v128, v129\n"
    "v_cvt_pk_bf16_f32 v129, v130, v131\n"
    "v_cvt_pk_bf16_f32 v248, v248, v249\n"
    "v_cvt_pk_bf16_f32 v249, v250, v251\n"
    "v_cvt_pk_bf16_f32 v208, v208, v209\n"
    "v_cvt_pk_bf16_f32 v209, v210, v211\n"
    "ds_write_b64 v40, v[168:169] offset:37888\n"
    "ds_write_b64 v40, v[128:129] offset:38912\n"
    "ds_write_b64 v40, v[248:249] offset:39936\n"
    "ds_write_b64 v40, v[208:209] offset:36864\n"
    "s_waitcnt lgkmcnt(0)\n"
    "s_barrier\n"
    "ds_read_b128 v[96:99], v41 offset:32768\n"
    "ds_read_b128 v[100:103], v41 offset:36864\n"
    "s_add_u32 s58, s53, 688128\n"
    "s_waitcnt lgkmcnt(1)\n"
    "buffer_store_dwordx4 v[96:99], v42, s[24:27], s58 offen sc0 sc1\n"
    "s_add_u32 s58, s53, 774144\n"
    "s_waitcnt lgkmcnt(0)\n"
    "buffer_store_dwordx4 v[100:103], v42, s[24:27], s58 offen sc0 sc1\n"
    "s_endpgm\n"
    "25:\n"
    "s_mov_b32 s80, 0x40000\n"
    "28:\n"
    "buffer_load_dword v36, off, s[28:31], s74 offset:2048 sc1\n"
    "s_waitcnt vmcnt(0)\n"
    "v_cmp_eq_u32 vcc, v36, v38\n"
    "s_cbranch_vccz 26b\n"
    "s_sleep 4\n"
    "s_sub_u32 s80, s80, 1\n"
    "s_cmp_lg_u32 s80, 0\n"
    "s_cbranch_scc1 28b\n"
    "s_branch 26b\n"
    "251:\n"
    "s_mov_b32 s80, 0x40000\n"
    "281:\n"
    "buffer_load_dword v22, v17, s[28:31], s74 offen sc1\n"
    "s_waitcnt vmcnt(0)\n"
    "v_cmp_eq_u32 vcc, 0, v22\n"
    "s_cbranch_vccz 261b\n"
    "s_sleep 4\n"
    "s_sub_u32 s80, s80, 1\n"
    "s_cmp_lg_u32 s80, 0\n"
    "s_cbranch_scc1 281b\n"
    "s_branch 261b\n"
    "252:\n"
    "s_mov_b32 s80, 0x40000\n"
    "282:\n"
    "buffer_load_dwordx2 v[22:23], v27, s[28:31], s74 offen offset:128 sc1\n"
    "s_waitcnt vmcnt(0)\n"
    "v_min_u32 v22, v22, v23\n"
    "v_cmp_eq_u32 vcc, 0, v22\n"
    "s_cbranch_vccz 262b\n"
    "s_sleep 4\n"
    "s_sub_u32 s80, s80, 1\n"
    "s_cmp_lg_u32 s80, 0\n"
    "s_cbranch_scc1 282b\n"
    "s_branch 262b\n"
    ".p2align 6\n"
    "90:\n"
    "s_waitcnt vmcnt(13)\n"
    "ds_read_b64_tr_b16 a[64:65], v12 offset:0\n"
    "ds_read_b64_tr_b16 a[66:67], v12 offset:256\n"
    "ds_read_b64_tr_b16 a[68:69], v12 offset:384\n"
    "ds_read_b64_tr_b16 a[70:71], v12 offset:640\n"
    "s_add_u32 m0, s48, 10496\n"
    "s_mov_b32 exec_hi, 0xffff\n"
    "buffer_load_dwordx4 v8, s[16:19], s58 offen lds\n"
    "s_mov_b32 exec_hi, -1\n"
    "s_waitcnt lgkmcnt(2)\n"
    "v_mfma_f32_32x32x16_bf16 v[48:63], a[64:67], a[228:231], 0\n"
    "s_waitcnt lgkmcnt(0)\n"
    "v_mfma_f32_32x32x16_bf16 v[48:63], a[68:71], a[232:235], v[48:63]\n"
    "s_waitcnt vmcnt(7)\n"
    "ds_read_b64_tr_b16 a[76:77], v12 offset:2816\n"
    "ds_read_b64_tr_b16 a[78:79], v12 offset:3072\n"
    "ds_read_b64_tr_b16 a[80:81], v12 offset:3200\n"
    "ds_read_b64_tr_b16 a[82:83], v12 offset:3456\n"
    "s_add_u32 s58, s51, 688128\n"
    "s_add_u32 m0, s48, 11264\n"
    "s_nop 0\n"
    "buffer_load_dwordx4 v6, s[16:19], s58 offen lds\n"
    "s_nop 7\n"
    "v_cvt_pk_bf16_f32 v48, v48, v49\n"
    "v_cvt_pk_bf16_f32 v49, v50, v51\n"
    "v_cvt_pk_bf16_f32 v50, v52, v53\n"
    "v_cvt_pk_bf16_f32 v51, v54, v55\n"
    "v_cvt_pk_bf16_f32 v52, v56, v57\n"
    "ds_write_b128 v15, v[48:51] offset:0\n"
    "v_cvt_pk_bf16_f32 v53, v58, v59\n"
    "s_add_u32 m0, s48, 12288\n"
    "s_nop 0\n"
    "buffer_load_dwordx4 v7, s[16:19], s58 offen lds\n"
    "v_cvt_pk_bf16_f32 v54, v60, v61\n"
    "v_cvt_pk_bf16_f32 v55, v62, v63\n"
    "s_nop 0\n"
    "ds_write_b128 v15, v[52:55] offset:576\n"
    "s_barrier\n"
    "ds_read_b64_tr_b16 a[128:129], v24 offset:0\n"
    "ds_read_b64_tr_b16 a[130:131], v28 offset:0\n"
    "ds_read_b64_tr_b16 a[132:133], v29 offset:0\n"
    "ds_read_b64_tr_b16 a[134:135], v30 offset:0\n"
    "ds_read_b64_tr_b16 a[136:137], v24 offset:512\n"
    "ds_read_b64_tr_b16 a[138:139], v28 offset:512\n"
    "ds_read_b64_tr_b16 a[140:141], v29 offset:512\n"
    "ds_read_b64_tr_b16 a[142:143], v30 offset:512\n"
    "s_add_u32 m0, s48, 13312\n"
    "s_mov_b32 exec_hi, 0xffff\n"
    "buffer_load_dwordx4 v8, s[16:19], s58 offen lds\n"
    "s_mov_b32 exec_hi, -1\n"
    "ds_read_b64_tr_b16 a[144:145], v24 offset:7168\n"
    "ds_read_b64_tr_b16 a[146:147], v28 offset:7168\n"
    "ds_read_b64_tr_b16 a[148:149], v29 offset:7168\n"
    "ds_read_b64_tr_b16 a[150:151], v30 offset:7168\n"
    "ds_read_b64_tr_b16 a[152:153], v24 offset:7680\n"
    "ds_read_b64_tr_b16 a[154:155], v28 offset:7680\n"
    "ds_read_b64_tr_b16 a[156:157], v29 offset:7680\n"
    "ds_read_b64_tr_b16 a[158:159], v30 offset:7680\n"
    "s_mov_b32 m0, s54\n"
    "s_nop 0\n"
    "buffer_load_dwordx4 v6, s[16:19], s57 offen lds\n"
    "s_waitcnt lgkmcnt(8)\n"
    "v_mfma_f32_4x4x4_16b_bf16 v[48:51], v[20:21], a[128:129], 0\n"
    "v_mfma_f32_4x4x4_16b_bf16 v[52:55], v[20:21], a[130:131], 0\n"
    "v_mfma_f32_4x4x4_16b_bf16 v[56:59], v[20:21], a[132:133], 0\n"
    "v_mfma_f32_4x4x4_16b_bf16 v[60:63], v[20:21], a[134:135], 0\n"
    "v_mfma_f32_4x4x4_16b_bf16 v[64:67], v[20:21], a[136:137], 0\n"
    "v_mfma_f32_4x4x4_16b_bf16 v[68:71], v[20:21], a[138:139], 0\n"
    "v_mfma_f32_4x4x4_16b_bf16 v[72:75], v[20:21], a[140:141], 0\n"
    "v_mfma_f32_4x4x4_16b_bf16 v[76:79], v[20:21], a[142:143], 0\n"
    "s_mov_b32 m0, s55\n"
    "s_nop 0\n"
    "buffer_load_dwordx4 v7, s[16:19], s57 offen lds\n"
    "s_nop 1\n"
    "v_cvt_pk_bf16_f32 v136, v48, v52\n"
    "v_cvt_pk_bf16_f32 v137, v56, v60\n"
    "v_cvt_pk_bf16_f32 v138, v64, v68\n"
    "v_cvt_pk_bf16_f32 v139, v72, v76\n"
    "v_cvt_pk_bf16_f32 v176, v49, v53\n"
    "v_cvt_pk_bf16_f32 v177, v57, v61\n"
    "v_cvt_pk_bf16_f32 v178, v65, v69\n"
    "s_mov_b32 m0, s56\n"
    "s_mov_b32 exec_hi, 0xffff\n"
    "buffer_load_dwordx4 v8, s[16:19], s57 offen lds\n"
    "s_mov_b32 exec_hi, -1\n"
    "s_add_u32 s51, s51, 64\n"
    "s_add_u32 s57, s57, s59\n"
    "v_cvt_pk_bf16_f32 v179, v73, v77\n"
    "v_cvt_pk_bf16_f32 v216, v50, v54\n"
    "v_cvt_pk_bf16_f32 v217, v58, v62\n"
    "v_cvt_pk_bf16_f32 v218, v66, v70\n"
    "v_cvt_pk_bf16_f32 v219, v74, v78\n"
    "s_waitcnt lgkmcnt(0)\n"
    "v_mfma_f32_4x4x4_16b_bf16 v[48:51], v[20:21], a[144:145], 0\n"
    "v_mfma_f32_4x4x4_16b_bf16 v[52:55], v[20:21], a[146:147], 0\n"
    "v_mfma_f32_4x4x4_16b_bf16 v[56:59], v[20:21], a[148:149], 0\n"
    "v_mfma_f32_4x4x4_16b_bf16 v[60:63], v[20:21], a[150:151], 0\n"
    "v_mfma_f32_4x4x4_16b_bf16 v[64:67], v[20:21], a[152:153], 0\n"
    "v_mfma_f32_4x4x4_16b_bf16 v[68:71], v[20:21], a[154:155], 0\n"
    "v_mfma_f32_4x4x4_16b_bf16 v[72:75], v[20:21], a[156:157], 0\n"
    "v_mfma_f32_4x4x4_16b_bf16 v[76:79], v[20:21], a[158:159], 0\n"
    "s_add_u32 m0, s60, 14336\n"
    "s_add_u32 s80, s63, 258048\n"
    "buffer_load_dwordx4 v9, s[20:23], s80 offen lds\n"
    "s_nop 1\n"
    "v_cvt_pk_bf16_f32 v140, v48, v52\n"
    "v_cvt_pk_bf16_f32 v141, v56, v60\n"
    "v_cvt_pk_bf16_f32 v142, v64, v68\n"
    "v_cvt_pk_bf16_f32 v143, v72, v76\n"
    "v_cvt_pk_bf16_f32 v180, v49, v53\n"
    "v_cvt_pk_bf16_f32 v181, v57, v61\n"
    "v_cvt_pk_bf16_f32 v182, v65, v69\n"
    "s_mov_b32 m0, s61\n"
    "s_add_u32 s80, s64, 258048\n"
    "buffer_load_dwordx4 v9, s[20:23], s80 offen lds\n"
    "s_add_u32 s61, s61, s62\n"
    "v_cvt_pk_bf16_f32 v183, v73, v77\n"
    "v_cvt_pk_bf16_f32 v220, v50, v54\n"
    "v_cvt_pk_bf16_f32 v221, v58, v62\n"
    "v_cvt_pk_bf16_f32 v222, v66, v70\n"
    "v_cvt_pk_bf16_f32 v223, v74, v78\n"
    "v_mfma_f32_32x32x16_bf16 v[64:79], a[76:79], a[228:231], 0\n"
    "v_mfma_f32_32x32x16_bf16 v[64:79], a[80:83], a[232:235], v[64:79]\n"
    "s_waitcnt vmcnt(11)\n"
    "ds_read_b64_tr_b16 a[64:65], v13\n"
    "ds_read_b64_tr_b16 a[66:67], v14\n"
    "ds_read_b64_tr_b16 a[68:69], v12 offset:5632\n"
    "ds_read_b64_tr_b16 a[70:71], v12 offset:5888\n"
    "ds_read_b64_tr_b16 a[72:73], v12 offset:6016\n"
    "ds_read_b64_tr_b16 a[74:75], v12 offset:6272\n"
    "s_cmp_lt_u32 s78, 21\n"
    "s_cbranch_scc0 17f\n"
    "s_barrier\n"
    "s_mov_b64 exec, 1\n"
    "buffer_store_dword v39, v3, s[28:31], s65 offen sc0 sc1\n"
    "s_mov_b64 exec, -1\n"
    "17:\n"
    "s_waitcnt lgkmcnt(4)\n"
    "v_mfma_f32_32x32x16_bf16 v[80:95], a[64:67], a[236:239], 0\n"
    "s_waitcnt lgkmcnt(2)\n"
    "v_mfma_f32_32x32x16_bf16 v[48:63], a[68:71], a[228:231], 0\n"
    "s_waitcnt lgkmcnt(0)\n"
    "v_mfma_f32_32x32x16_bf16 v[48:63], a[72:75], a[232:235], v[48:63]\n"
    "s_waitcnt vmcnt(8)\n"
    "ds_read_b64_tr_b16 a[76:77], v12 offset:8448\n"
    "ds_read_b64_tr_b16 a[78:79], v12 offset:8704\n"
    "ds_read_b64_tr_b16 a[80:81], v12 offset:8832\n"
    "ds_read_b64_tr_b16 a[82:83], v12 offset:9088\n"
    "v_cvt_pk_bf16_f32 v64, v64, v65\n"
    "v_cvt_pk_bf16_f32 v65, v66, v67\n"
    "v_cvt_pk_bf16_f32 v66, v68, v69\n"
    "v_cvt_pk_bf16_f32 v67, v70, v71\n"
    "v_cvt_pk_bf16_f32 v68, v72, v73\n"
    "ds_write_b128 v15, v[64:67] offset:1152\n"
    "v_cvt_pk_bf16_f32 v69, v74, v75\n"
    "v_cvt_pk_bf16_f32 v70, v76, v77\n"
    "v_cvt_pk_bf16_f32 v71, v78, v79\n"
    "s_nop 0\n"
    "ds_write_b128 v15, v[68:71] offset:1728\n"
    "s_waitcnt lgkmcnt(4)\n"
    "v_mfma_f32_32x32x16_bf16 v[64:79], a[76:79], a[228:231], 0\n"
    "s_waitcnt lgkmcnt(2)\n"
    "v_mfma_f32_32x32x16_bf16 v[64:79], a[80:83], a[232:235], v[64:79]\n"
    "s_waitcnt vmcnt(5)\n"
    "ds_read_b64_tr_b16 a[64:65], v12 offset:11264\n"
    "ds_read_b64_tr_b16 a[66:67], v12 offset:11520\n"
    "ds_read_b64_tr_b16 a[68:69], v12 offset:11648\n"
    "ds_read_b64_tr_b16 a[70:71], v12 offset:11904\n"
    "s_mul_i32 s72, s44, 21504\n"
    "v_add_u32 v25, s72, v25\n"
    "v_mov_b32 v34, v25\n"
    "s_mul_i32 s72, s44, 163840\n"
    "s_mov_b32 exec_hi, 0\n"
    "v_add_u32 v34, s72, v34\n"
    "s_mov_b32 exec_hi, -1\n"
    "s_mul_i32 s72, s44, 1728\n"
    "v_add_u32 v19, s72, v19\n"
    "v_cvt_pk_bf16_f32 v48, v48, v49\n"
    "v_cvt_pk_bf16_f32 v49, v50, v51\n"
    "v_cvt_pk_bf16_f32 v50, v52, v53\n"
    "v_cvt_pk_bf16_f32 v51, v54, v55\n"
    "v_cvt_pk_bf16_f32 v52, v56, v57\n"
    "ds_write_b128 v15, v[48:51] offset:2304\n"
    "v_cvt_pk_bf16_f32 v53, v58, v59\n"
    "v_cvt_pk_bf16_f32 v54, v60, v61\n"
    "v_cvt_pk_bf16_f32 v55, v62, v63\n"
    "s_nop 0\n"
    "ds_write_b128 v15, v[52:55] offset:2880\n"
    "s_waitcnt lgkmcnt(4)\n"
    "v_mfma_f32_32x32x16_bf16 v[48:63], a[64:67], a[228:231], 0\n"
    "s_waitcnt lgkmcnt(2)\n"
    "v_mfma_f32_32x32x16_bf16 v[48:63], a[68:71], a[232:235], v[48:63]\n"
    "s_waitcnt vmcnt(2)\n"
    "ds_read_b64_tr_b16 a[76:77], v37 offset:14080\n"
    "ds_read_b64_tr_b16 a[78:79], v37 offset:14336\n"
    "ds_read_b64_tr_b16 a[80:81], v37 offset:14464\n"
    "ds_read_b64_tr_b16 a[82:83], v37 offset:14720\n"
    "ds_read_b64_tr_b16 a[84:85], v31\n"
    "ds_read_b64_tr_b16 a[86:87], v32\n"
    "v_xor_b32 v12, 32, v12\n"
    "v_xor_b32 v13, 32, v13\n"
    "v_xor_b32 v14, 32, v14\n"
    "v_xor_b32 v31, 32, v31\n"
    "v_xor_b32 v32, 32, v32\n"
    "v_xor_b32 v37, 32, v37\n"
    "v_cvt_pk_bf16_f32 v64, v64, v65\n"
    "v_cvt_pk_bf16_f32 v65, v66, v67\n"
    "v_cvt_pk_bf16_f32 v66, v68, v69\n"
    "v_cvt_pk_bf16_f32 v67, v70, v71\n"
    "v_cvt_pk_bf16_f32 v68, v72, v73\n"
    "ds_write_b128 v15, v[64:67] offset:3456\n"
    "v_cvt_pk_bf16_f32 v69, v74, v75\n"
    "v_cvt_pk_bf16_f32 v70, v76, v77\n"
    "v_cvt_pk_bf16_f32 v71, v78, v79\n"
    "s_nop 0\n"
    "ds_write_b128 v15, v[68:71] offset:4032\n"
    "s_waitcnt lgkmcnt(6)\n"
    "v_mfma_f32_32x32x16_bf16 v[64:79], a[76:79], a[228:231], 0\n"
    "s_waitcnt lgkmcnt(4)\n"
    "v_mfma_f32_32x32x16_bf16 v[64:79], a[80:83], a[232:235], v[64:79]\n"
    "s_waitcnt lgkmcnt(2)\n"
    "v_mfma_f32_32x32x16_bf16 v[80:95], a[84:87], a[240:243], v[80:95]\n"
    "s_add_u32 m0, s60, 21504\n"
    "s_add_u32 s80, s63, 387072\n"
    "buffer_load_dwordx4 v9, s[20:23], s80 offen lds\n"
    "v_cvt_pk_bf16_f32 v48, v48, v49\n"
    "v_cvt_pk_bf16_f32 v49, v50, v51\n"
    "v_cvt_pk_bf16_f32 v50, v52, v53\n"
    "v_cvt_pk_bf16_f32 v51, v54, v55\n"
    "v_cvt_pk_bf16_f32 v52, v56, v57\n"
    "ds_write_b128 v15, v[48:51] offset:4608\n"
    "v_cvt_pk_bf16_f32 v53, v58, v59\n"
    "v_cvt_pk_bf16_f32 v54, v60, v61\n"
    "s_mov_b32 m0, s61\n"
    "s_add_u32 s80, s64, 387072\n"
    "buffer_load_dwordx4 v9, s[20:23], s80 offen lds\n"
    "s_add_u32 s61, s61, s62\n"
    "v_cvt_pk_bf16_f32 v55, v62, v63\n"
    "s_nop 0\n"
    "ds_write_b128 v15, v[52:55] offset:5184\n"
    "v_cvt_pk_bf16_f32 v64, v64, v65\n"
    "v_cvt_pk_bf16_f32 v65, v66, v67\n"
    "v_cvt_pk_bf16_f32 v66, v68, v69\n"
    "v_cvt_pk_bf16_f32 v67, v70, v71\n"
    "v_cvt_pk_bf16_f32 v68, v72, v73\n"
    "s_add_u32 m0, s60, 28672\n"
    "s_add_u32 s80, s63, 516096\n"
    "buffer_load_dwordx4 v9, s[20:23], s80 offen lds\n"
    "ds_write_b128 v33, v[64:67] offset:5760\n"
    "v_cvt_pk_bf16_f32 v69, v74, v75\n"
    "v_cvt_pk_bf16_f32 v70, v76, v77\n"
    "v_cvt_pk_bf16_f32 v71, v78, v79\n"
    "s_nop 0\n"
    "ds_write_b128 v33, v[68:71] offset:6336\n"
    "v_cvt_pk_bf16_f32 v80, v80, v81\n"
    "v_cvt_pk_bf16_f32 v81, v82, v83\n"
    "s_mov_b32 m0, s61\n"
    "s_add_u32 s80, s64, 516096\n"
    "buffer_load_dwordx4 v9, s[20:23], s80 offen lds\n"
    "s_add_u32 s61, s61, s62\n"
    "v_cvt_pk_bf16_f32 v82, v84, v85\n"
    "v_cvt_pk_bf16_f32 v83, v86, v87\n"
    "v_cvt_pk_bf16_f32 v84, v88, v89\n"
    "ds_write_b128 v16, v[80:83] offset:0\n"
    "v_cvt_pk_bf16_f32 v85, v90, v91\n"
    "v_cvt_pk_bf16_f32 v86, v92, v93\n"
    "v_cvt_pk_bf16_f32 v87, v94, v95\n"
    "s_nop 0\n"
    "ds_write_b128 v16, v[84:87] offset:576\n"
    "s_add_u32 m0, s60, 35840\n"
    "s_add_u32 s80, s63, 645120\n"
    "buffer_load_dwordx4 v9, s[20:23], s80 offen lds\n"
    "s_mov_b32 m0, s61\n"
    "s_add_u32 s80, s64, 645120\n"
    "buffer_load_dwordx4 v9, s[20:23], s80 offen lds\n"
    "s_add_u32 s61, s61, s62\n"
    "s_add_u32 m0, s60, 43008\n"
    "s_add_u32 s80, s63, 774144\n"
    "buffer_load_dwordx4 v9, s[20:23], s80 offen lds\n"
    "s_mov_b32 m0, s61\n"
    "s_add_u32 s80, s64, 774144\n"
    "buffer_load_dwordx4 v9, s[20:23], s80 offen lds\n"
    "s_add_u32 s61, s61, s62\n"
    "s_mov_b32 s74, 1695744\n"
    "s_waitcnt vmcnt(8)\n"
    "s_waitcnt lgkmcnt(0)\n"
    "s_barrier\n"
    "ds_read_b128 a[0:3], v18 offset:0\n"
    "ds_read_b128 a[4:7], v18 offset:576\n"
    "ds_read_b128 a[8:11], v18 offset:1152\n"
    "ds_read_b64_tr_b16 a[128:129], v24 offset:14336\n"
    "ds_read_b64_tr_b16 a[130:131], v28 offset:14336\n"
    "ds_read_b64_tr_b16 a[132:133], v29 offset:14336\n"
    "ds_read_b64_tr_b16 a[134:135], v30 offset:14336\n"
    "ds_read_b64_tr_b16 a[136:137], v24 offset:14848\n"
    "ds_read_b64_tr_b16 a[138:139], v28 offset:14848\n"
    "ds_read_b64_tr_b16 a[140:141], v29 offset:14848\n"
    "ds_read_b64_tr_b16 a[142:143], v30 offset:14848\n"
    "ds_read_b128 a[12:15], v18 offset:1728\n"
    "ds_read_b128 a[16:19], v18 offset:2304\n"
    "ds_read_b128 a[20:23], v18 offset:2880\n"
    "ds_read_b128 a[24:27], v18 offset:3456\n"
    "ds_read_b128 a[28:31], v18 offset:4032\n"
    "ds_read_b128 a[32:35], v18 offset:4608\n"
    "s_waitcnt lgkmcnt(15)\n"
    "v_mfma_f32_16x16x32_bf16 v[96:99], v[136:139], a[0:3], 0\n"
    "v_mfma_f32_16x16x32_bf16 v[100:103], v[136:139], a[4:7], 0\n"
    "s_waitcnt lgkmcnt(6)\n"
    "ds_read_b128 a[64:67], v18 offset:80\n"
    "v_mfma_f32_4x4x4_16b_bf16 v[48:51], v[20:21], a[128:129], 0\n"
    "v_mfma_f32_4x4x4_16b_bf16 v[52:55], v[20:21], a[130:131], 0\n"
    "v_mfma_f32_4x4x4_16b_bf16 v[56:59], v[20:21], a[132:133], 0\n"
    "v_mfma_f32_4x4x4_16b_bf16 v[60:63], v[20:21], a[134:135], 0\n"
    "v_mfma_f32_4x4x4_16b_bf16 v[64:67], v[20:21], a[136:137], 0\n"
    "v_mfma_f32_4x4x4_16b_bf16 v[68:71], v[20:21], a[138:139], 0\n"
    "v_mfma_f32_4x4x4_16b_bf16 v[72:75], v[20:21], a[140:141], 0\n"
    "v_mfma_f32_4x4x4_16b_bf16 v[76:79], v[20:21], a[142:143], 0\n"
    "v_cvt_pk_bf16_f32 v144, v48, v52\n"
    "v_mfma_f32_16x16x32_bf16 v[104:107], v[136:139], a[8:11], 0\n"
    "s_waitcnt vmcnt(6) lgkmcnt(6)\n"
    "s_barrier\n"
    "v_cvt_pk_bf16_f32 v145, v56, v60\n"
    "v_mfma_f32_16x16x32_bf16 v[108:111], v[136:139], a[12:15], 0\n"
    "s_waitcnt lgkmcnt(5)\n"
    "ds_read_b64_tr_b16 a[144:145], v24 offset:21504\n"
    "ds_read_b64_tr_b16 a[146:147], v28 offset:21504\n"
    "v_mfma_f32_16x16x32_bf16 v[112:115], v[136:139], a[16:19], 0\n"
    "s_waitcnt lgkmcnt(6)\n"
    "ds_read_b64_tr_b16 a[148:149], v29 offset:21504\n"
    "ds_read_b64_tr_b16 a[150:151], v30 offset:21504\n"
    "v_mfma_f32_16x16x32_bf16 v[116:119], v[136:139], a[20:23], 0\n"
    "s_waitcnt lgkmcnt(7)\n"
    "ds_read_b64_tr_b16 a[152:153], v24 offset:22016\n"
    "ds_read_b64_tr_b16 a[154:155], v28 offset:22016\n"
    "v_mfma_f32_16x16x32_bf16 v[120:123], v[136:139], a[24:27], 0\n"
    "s_waitcnt lgkmcnt(8)\n"
    "ds_read_b64_tr_b16 a[156:157], v29 offset:22016\n"
    "ds_read_b64_tr_b16 a[158:159], v30 offset:22016\n"
    "v_mfma_f32_16x16x32_bf16 v[124:127], v[136:139], a[28:31], 0\n"
    "s_waitcnt lgkmcnt(9)\n"
    "ds_read_b128 a[36:39], v18 offset:5184\n"
    "v_cvt_pk_bf16_f32 v146, v64, v68\n"
    "v_mfma_f32_16x16x32_bf16 v[128:131], v[136:139], a[32:35], 0\n"
    "v_cvt_pk_bf16_f32 v147, v72, v76\n"
    "v_cvt_pk_bf16_f32 v184, v49, v53\n"
    "v_mfma_f32_16x16x32_bf16 v[96:99], v[140:143], a[4:7], v[96:99]\n"
    "v_cvt_pk_bf16_f32 v185, v57, v61\n"
    "v_cvt_pk_bf16_f32 v186, v65, v69\n"
    "v_mfma_f32_16x16x32_bf16 v[100:103], v[140:143], a[8:11], v[100:103]\n"
    "v_cvt_pk_bf16_f32 v187, v73, v77\n"
    "v_cvt_pk_bf16_f32 v224, v50, v54\n"
    "v_mfma_f32_16x16x32_bf16 v[104:107], v[140:143], a[12:15], v[104:107]\n"
    "v_cvt_pk_bf16_f32 v225, v58, v62\n"
    "v_cvt_pk_bf16_f32 v226, v66, v70\n"
    "v_mfma_f32_16x16x32_bf16 v[108:111], v[140:143], a[16:19], v[108:111]\n"
    "s_waitcnt lgkmcnt(1)\n"
    "v_cvt_pk_bf16_f32 v227, v74, v78\n"
    "ds_read_b128 a[68:71], v18 offset:656\n"
    "v_mfma_f32_4x4x4_16b_bf16 v[48:51], v[20:21], a[144:145], 0\n"
    "v_mfma_f32_4x4x4_16b_bf16 v[52:55], v[20:21], a[146:147], 0\n"
    "v_mfma_f32_4x4x4_16b_bf16 v[56:59], v[20:21], a[148:149], 0\n"
    "v_mfma_f32_4x4x4_16b_bf16 v[60:63], v[20:21], a[150:151], 0\n"
    "v_mfma_f32_4x4x4_16b_bf16 v[64:67], v[20:21], a[152:153], 0\n"
    "v_mfma_f32_4x4x4_16b_bf16 v[68:71], v[20:21], a[154:155], 0\n"
    "v_mfma_f32_4x4x4_16b_bf16 v[72:75], v[20:21], a[156:157], 0\n"
    "v_mfma_f32_4x4x4_16b_bf16 v[76:79], v[20:21], a[158:159], 0\n"
    "v_cvt_pk_bf16_f32 v148, v48, v52\n"
    "v_mfma_f32_16x16x32_bf16 v[112:115], v[140:143], a[20:23], v[112:115]\n"
    "s_waitcnt vmcnt(4)\n"
    "s_barrier\n"
    "v_cvt_pk_bf16_f32 v149, v56, v60\n"
    "v_mfma_f32_16x16x32_bf16 v[116:119], v[140:143], a[24:27], v[116:119]\n"
    "ds_read_b64_tr_b16 a[128:129], v24 offset:28672\n"
    "ds_read_b64_tr_b16 a[130:131], v28 offset:28672\n"
    "v_mfma_f32_16x16x32_bf16 v[120:123], v[140:143], a[28:31], v[120:123]\n"
    "ds_read_b64_tr_b16 a[132:133], v29 offset:28672\n"
    "ds_read_b64_tr_b16 a[134:135], v30 offset:28672\n"
    "v_mfma_f32_16x16x32_bf16 v[124:127], v[140:143], a[32:35], v[124:127]\n"
    "s_waitcnt lgkmcnt(5)\n"
    "ds_read_b128 a[40:43], v18 offset:5760\n"
    "ds_read_b64_tr_b16 a[136:137], v24 offset:29184\n"
    "v_mfma_f32_16x16x32_bf16 v[128:131], v[140:143], a[36:39], v[128:131]\n"
    "ds_read_b64_tr_b16 a[138:139], v28 offset:29184\n"
    "ds_read_b64_tr_b16 a[140:141], v29 offset:29184\n"
    "v_mfma_f32_16x16x32_bf16 v[96:99], v[144:147], a[8:11], v[96:99]\n"
    "ds_read_b64_tr_b16 a[142:143], v30 offset:29184\n"
    "v_cvt_pk_bf16_f32 v150, v64, v68\n"
    "v_mfma_f32_16x16x32_bf16 v[100:103], v[144:147], a[12:15], v[100:103]\n"
    "v_cvt_pk_bf16_f32 v151, v72, v76\n"
    "v_cvt_pk_bf16_f32 v188, v49, v53\n"
    "v_mfma_f32_16x16x32_bf16 v[104:107], v[144:147], a[16:19], v[104:107]\n"
    "v_cvt_pk_bf16_f32 v189, v57, v61\n"
    "v_cvt_pk_bf16_f32 v190, v65, v69\n"
    "v_mfma_f32_16x16x32_bf16 v[108:111], v[144:147], a[20:23], v[108:111]\n"
    "v_cvt_pk_bf16_f32 v191, v73, v77\n"
    "v_cvt_pk_bf16_f32 v228, v50, v54\n"
    "v_mfma_f32_16x16x32_bf16 v[112:115], v[144:147], a[24:27], v[112:115]\n"
    "v_cvt_pk_bf16_f32 v229, v58, v62\n"
    "v_cvt_pk_bf16_f32 v230, v66, v70\n"
    "v_mfma_f32_16x16x32_bf16 v[116:119], v[144:147], a[28:31], v[116:119]\n"
    "v_cvt_pk_bf16_f32 v231, v74, v78\n"
    "ds_read_b128 a[72:75], v18 offset:1232\n"
    "v_mfma_f32_16x16x32_bf16 v[120:123], v[144:147], a[32:35], v[120:123]\n"
    "s_waitcnt lgkmcnt(1)\n"
    "ds_read_b128 a[76:79], v18 offset:1808\n"
    "ds_read_b128 a[80:83], v18 offset:2384\n"
    "v_mfma_f32_4x4x4_16b_bf16 v[48:51], v[20:21], a[128:129], 0\n"
    "v_mfma_f32_4x4x4_16b_bf16 v[52:55], v[20:21], a[130:131], 0\n"
    "v_mfma_f32_4x4x4_16b_bf16 v[56:59], v[20:21], a[132:133], 0\n"
    "v_mfma_f32_4x4x4_16b_bf16 v[60:63], v[20:21], a[134:135], 0\n"
    "v_mfma_f32_4x4x4_16b_bf16 v[64:67], v[20:21], a[136:137], 0\n"
    "v_mfma_f32_4x4x4_16b_bf16 v[68:71], v[20:21], a[138:139], 0\n"
    "v_mfma_f32_4x4x4_16b_bf16 v[72:75], v[20:21], a[140:141], 0\n"
    "v_mfma_f32_4x4x4_16b_bf16 v[76:79], v[20:21], a[142:143], 0\n"
    "v_cvt_pk_bf16_f32 v152, v48, v52\n"
    "v_mfma_f32_16x16x32_bf16 v[124:127], v[144:147], a[36:39], v[124:127]\n"
    "s_waitcnt vmcnt(2)\n"
    "ds_read_b128 a[44:47], v18 offset:6336\n"
    "s_barrier\n"
    "v_mfma_f32_16x16x32_bf16 v[128:131], v[144:147], a[40:43], v[128:131]\n"
    "ds_read_b64_tr_b16 a[144:145], v24 offset:35840\n"
    "ds_read_b64_tr_b16 a[146:147], v28 offset:35840\n"
    "v_mfma_f32_16x16x32_bf16 v[96:99], v[148:151], a[12:15], v[96:99]\n"
    "ds_read_b64_tr_b16 a[148:149], v29 offset:35840\n"
    "ds_read_b64_tr_b16 a[150:151], v30 offset:35840\n"
    "v_mfma_f32_16x16x32_bf16 v[100:103], v[148:151], a[16:19], v[100:103]\n"
    "ds_read_b64_tr_b16 a[152:153], v24 offset:36352\n"
    "ds_read_b64_tr_b16 a[154:155], v28 offset:36352\n"
    "v_mfma_f32_16x16x32_bf16 v[104:107], v[148:151], a[20:23], v[104:107]\n"
    "ds_read_b64_tr_b16 a[156:157], v29 offset:36352\n"
    "ds_read_b64_tr_b16 a[158:159], v30 offset:36352\n"
    "v_mfma_f32_16x16x32_bf16 v[108:111], v[148:151], a[24:27], v[108:111]\n"
    "v_cvt_pk_bf16_f32 v153, v56, v60\n"
    "v_cvt_pk_bf16_f32 v154, v64, v68\n"
    "v_mfma_f32_16x16x32_bf16 v[112:115], v[148:151], a[28:31], v[112:115]\n"
    "v_cvt_pk_bf16_f32 v155, v72, v76\n"
    "v_cvt_pk_bf16_f32 v192, v49, v53\n"
    "v_mfma_f32_16x16x32_bf16 v[116:119], v[148:151], a[32:35], v[116:119]\n"
    "v_cvt_pk_bf16_f32 v193, v57, v61\n"
    "v_cvt_pk_bf16_f32 v194, v65, v69\n"
    "v_mfma_f32_16x16x32_bf16 v[120:123], v[148:151], a[36:39], v[120:123]\n"
    "v_cvt_pk_bf16_f32 v195, v73, v77\n"
    "v_cvt_pk_bf16_f32 v232, v50, v54\n"
    "v_mfma_f32_16x16x32_bf16 v[124:127], v[148:151], a[40:43], v[124:127]\n"
    "s_waitcnt lgkmcnt(8)\n"
    "ds_read_b128 a[48:51], v18 offset:6912\n"
    "v_cvt_pk_bf16_f32 v233, v58, v62\n"
    "v_mfma_f32_16x16x32_bf16 v[128:131], v[148:151], a[44:47], v[128:131]\n"
    "v_cvt_pk_bf16_f32 v234, v66, v70\n"
    "v_cvt_pk_bf16_f32 v235, v74, v78\n"
    "v_mfma_f32_16x16x32_bf16 v[96:99], v[152:155], a[16:19], v[96:99]\n"
    "s_waitcnt lgkmcnt(1)\n"
    "ds_read_b128 a[84:87], v18 offset:2960\n"
    "ds_read_b128 a[88:91], v18 offset:3536\n"
    "v_mfma_f32_4x4x4_16b_bf16 v[48:51], v[20:21], a[144:145], 0\n"
    "v_mfma_f32_4x4x4_16b_bf16 v[52:55], v[20:21], a[146:147], 0\n"
    "v_mfma_f32_4x4x4_16b_bf16 v[56:59], v[20:21], a[148:149], 0\n"
    "v_mfma_f32_4x4x4_16b_bf16 v[60:63], v[20:21], a[150:151], 0\n"
    "v_mfma_f32_4x4x4_16b_bf16 v[64:67], v[20:21], a[152:153], 0\n"
    "v_mfma_f32_4x4x4_16b_bf16 v[68:71], v[20:21], a[154:155], 0\n"
    "v_mfma_f32_4x4x4_16b_bf16 v[72:75], v[20:21], a[156:157], 0\n"
    "v_mfma_f32_4x4x4_16b_bf16 v[76:79], v[20:21], a[158:159], 0\n"
    "v_cvt_pk_bf16_f32 v156, v48, v52\n"
    "v_mfma_f32_16x16x32_bf16 v[100:103], v[152:155], a[20:23], v[100:103]\n"
    "v_cvt_pk_bf16_f32 v157, v56, v60\n"
    "v_cvt_pk_bf16_f32 v158, v64, v68\n"
    "v_mfma_f32_16x16x32_bf16 v[104:107], v[152:155], a[24:27], v[104:107]\n"
    "s_waitcnt vmcnt(0)\n"
    "v_cvt_pk_bf16_f32 v159, v72, v76\n"
    "s_barrier\n"
    "v_mfma_f32_16x16x32_bf16 v[108:111], v[152:155], a[28:31], v[108:111]\n"
    "ds_read_b64_tr_b16 a[128:129], v24 offset:43008\n"
    "ds_read_b64_tr_b16 a[130:131], v28 offset:43008\n"
    "v_mfma_f32_16x16x32_bf16 v[112:115], v[152:155], a[32:35], v[112:115]\n"
    "ds_read_b64_tr_b16 a[132:133], v29 offset:43008\n"
    "ds_read_b64_tr_b16 a[134:135], v30 offset:43008\n"
    "v_mfma_f32_16x16x32_bf16 v[116:119], v[152:155], a[36:39], v[116:119]\n"
    "ds_read_b64_tr_b16 a[136:137], v24 offset:43520\n"
    "ds_read_b64_tr_b16 a[138:139], v28 offset:43520\n"
    "v_mfma_f32_16x16x32_bf16 v[120:123], v[152:155], a[40:43], v[120:123]\n"
    "ds_read_b64_tr_b16 a[140:141], v29 offset:43520\n"
    "ds_read_b64_tr_b16 a[142:143], v30 offset:43520\n"
    "v_mfma_f32_16x16x32_bf16 v[124:127], v[152:155], a[44:47], v[124:127]\n"
    "s_waitcnt lgkmcnt(10)\n"
    "ds_read_b128 a[52:55], v18 offset:7488\n"
    "v_cvt_pk_bf16_f32 v196, v49, v53\n"
    "v_mfma_f32_16x16x32_bf16 v[128:131], v[152:155], a[48:51], v[128:131]\n"
    "v_cvt_pk_bf16_f32 v197, v57, v61\n"
    "v_cvt_pk_bf16_f32 v198, v65, v69\n"
    "v_mfma_f32_16x16x32_bf16 v[96:99], v[156:159], a[20:23], v[96:99]\n"
    "v_cvt_pk_bf16_f32 v199, v73, v77\n"
    "v_cvt_pk_bf16_f32 v236, v50, v54\n"
    "v_mfma_f32_16x16x32_bf16 v[100:103], v[156:159], a[24:27], v[100:103]\n"
    "v_cvt_pk_bf16_f32 v237, v58, v62\n"
    "v_cvt_pk_bf16_f32 v238, v66, v70\n"
    "v_mfma_f32_16x16x32_bf16 v[104:107], v[156:159], a[28:31], v[104:107]\n"
    "v_cvt_pk_bf16_f32 v239, v74, v78\n"
    "ds_read_b64_tr_b16 a[220:221], v13\n"
    "v_mfma_f32_16x16x32_bf16 v[108:111], v[156:159], a[32:35], v[108:111]\n"
    "s_waitcnt lgkmcnt(2)\n"
    "ds_read_b64_tr_b16 a[222:223], v14\n"
    "buffer_load_dword v36, off, s[28:31], s74 offset:2048 sc1\n"
    "v_mfma_f32_4x4x4_16b_bf16 v[48:51], v[20:21], a[128:129], 0\n"
    "v_mfma_f32_4x4x4_16b_bf16 v[52:55], v[20:21], a[130:131], 0\n"
    "v_mfma_f32_4x4x4_16b_bf16 v[56:59], v[20:21], a[132:133], 0\n"
    "v_mfma_f32_4x4x4_16b_bf16 v[60:63], v[20:21], a[134:135], 0\n"
    "v_mfma_f32_4x4x4_16b_bf16 v[64:67], v[20:21], a[136:137], 0\n"
    "v_mfma_f32_4x4x4_16b_bf16 v[68:71], v[20:21], a[138:139], 0\n"
    "v_mfma_f32_4x4x4_16b_bf16 v[72:75], v[20:21], a[140:141], 0\n"
    "v_mfma_f32_4x4x4_16b_bf16 v[76:79], v[20:21], a[142:143], 0\n"
    "v_cvt_pk_bf16_f32 v160, v48, v52\n"
    "v_mfma_f32_16x16x32_bf16 v[112:115], v[156:159], a[36:39], v[112:115]\n"
    "v_cvt_pk_bf16_f32 v161, v56, v60\n"
    "v_cvt_pk_bf16_f32 v162, v64, v68\n"
    "v_mfma_f32_16x16x32_bf16 v[116:119], v[156:159], a[40:43], v[116:119]\n"
    "v_cvt_pk_bf16_f32 v163, v72, v76\n"
    "v_cvt_pk_bf16_f32 v200, v49, v53\n"
    "v_mfma_f32_16x16x32_bf16 v[120:123], v[156:159], a[44:47], v[120:123]\n"
    "ds_read_b64_tr_b16 a[212:213], v34 offset:0\n"
    "ds_read_b64_tr_b16 a[214:215], v34 offset:512\n"
    "v_mfma_f32_16x16x32_bf16 v[124:127], v[156:159], a[48:51], v[124:127]\n"
    "s_waitcnt lgkmcnt(4)\n"
    "ds_read_b128 a[56:59], v18 offset:8064\n"
    "ds_read_b64_tr_b16 a[216:217], v25 offset:14336\n"
    "v_mfma_f32_16x16x32_bf16 v[128:131], v[156:159], a[52:55], v[128:131]\n"
    "ds_read_b64_tr_b16 a[218:219], v25 offset:14848\n"
    "v_cvt_pk_bf16_f32 v201, v57, v61\n"
    "v_mfma_f32_16x16x32_bf16 v[96:99], v[160:163], a[24:27], v[96:99]\n"
    "v_cvt_pk_bf16_f32 v202, v65, v69\n"
    "v_cvt_pk_bf16_f32 v203, v73, v77\n"
    "v_mfma_f32_16x16x32_bf16 v[100:103], v[160:163], a[28:31], v[100:103]\n"
    "v_cvt_pk_bf16_f32 v240, v50, v54\n"
    "v_cvt_pk_bf16_f32 v241, v58, v62\n"
    "v_mfma_f32_16x16x32_bf16 v[104:107], v[160:163], a[32:35], v[104:107]\n"
    "v_cvt_pk_bf16_f32 v242, v66, v70\n"
    "v_cvt_pk_bf16_f32 v243, v74, v78\n"
    "v_mfma_f32_16x16x32_bf16 v[108:111], v[160:163], a[36:39], v[108:111]\n"
    "s_waitcnt lgkmcnt(0)\n"
    "s_barrier\n"
    "v_mfma_f32_16x16x32_bf16 v[112:115], v[160:163], a[40:43], v[112:115]\n"
    "ds_read_b128 a[92:95], v18 offset:4112\n"
    "ds_read_b128 a[96:99], v18 offset:4688\n"
    "v_mfma_f32_32x32x16_bf16 v[80:95], a[220:223], a[236:239], 0\n"
    "ds_read_b128 a[100:103], v18 offset:5264\n"
    "ds_read_b128 a[104:107], v18 offset:5840\n"
    "ds_read_b128 a[108:111], v18 offset:6416\n"
    "ds_read_b128 a[112:115], v18 offset:6992\n"
    "ds_read_b128 a[116:119], v18 offset:7568\n"
    "ds_read_b128 a[120:123], v18 offset:8144\n"
    "v_mfma_f32_16x16x32_bf16 v[116:119], v[160:163], a[44:47], v[116:119]\n"
    "ds_read_b64_tr_b16 a[224:225], v12 offset:0\n"
    "ds_read_b64_tr_b16 a[226:227], v12 offset:256\n"
    "v_mfma_f32_16x16x32_bf16 v[120:123], v[160:163], a[48:51], v[120:123]\n"
    "buffer_store_dwordx4 v[132:135], v5, s[28:31], s68 offen sc0 sc1\n"
    "s_add_u32 s68, s68, 7168\n"
    "v_mfma_f32_16x16x32_bf16 v[124:127], v[160:163], a[52:55], v[124:127]\n"
    "buffer_store_dwordx4 v[172:175], v5, s[28:31], s68 offen sc0 sc1\n"
    "s_add_u32 s68, s68, 7168\n"
    "v_mfma_f32_16x16x32_bf16 v[128:131], v[160:163], a[56:59], v[128:131]\n"
    "buffer_store_dwordx4 v[212:215], v5, s[28:31], s68 offen sc0 sc1\n"
    "v_min_u32 v17, 20, v4\n"
    "v_mfma_f32_16x16x32_bf16 v[136:139], v[176:179], a[64:67], 0\n"
    "s_mov_b64 exec, s[70:71]\n"
    "buffer_store_dwordx4 v[252:255], v5, s[28:31], s66 offen sc0 sc1\n"
    "s_mov_b64 exec, -1\n"
    "v_mfma_f32_16x16x32_bf16 v[136:139], v[180:183], a[68:71], v[136:139]\n"
    "ds_read_b128 a[64:67], v19 offset:0\n"
    "ds_read_b128 a[0:3], v18 offset:160\n"
    "v_mfma_f32_16x16x32_bf16 v[136:139], v[184:187], a[72:75], v[136:139]\n"
    "s_waitcnt lgkmcnt(2)\n"
    "v_lshlrev_b32 v17, 2, v17\n"
    "v_min_u32 v27, 41, v4\n"
    "v_mfma_f32_32x32x16_bf16 v[48:63], a[224:227], a[228:231], 0\n"
    "v_lshlrev_b32 v27, 3, v27\n"
    "s_cmp_lt_u32 s78, 21\n"
    "s_cselect_b32 s60, 1073741824, s65\n"
    "s_cselect_b32 s61, s65, 1073741824\n"
    "s_mov_b32 s52, 0\n"
    "s_lshl_b32 s72, s36, 2\n"
    "s_add_u32 s72, s72, 1695744\n"
    "v_mfma_f32_16x16x32_bf16 v[136:139], v[188:191], a[76:79], v[136:139]\n"
    "ds_read_b64_tr_b16 a[220:221], v12 offset:384\n"
    "ds_read_b64_tr_b16 a[222:223], v12 offset:640\n"
    "v_mfma_f32_16x16x32_bf16 v[136:139], v[192:195], a[80:83], v[136:139]\n"
    "ds_read_b128 a[4:7], v18 offset:736\n"
    "v_mov_b32 v35, s72\n"
    "v_mfma_f32_16x16x32_bf16 v[136:139], v[196:199], a[84:87], v[136:139]\n"
    "s_lshl_b32 s72, s37, 2\n"
    "s_lshl_b32 s73, s43, 1\n"
    "s_add_u32 s72, s72, s73\n"
    "v_mfma_f32_16x16x32_bf16 v[136:139], v[200:203], a[88:91], v[136:139]\n"
    "s_add_u32 s72, s72, s44\n"
    "s_mul_i32 s50, s72, 141312\n"
    "s_add_u32 s50, s50, 23552\n"
    "v_mfma_f32_16x16x32_bf16 v[140:143], v[176:179], a[68:71], 0\n"
    "s_xor_b32 s72, s38, 1\n"
    "s_mul_i32 s73, s72, 0\n"
    "s_add_u32 s50, s50, s73\n"
    "v_mfma_f32_16x16x32_bf16 v[140:143], v[180:183], a[72:75], v[140:143]\n"
    "s_mul_i32 s73, s72, 0\n"
    "ds_read_b128 a[68:71], v19 offset:576\n"
    "ds_read_b128 a[8:11], v18 offset:1312\n"
    "v_mfma_f32_16x16x32_bf16 v[140:143], v[184:187], a[76:79], v[140:143]\n"
    "s_waitcnt vmcnt(4)\n"
    "v_cmp_ne_u32 vcc, v36, v38\n"
    "s_cbranch_vccz 15f\n"
    "16:\n"
    "v_mfma_f32_16x16x32_bf16 v[140:143], v[188:191], a[80:83], v[140:143]\n"
    "s_waitcnt lgkmcnt(3)\n"
    "s_sub_u32 s62, 3072, s73\n"
    "s_xor_b32 s72, s42, 2\n"
    "v_mfma_f32_32x32x16_bf16 v[48:63], a[220:223], a[232:235], v[48:63]\n"
    "s_min_u32 s73, s72, 1\n"
    "s_mul_i32 s72, s72, 5\n"
    "s_add_u32 s72, s72, s73\n"
    "s_mul_i32 s49, s72, 2816\n"
    "s_add_u32 s49, s49, 100352\n"
    "s_mov_b32 s26, s18\n"
    "s_mov_b32 s27, s19\n"
    "v_mfma_f32_16x16x32_bf16 v[140:143], v[192:195], a[84:87], v[140:143]\n"
    "s_movk_i32 s72, 8192\n"
    "ds_read_b64_tr_b16 a[224:225], v12 offset:2816\n"
    "ds_read_b64_tr_b16 a[226:227], v12 offset:3072\n"
    "v_mfma_f32_16x16x32_bf16 v[140:143], v[196:199], a[88:91], v[140:143]\n"
    "s_add_u32 s58, s51, 0\n"
    "ds_read_b128 a[12:15], v18 offset:1888\n"
    "buffer_load_dwordx4 v[40:43], v5, s[12:15], s72 offen\n"
    "v_mfma_f32_16x16x32_bf16 v[140:143], v[200:203], a[92:95], v[140:143]\n"
    "s_mul_i32 s53, s39, 72\n"
    "v_cvt_pk_bf16_f32 v48, v48, v49\n"
    "v_cvt_pk_bf16_f32 v49, v50, v51\n"
    "v_mfma_f32_16x16x32_bf16 v[144:147], v[176:179], a[72:75], 0\n"
    "s_add_u32 s53, s53, s41\n"
    "v_cvt_pk_bf16_f32 v50, v52, v53\n"
    "v_cvt_pk_bf16_f32 v51, v54, v55\n"
    "v_mfma_f32_16x16x32_bf16 v[144:147], v[180:183], a[76:79], v[144:147]\n"
    "s_mul_i32 s53, s53, 43008\n"
    "v_cvt_pk_bf16_f32 v52, v56, v57\n"
    "v_cvt_pk_bf16_f32 v53, v58, v59\n"
    "v_mfma_f32_16x16x32_bf16 v[144:147], v[184:187], a[80:83], v[144:147]\n"
    "s_lshl_b32 s73, s40, 4\n"
    "v_cvt_pk_bf16_f32 v54, v60, v61\n"
    "v_cvt_pk_bf16_f32 v55, v62, v63\n"
    "v_mfma_f32_16x16x32_bf16 v[144:147], v[188:191], a[84:87], v[144:147]\n"
    "s_add_u32 s73, s73, s42\n"
    "ds_read_b128 a[72:75], v19 offset:1152\n"
    "ds_read_b128 a[16:19], v18 offset:2464\n"
    "v_mfma_f32_16x16x32_bf16 v[144:147], v[192:195], a[88:91], v[144:147]\n"
    "s_waitcnt lgkmcnt(3)\n"
    "s_lshl_b32 s73, s73, 1\n"
    "s_add_u32 s73, s73, s38\n"
    "v_mfma_f32_32x32x16_bf16 v[64:79], a[224:227], a[228:231], 0\n"
    "s_mul_i32 s73, s73, 192\n"
    "ds_write_b128 v15, v[48:51] offset:50176\n"
    "s_add_u32 s53, s53, s73\n"
    "v_mfma_f32_16x16x32_bf16 v[144:147], v[196:199], a[92:95], v[144:147]\n"
    "s_lshl_b32 s73, s37, 6\n"
    "s_add_u32 s53, s53, s73\n"
    "s_add_u32 m0, s48, 0\n"
    "v_mfma_f32_16x16x32_bf16 v[144:147], v[200:203], a[96:99], v[144:147]\n"
    "v_add_u32 v10, s58, v6\n"
    "v_mfma_f32_16x16x32_bf16 v[148:151], v[176:179], a[76:79], 0\n"
    "ds_read_b64_tr_b16 a[220:221], v12 offset:3200\n"
    "ds_read_b64_tr_b16 a[222:223], v12 offset:3456\n"
    "v_mfma_f32_16x16x32_bf16 v[148:151], v[180:183], a[80:83], v[148:151]\n"
    "ds_read_b128 a[76:79], v19 offset:1728\n"
    "ds_read_b128 a[20:23], v18 offset:3040\n"
    "v_mfma_f32_16x16x32_bf16 v[148:151], v[184:187], a[84:87], v[148:151]\n"
    "ds_read_b128 a[24:27], v18 offset:3616\n"
    "buffer_load_dwordx4 v10, s[16:19], 0 offen lds\n"
    "v_mfma_f32_16x16x32_bf16 v[148:151], v[188:191], a[88:91], v[148:151]\n"
    "s_add_u32 m0, s48, 1024\n"
    "v_add_u32 v10, s58, v7\n"
    "v_mfma_f32_16x16x32_bf16 v[148:151], v[192:195], a[92:95], v[148:151]\n"
    "buffer_load_dwordx4 v10, s[16:19], 0 offen lds\n"
    "v_mfma_f32_16x16x32_bf16 v[148:151], v[196:199], a[96:99], v[148:151]\n"
    "s_waitcnt lgkmcnt(3)\n"
    "ds_read_b128 a[28:31], v18 offset:4192\n"
    "v_mfma_f32_32x32x16_bf16 v[64:79], a[220:223], a[232:235], v[64:79]\n"
    "ds_write_b128 v15, v[52:55] offset:50752\n"
    "v_mfma_f32_16x16x32_bf16 v[148:151], v[200:203], a[100:103], v[148:151]\n"
    "v_mfma_f32_16x16x32_bf16 v[152:155], v[176:179], a[80:83], 0\n"
    "v_mfma_f32_16x16x32_bf16 v[152:155], v[180:183], a[84:87], v[152:155]\n"
    "s_add_u32 m0, s48, 2048\n"
    "ds_read_b64_tr_b16 a[224:225], v12 offset:5632\n"
    "ds_read_b64_tr_b16 a[226:227], v12 offset:5888\n"
    "v_mfma_f32_16x16x32_bf16 v[152:155], v[184:187], a[88:91], v[152:155]\n"
    "v_cvt_pk_bf16_f32 v64, v64, v65\n"
    "v_cvt_pk_bf16_f32 v65, v66, v67\n"
    "v_mfma_f32_16x16x32_bf16 v[152:155], v[188:191], a[92:95], v[152:155]\n"
    "v_cvt_pk_bf16_f32 v66, v68, v69\n"
    "v_cvt_pk_bf16_f32 v67, v70, v71\n"
    "v_mfma_f32_16x16x32_bf16 v[152:155], v[192:195], a[96:99], v[152:155]\n"
    "v_cvt_pk_bf16_f32 v68, v72, v73\n"
    "v_cvt_pk_bf16_f32 v69, v74, v75\n"
    "v_mfma_f32_16x16x32_bf16 v[152:155], v[196:199], a[100:103], v[152:155]\n"
    "v_cvt_pk_bf16_f32 v70, v76, v77\n"
    "v_cvt_pk_bf16_f32 v71, v78, v79\n"
    "v_mfma_f32_16x16x32_bf16 v[152:155], v[200:203], a[104:107], v[152:155]\n"
    "s_waitcnt lgkmcnt(0)\n"
    "ds_read_b128 a[80:83], v19 offset:2304\n"
    "ds_read_b128 a[32:35], v18 offset:4768\n"
    "v_mfma_f32_32x32x16_bf16 v[48:63], a[224:227], a[228:231], 0\n"
    "ds_write_b128 v15, v[64:67] offset:51328\n"
    "v_add_u32 v10, s58, v8\n"
    "v_mfma_f32_16x16x32_bf16 v[156:159], v[176:179], a[84:87], 0\n"
    "s_mov_b32 exec_hi, 0xffff\n"
    "buffer_load_dwordx4 v10, s[16:19], 0 offen lds\n"
    "s_mov_b32 exec_hi, -1\n"
    "v_mfma_f32_16x16x32_bf16 v[156:159], v[180:183], a[88:91], v[156:159]\n"
    "s_add_u32 s58, s51, 172032\n"
    "buffer_load_dword v22, v17, s[28:31], s74 offen sc1\n"
    "s_add_u32 m0, s48, 2816\n"
    "v_mfma_f32_16x16x32_bf16 v[156:159], v[184:187], a[92:95], v[156:159]\n"
    "ds_read_b64_tr_b16 a[220:221], v12 offset:6016\n"
    "ds_read_b64_tr_b16 a[222:223], v12 offset:6272\n"
    "v_mfma_f32_16x16x32_bf16 v[156:159], v[188:191], a[96:99], v[156:159]\n"
    "ds_read_b128 a[84:87], v19 offset:2880\n"
    "ds_read_b128 a[36:39], v18 offset:5344\n"
    "v_mfma_f32_16x16x32_bf16 v[156:159], v[192:195], a[100:103], v[156:159]\n"
    "ds_read_b128 a[40:43], v18 offset:5920\n"
    "v_add_u32 v10, s58, v6\n"
    "v_mfma_f32_16x16x32_bf16 v[156:159], v[196:199], a[104:107], v[156:159]\n"
    "ds_read_b128 a[44:47], v18 offset:6496\n"
    "buffer_load_dwordx4 v10, s[16:19], 0 offen lds\n"
    "v_mfma_f32_16x16x32_bf16 v[156:159], v[200:203], a[108:111], v[156:159]\n"
    "v_mfma_f32_16x16x32_bf16 v[160:163], v[176:179], a[88:91], 0\n"
    "s_waitcnt lgkmcnt(4)\n"
    "s_add_u32 m0, s48, 3840\n"
    "v_add_u32 v10, s58, v7\n"
    "v_mfma_f32_32x32x16_bf16 v[48:63], a[220:223], a[232:235], v[48:63]\n"
    "ds_write_b128 v15, v[68:71] offset:51904\n"
    "buffer_load_dwordx4 v10, s[16:19], 0 offen lds\n"
    "v_mfma_f32_16x16x32_bf16 v[160:163], v[180:183], a[92:95], v[160:163]\n"
    "v_mfma_f32_16x16x32_bf16 v[160:163], v[184:187], a[96:99], v[160:163]\n"
    "v_mfma_f32_16x16x32_bf16 v[160:163], v[188:191], a[100:103], v[160:163]\n"
    "ds_read_b64_tr_b16 a[224:225], v31\n"
    "ds_read_b64_tr_b16 a[226:227], v32\n"
    "v_mfma_f32_16x16x32_bf16 v[160:163], v[192:195], a[104:107], v[160:163]\n"
    "v_cvt_pk_bf16_f32 v48, v48, v49\n"
    "v_cvt_pk_bf16_f32 v49, v50, v51\n"
    "v_mfma_f32_16x16x32_bf16 v[160:163], v[196:199], a[108:111], v[160:163]\n"
    "s_add_u32 m0, s48, 4864\n"
    "v_cvt_pk_bf16_f32 v50, v52, v53\n"
    "v_cvt_pk_bf16_f32 v51, v54, v55\n"
    "v_mfma_f32_16x16x32_bf16 v[160:163], v[200:203], a[112:115], v[160:163]\n"
    "v_cvt_pk_bf16_f32 v52, v56, v57\n"
    "v_cvt_pk_bf16_f32 v53, v58, v59\n"
    "v_mfma_f32_16x16x32_bf16 v[164:167], v[176:179], a[92:95], 0\n"
    "s_waitcnt lgkmcnt(0)\n"
    "v_cvt_pk_bf16_f32 v54, v60, v61\n"
    "v_cvt_pk_bf16_f32 v55, v62, v63\n"
    "v_mfma_f32_32x32x16_bf16 v[80:95], a[224:227], a[240:243], v[80:95]\n"
    "s_waitcnt vmcnt(7)\n"
    "ds_write_b128 v15, v[48:51] offset:52480\n"
    "s_barrier\n"
    "v_mfma_f32_16x16x32_bf16 v[164:167], v[180:183], a[96:99], v[164:167]\n"
    "s_mov_b64 exec, 1\n"
    "buffer_store_dword v39, v3, s[28:31], s60 offen sc0 sc1\n"
    "s_mov_b64 exec, -1\n"
    "v_mfma_f32_16x16x32_bf16 v[164:167], v[184:187], a[100:103], v[164:167]\n"
    "v_add_u32 v10, s58, v8\n"
    "v_mfma_f32_16x16x32_bf16 v[164:167], v[188:191], a[104:107], v[164:167]\n"
    "ds_read_b64_tr_b16 a[220:221], v12 offset:8448\n"
    "ds_read_b64_tr_b16 a[222:223], v12 offset:8704\n"
    "v_mfma_f32_16x16x32_bf16 v[164:167], v[192:195], a[108:111], v[164:167]\n"
    "v_cvt_pk_bf16_f32 v80, v80, v81\n"
    "v_cvt_pk_bf16_f32 v81, v82, v83\n"
    "v_mfma_f32_16x16x32_bf16 v[164:167], v[196:199], a[112:115], v[164:167]\n"
    "v_cvt_pk_bf16_f32 v82, v84, v85\n"
    "v_cvt_pk_bf16_f32 v83, v86, v87\n"
    "v_mfma_f32_16x16x32_bf16 v[164:167], v[200:203], a[116:119], v[164:167]\n"
    "v_cvt_pk_bf16_f32 v84, v88, v89\n"
    "v_cvt_pk_bf16_f32 v85, v90, v91\n"
    "v_mfma_f32_16x16x32_bf16 v[168:171], v[176:179], a[96:99], 0\n"
    "v_cvt_pk_bf16_f32 v86, v92, v93\n"
    "v_cvt_pk_bf16_f32 v87, v94, v95\n"
    "v_mfma_f32_16x16x32_bf16 v[168:171], v[180:183], a[100:103], v[168:171]\n"
    "s_waitcnt lgkmcnt(0)\n"
    "ds_read_b128 a[88:91], v19 offset:3456\n"
    "ds_read_b128 a[48:51], v18 offset:7072\n"
    "v_mfma_f32_32x32x16_bf16 v[64:79], a[220:223], a[228:231], 0\n"
    "ds_write_b128 v15, v[52:55] offset:53056\n"
    "v_mfma_f32_16x16x32_bf16 v[168:171], v[184:187], a[104:107], v[168:171]\n"
    "s_mov_b32 exec_hi, 0xffff\n"
    "buffer_load_dwordx4 v10, s[16:19], 0 offen lds\n"
    "s_mov_b32 exec_hi, -1\n"
    "v_mfma_f32_16x16x32_bf16 v[168:171], v[188:191], a[108:111], v[168:171]\n"
    "s_add_u32 s58, s51, 344064\n"
    "s_add_u32 m0, s48, 5632\n"
    "v_mfma_f32_16x16x32_bf16 v[168:171], v[192:195], a[112:115], v[168:171]\n"
    "ds_read_b64_tr_b16 a[224:225], v12 offset:8832\n"
    "ds_read_b64_tr_b16 a[226:227], v12 offset:9088\n"
    "v_mfma_f32_16x16x32_bf16 v[168:171], v[196:199], a[116:119], v[168:171]\n"
    "ds_read_b128 a[92:95], v19 offset:4032\n"
    "ds_read_b128 a[52:55], v18 offset:7648\n"
    "v_mfma_f32_16x16x32_bf16 v[168:171], v[200:203], a[120:123], v[168:171]\n"
    "ds_read_b128 a[56:59], v18 offset:8224\n"
    "buffer_load_dwordx4 v6, s[16:19], s58 offen lds\n"
    "v_mfma_f32_16x16x32_bf16 v[176:179], v[216:219], a[0:3], 0\n"
    "s_waitcnt vmcnt(5)\n"
    "v_cmp_eq_u32 vcc, 0, v22\n"
    "s_cbranch_vccnz 151f\n"
    "161:\n"
    "v_mfma_f32_16x16x32_bf16 v[176:179], v[220:223], a[4:7], v[176:179]\n"
    "buffer_load_dwordx4 a[128:131], v5, s[28:31], s50 offen offset:0 sc1\n"
    "ds_read_b128 a[96:99], v19 offset:4608\n"
    "v_mfma_f32_16x16x32_bf16 v[176:179], v[224:227], a[8:11], v[176:179]\n"
    "s_waitcnt lgkmcnt(4)\n"
    "s_add_u32 m0, s48, 6656\n"
    "v_mfma_f32_32x32x16_bf16 v[64:79], a[224:227], a[232:235], v[64:79]\n"
    "ds_write_b128 v16, v[80:83] offset:50176\n"
    "buffer_load_dwordx4 v7, s[16:19], s58 offen lds\n"
    "v_mfma_f32_16x16x32_bf16 v[176:179], v[228:231], a[12:15], v[176:179]\n"
    "v_mfma_f32_16x16x32_bf16 v[176:179], v[232:235], a[16:19], v[176:179]\n"
    "buffer_load_dwordx4 a[132:135], v5, s[28:31], s50 offen offset:1024 sc1\n"
    "v_mfma_f32_16x16x32_bf16 v[176:179], v[236:239], a[20:23], v[176:179]\n"
    "ds_read_b64_tr_b16 a[220:221], v12 offset:11264\n"
    "ds_read_b64_tr_b16 a[222:223], v12 offset:11520\n"
    "v_mfma_f32_16x16x32_bf16 v[176:179], v[240:243], a[24:27], v[176:179]\n"
    "v_cvt_pk_bf16_f32 v64, v64, v65\n"
    "v_cvt_pk_bf16_f32 v65, v66, v67\n"
    "v_mfma_f32_16x16x32_bf16 v[180:183], v[216:219], a[4:7], 0\n"
    "s_add_u32 m0, s48, 7680\n"
    "v_cvt_pk_bf16_f32 v66, v68, v69\n"
    "v_cvt_pk_bf16_f32 v67, v70, v71\n"
    "v_mfma_f32_16x16x32_bf16 v[180:183], v[220:223], a[8:11], v[180:183]\n"
    "v_cvt_pk_bf16_f32 v68, v72, v73\n"
    "v_cvt_pk_bf16_f32 v69, v74, v75\n"
    "v_mfma_f32_16x16x32_bf16 v[180:183], v[224:227], a[12:15], v[180:183]\n"
    "v_cvt_pk_bf16_f32 v70, v76, v77\n"
    "v_cvt_pk_bf16_f32 v71, v78, v79\n"
    "v_mfma_f32_16x16x32_bf16 v[180:183], v[228:231], a[16:19], v[180:183]\n"
    "s_waitcnt lgkmcnt(0)\n"
    "buffer_load_dwordx4 a[136:139], v5, s[28:31], s50 offen offset:2048 sc1\n"
    "ds_read_b128 a[100:103], v19 offset:5184\n"
    "v_mfma_f32_32x32x16_bf16 v[48:63], a[220:223], a[228:231], 0\n"
    "ds_write_b128 v15, v[64:67] offset:53632\n"
    "v_mfma_f32_16x16x32_bf16 v[180:183], v[232:235], a[20:23], v[180:183]\n"
    "s_mov_b32 exec_hi, 0xffff\n"
    "buffer_load_dwordx4 v8, s[16:19], s58 offen lds\n"
    "s_mov_b32 exec_hi, -1\n"
    "v_mfma_f32_16x16x32_bf16 v[180:183], v[236:239], a[24:27], v[180:183]\n"
    "s_add_u32 s58, s51, 516096\n"
    "buffer_load_dwordx4 a[140:143], v5, s[28:31], s50 offen offset:3072 sc1\n"
    "s_add_u32 s50, s50, 4096\n"
    "v_mfma_f32_16x16x32_bf16 v[180:183], v[240:243], a[28:31], v[180:183]\n"
    "s_add_u32 m0, s48, 8448\n"
    "ds_read_b64_tr_b16 a[224:225], v12 offset:11648\n"
    "ds_read_b64_tr_b16 a[226:227], v12 offset:11904\n"
    "v_mfma_f32_16x16x32_bf16 v[184:187], v[216:219], a[8:11], 0\n"
    "ds_read_b128 a[104:107], v19 offset:5760\n"
    "buffer_load_dwordx4 v6, s[16:19], s58 offen lds\n"
    "v_mfma_f32_16x16x32_bf16 v[184:187], v[220:223], a[12:15], v[184:187]\n"
    "v_mfma_f32_16x16x32_bf16 v[184:187], v[224:227], a[16:19], v[184:187]\n"
    "buffer_load_dwordx4 a[144:147], v5, s[28:31], s50 offen offset:0 sc1\n"
    "v_mfma_f32_16x16x32_bf16 v[184:187], v[228:231], a[20:23], v[184:187]\n"
    "v_mfma_f32_16x16x32_bf16 v[184:187], v[232:235], a[24:27], v[184:187]\n"
    "s_waitcnt lgkmcnt(1)\n"
    "s_add_u32 m0, s48, 9472\n"
    "v_mfma_f32_32x32x16_bf16 v[48:63], a[224:227], a[232:235], v[48:63]\n"
    "ds_write_b128 v15, v[68:71] offset:54208\n"
    "buffer_load_dwordx4 v7, s[16:19], s58 offen lds\n"
    "v_mfma_f32_16x16x32_bf16 v[184:187], v[236:239], a[28:31], v[184:187]\n"
    "v_mfma_f32_16x16x32_bf16 v[184:187], v[240:243], a[32:35], v[184:187]\n"
    "buffer_load_dwordx4 a[148:151], v5, s[28:31], s50 offen offset:1024 sc1\n"
    "v_mfma_f32_16x16x32_bf16 v[188:191], v[216:219], a[12:15], 0\n"
    "ds_read_b64_tr_b16 a[220:221], v37 offset:14080\n"
    "ds_read_b64_tr_b16 a[222:223], v37 offset:14336\n"
    "v_mfma_f32_16x16x32_bf16 v[188:191], v[220:223], a[16:19], v[188:191]\n"
    "v_cvt_pk_bf16_f32 v48, v48, v49\n"
    "v_cvt_pk_bf16_f32 v49, v50, v51\n"
    "v_mfma_f32_16x16x32_bf16 v[188:191], v[224:227], a[20:23], v[188:191]\n"
    "s_add_u32 m0, s48, 10496\n"
    "v_cvt_pk_bf16_f32 v50, v52, v53\n"
    "v_cvt_pk_bf16_f32 v51, v54, v55\n"
    "v_mfma_f32_16x16x32_bf16 v[188:191], v[228:231], a[24:27], v[188:191]\n"
    "v_cvt_pk_bf16_f32 v52, v56, v57\n"
    "v_cvt_pk_bf16_f32 v53, v58, v59\n"
    "v_mfma_f32_16x16x32_bf16 v[188:191], v[232:235], a[28:31], v[188:191]\n"
    "v_cvt_pk_bf16_f32 v54, v60, v61\n"
    "v_cvt_pk_bf16_f32 v55, v62, v63\n"
    "v_mfma_f32_16x16x32_bf16 v[188:191], v[236:239], a[32:35], v[188:191]\n"
    "s_waitcnt lgkmcnt(0)\n"
    "buffer_load_dwordx4 a[152:155], v5, s[28:31], s50 offen offset:2048 sc1\n"
    "v_mfma_f32_32x32x16_bf16 v[64:79], a[220:223], a[228:231], 0\n"
    "ds_write_b128 v15, v[48:51] offset:54784\n"
    "v_mfma_f32_16x16x32_bf16 v[188:191], v[240:243], a[36:39], v[188:191]\n"
    "s_mov_b32 exec_hi, 0xffff\n"
    "buffer_load_dwordx4 v8, s[16:19], s58 offen lds\n"
    "s_mov_b32 exec_hi, -1\n"
    "v_mfma_f32_16x16x32_bf16 v[192:195], v[216:219], a[16:19], 0\n"
    "s_add_u32 s58, s51, 688128\n"
    "s_add_u32 m0, s48, 11264\n"
    "v_mfma_f32_16x16x32_bf16 v[192:195], v[220:223], a[20:23], v[192:195]\n"
    "ds_read_b64_tr_b16 a[224:225], v37 offset:14464\n"
    "ds_read_b64_tr_b16 a[226:227], v37 offset:14720\n"
    "v_mfma_f32_16x16x32_bf16 v[192:195], v[224:227], a[24:27], v[192:195]\n"
    "v_xor_b32 v12, 32, v12\n"
    "v_xor_b32 v13, 32, v13\n"
    "v_mfma_f32_16x16x32_bf16 v[192:195], v[228:231], a[28:31], v[192:195]\n"
    "v_xor_b32 v14, 32, v14\n"
    "v_xor_b32 v31, 32, v31\n"
    "v_mfma_f32_16x16x32_bf16 v[192:195], v[232:235], a[32:35], v[192:195]\n"
    "v_xor_b32 v32, 32, v32\n"
    "v_xor_b32 v37, 32, v37\n"
    "v_mfma_f32_16x16x32_bf16 v[192:195], v[236:239], a[36:39], v[192:195]\n"
    "buffer_load_dwordx4 v6, s[16:19], s58 offen lds\n"
    "v_mfma_f32_16x16x32_bf16 v[192:195], v[240:243], a[40:43], v[192:195]\n"
    "s_waitcnt lgkmcnt(0)\n"
    "s_add_u32 m0, s48, 12288\n"
    "v_mfma_f32_32x32x16_bf16 v[64:79], a[224:227], a[232:235], v[64:79]\n"
    "ds_write_b128 v15, v[52:55] offset:55360\n"
    "buffer_load_dwordx4 v7, s[16:19], s58 offen lds\n"
    "v_mfma_f32_16x16x32_bf16 v[196:199], v[216:219], a[20:23], 0\n"
    "v_mfma_f32_16x16x32_bf16 v[196:199], v[220:223], a[24:27], v[196:199]\n"
    "v_mfma_f32_16x16x32_bf16 v[196:199], v[224:227], a[28:31], v[196:199]\n"
    "v_cvt_pk_bf16_f32 v64, v64, v65\n"
    "v_cvt_pk_bf16_f32 v65, v66, v67\n"
    "v_mfma_f32_16x16x32_bf16 v[196:199], v[228:231], a[32:35], v[196:199]\n"
    "v_cvt_pk_bf16_f32 v66, v68, v69\n"
    "v_cvt_pk_bf16_f32 v67, v70, v71\n"
    "v_mfma_f32_16x16x32_bf16 v[196:199], v[232:235], a[36:39], v[196:199]\n"
    "s_add_u32 m0, s48, 13312\n"
    "v_cvt_pk_bf16_f32 v68, v72, v73\n"
    "v_cvt_pk_bf16_f32 v69, v74, v75\n"
    "v_mfma_f32_16x16x32_bf16 v[196:199], v[236:239], a[40:43], v[196:199]\n"
    "v_cvt_pk_bf16_f32 v70, v76, v77\n"
    "v_cvt_pk_bf16_f32 v71, v78, v79\n"
    "v_mfma_f32_16x16x32_bf16 v[196:199], v[240:243], a[44:47], v[196:199]\n"
    "ds_write_b128 v16, v[84:87] offset:50752\n"
    "v_mfma_f32_16x16x32_bf16 v[200:203], v[216:219], a[24:27], 0\n"
    "s_mov_b32 exec_hi, 0xffff\n"
    "buffer_load_dwordx4 v8, s[16:19], s58 offen lds\n"
    "s_mov_b32 exec_hi, -1\n"
    "v_mfma_f32_16x16x32_bf16 v[200:203], v[220:223], a[28:31], v[200:203]\n"
    "v_mfma_f32_16x16x32_bf16 v[200:203], v[224:227], a[32:35], v[200:203]\n"
    "ds_write_b128 v33, v[64:67] offset:55936\n"
    "v_mfma_f32_16x16x32_bf16 v[200:203], v[228:231], a[36:39], v[200:203]\n"
    "s_mov_b32 m0, s54\n"
    "v_mfma_f32_16x16x32_bf16 v[200:203], v[232:235], a[40:43], v[200:203]\n"
    "buffer_load_dwordx4 v6, s[16:19], s57 offen lds\n"
    "v_mfma_f32_16x16x32_bf16 v[200:203], v[236:239], a[44:47], v[200:203]\n"
    "ds_write_b128 v33, v[68:71] offset:56512\n"
    "v_mfma_f32_16x16x32_bf16 v[200:203], v[240:243], a[48:51], v[200:203]\n"
    "v_mfma_f32_16x16x32_bf16 v[204:207], v[216:219], a[28:31], 0\n"
    "buffer_load_dwordx4 a[156:159], v5, s[28:31], s50 offen offset:3072 sc1\n"
    "s_add_u32 s50, s50, 4096\n"
    "v_mfma_f32_16x16x32_bf16 v[204:207], v[220:223], a[32:35], v[204:207]\n"
    "v_mfma_f32_16x16x32_bf16 v[204:207], v[224:227], a[36:39], v[204:207]\n"
    "s_mov_b32 m0, s55\n"
    "v_mfma_f32_16x16x32_bf16 v[204:207], v[228:231], a[40:43], v[204:207]\n"
    "buffer_load_dwordx4 v7, s[16:19], s57 offen lds\n"
    "v_mfma_f32_16x16x32_bf16 v[204:207], v[232:235], a[44:47], v[204:207]\n"
    "v_mfma_f32_16x16x32_bf16 v[204:207], v[236:239], a[48:51], v[204:207]\n"
    "s_waitcnt lgkmcnt(0)\n"
    "s_barrier\n"
    "v_mfma_f32_16x16x32_bf16 v[204:207], v[240:243], a[52:55], v[204:207]\n"
    "ds_read_b64_tr_b16 a[220:221], v12 offset:0\n"
    "ds_read_b64_tr_b16 a[222:223], v12 offset:256\n"
    "v_mfma_f32_16x16x32_bf16 v[208:211], v[216:219], a[32:35], 0\n"
    "buffer_load_dwordx4 a[160:163], v5, s[28:31], s50 offen offset:0 sc1\n"
    "ds_read_b128 a[0:3], v18 offset:50176\n"
    "v_mfma_f32_16x16x32_bf16 v[208:211], v[220:223], a[36:39], v[208:211]\n"
    "s_mov_b64 exec, 1\n"
    "buffer_store_dword v39, v35, s[28:31], s65 offen offset:1024 sc0 sc1\n"
    "s_mov_b64 exec, -1\n"
    "v_mfma_f32_16x16x32_bf16 v[208:211], v[224:227], a[40:43], v[208:211]\n"
    "s_mov_b32 m0, s56\n"
    "ds_read_b64_tr_b16 a[224:225], v12 offset:384\n"
    "ds_read_b64_tr_b16 a[226:227], v12 offset:640\n"
    "v_mfma_f32_16x16x32_bf16 v[208:211], v[228:231], a[44:47], v[208:211]\n"
    "s_waitcnt lgkmcnt(3)\n"
    "ds_read_b128 a[4:7], v18 offset:50752\n"
    "v_mfma_f32_32x32x16_bf16 v[48:63], a[220:223], a[228:231], 0\n"
    "ds_read_b128 a[8:11], v18 offset:51328\n"
    "s_mov_b32 exec_hi, 0xffff\n"
    "buffer_load_dwordx4 v8, s[16:19], s57 offen lds\n"
    "s_mov_b32 exec_hi, -1\n"
    "v_mfma_f32_16x16x32_bf16 v[208:211], v[232:235], a[48:51], v[208:211]\n"
    "s_add_u32 s51, s51, 64\n"
    "ds_read_b128 a[12:15], v18 offset:51904\n"
    "s_add_u32 s57, s57, s59\n"
    "v_mfma_f32_16x16x32_bf16 v[208:211], v[236:239], a[52:55], v[208:211]\n"
    "ds_read_b128 a[16:19], v18 offset:52480\n"
    "v_mfma_f32_16x16x32_bf16 v[208:211], v[240:243], a[56:59], v[208:211]\n"
    "ds_read_b128 a[20:23], v18 offset:53056\n"
    "v_mfma_f32_16x16x32_bf16 v[216:219], a[212:215], a[64:67], 0\n"
    "buffer_load_dwordx4 a[164:167], v5, s[28:31], s50 offen offset:1024 sc1\n"
    "ds_read_b128 a[24:27], v18 offset:53632\n"
    "v_mfma_f32_16x16x32_bf16 v[216:219], a[216:219], a[72:75], v[216:219]\n"
    "s_waitcnt vmcnt(17)\n"
    "ds_read_b64_tr_b16 a[220:221], v13\n"
    "ds_read_b64_tr_b16 a[222:223], v14\n"
    "v_mfma_f32_16x16x32_bf16 v[220:223], a[212:215], a[68:71], 0\n"
    "s_waitcnt lgkmcnt(8)\n"
    "ds_read_b128 a[28:31], v18 offset:54208\n"
    "v_mfma_f32_32x32x16_bf16 v[48:63], a[224:227], a[232:235], v[48:63]\n"
    "ds_read_b128 a[32:35], v18 offset:54784\n"
    "v_mfma_f32_16x16x32_bf16 v[220:223], a[216:219], a[76:79], v[220:223]\n"
    "ds_read_b128 a[36:39], v18 offset:55360\n"
    "v_mfma_f32_16x16x32_bf16 v[224:227], a[212:215], a[72:75], 0\n"
    "ds_read_b128 a[40:43], v18 offset:55936\n"
    "v_mfma_f32_16x16x32_bf16 v[224:227], a[216:219], a[80:83], v[224:227]\n"
    "v_cvt_pk_bf16_f32 v48, v48, v49\n"
    "v_cvt_pk_bf16_f32 v49, v50, v51\n"
    "v_mfma_f32_16x16x32_bf16 v[228:231], a[212:215], a[76:79], 0\n"
    "v_cvt_pk_bf16_f32 v50, v52, v53\n"
    "v_cvt_pk_bf16_f32 v51, v54, v55\n"
    "v_mfma_f32_16x16x32_bf16 v[228:231], a[216:219], a[84:87], v[228:231]\n"
    "v_cvt_pk_bf16_f32 v52, v56, v57\n"
    "v_cvt_pk_bf16_f32 v53, v58, v59\n"
    "v_mfma_f32_16x16x32_bf16 v[232:235], a[212:215], a[80:83], 0\n"
    "s_waitcnt lgkmcnt(4)\n"
    "v_cvt_pk_bf16_f32 v54, v60, v61\n"
    "v_cvt_pk_bf16_f32 v55, v62, v63\n"
    "v_mfma_f32_32x32x16_bf16 v[80:95], a[220:223], a[236:239], 0\n"
    "ds_read_b64_tr_b16 a[224:225], v12 offset:2816\n"
    "ds_read_b64_tr_b16 a[226:227], v12 offset:3072\n"
    "buffer_load_dwordx4 a[168:171], v5, s[28:31], s50 offen offset:2048 sc1\n"
    "ds_read_b128 a[44:47], v18 offset:56512\n"
    "v_mfma_f32_16x16x32_bf16 v[232:235], a[216:219], a[88:91], v[232:235]\n"
    "ds_read_b128 a[48:51], v18 offset:57088\n"
    "v_mfma_f32_16x16x32_bf16 v[236:239], a[212:215], a[84:87], 0\n"
    "ds_read_b128 a[52:55], v18 offset:57664\n"
    "v_mfma_f32_16x16x32_bf16 v[236:239], a[216:219], a[92:95], v[236:239]\n"
    "ds_read_b128 a[56:59], v18 offset:58240\n"
    "v_mfma_f32_16x16x32_bf16 v[240:243], a[212:215], a[88:91], 0\n"
    "v_mfma_f32_16x16x32_bf16 v[240:243], a[216:219], a[96:99], v[240:243]\n"
    "v_mfma_f32_16x16x32_bf16 v[244:247], a[212:215], a[92:95], 0\n"
    "s_waitcnt lgkmcnt(4)\n"
    "v_mfma_f32_32x32x16_bf16 v[64:79], a[224:227], a[228:231], 0\n"
    "ds_write_b128 v15, v[48:51] offset:0\n"
    "v_mfma_f32_16x16x32_bf16 v[244:247], a[216:219], a[100:103], v[244:247]\n"
    "v_mfma_f32_16x16x32_bf16 v[248:251], a[212:215], a[96:99], 0\n"
    "v_mfma_f32_16x16x32_bf16 v[248:251], a[216:219], a[104:107], v[248:251]\n"
    "s_waitcnt vmcnt(12) lgkmcnt(4)\n"
    ".p2align 6\n"
    "11:\n"
    "v_mfma_f32_16x16x32_bf16 v[96:99], a[128:131], a[0:3], v[96:99]\n"
    "ds_read_b64_tr_b16 a[220:221], v12 offset:3200\n"
    "ds_read_b64_tr_b16 a[222:223], v12 offset:3456\n"
    "v_mfma_f32_16x16x32_bf16 v[96:99], a[132:135], a[4:7], v[96:99]\n"
    "buffer_load_dwordx4 a[172:175], v5, s[28:31], s50 offen offset:3072 sc1\n"
    "s_add_u32 s50, s50, 4096\n"
    "v_mfma_f32_16x16x32_bf16 v[96:99], a[136:139], a[8:11], v[96:99]\n"
    "ds_read_b128 a[64:67], v18 offset:50256\n"
    "v_mfma_f32_16x16x32_bf16 v[96:99], a[140:143], a[12:15], v[96:99]\n"
    "buffer_load_dwordx4 a[176:179], v5, s[28:31], s50 offen offset:0 sc1\n"
    "v_mfma_f32_16x16x32_bf16 v[96:99], a[144:147], a[16:19], v[96:99]\n"
    "ds_read_b128 a[68:71], v18 offset:50832\n"
    "v_mfma_f32_16x16x32_bf16 v[96:99], a[148:151], a[20:23], v[96:99]\n"
    "buffer_load_dwordx4 a[180:183], v5, s[28:31], s50 offen offset:1024 sc1\n"
    "v_mfma_f32_16x16x32_bf16 v[96:99], a[152:155], a[24:27], v[96:99]\n"
    "s_waitcnt lgkmcnt(2)\n"
    "v_mfma_f32_32x32x16_bf16 v[64:79], a[220:223], a[232:235], v[64:79]\n"
    "ds_write_b128 v15, v[52:55] offset:576\n"
    "v_mfma_f32_16x16x32_bf16 v[100:103], a[128:131], a[4:7], v[100:103]\n"
    "v_mfma_f32_16x16x32_bf16 v[100:103], a[132:135], a[8:11], v[100:103]\n"
    "v_mfma_f32_16x16x32_bf16 v[100:103], a[136:139], a[12:15], v[100:103]\n"
    "v_cvt_pk_bf16_f32 v64, v64, v65\n"
    "v_cvt_pk_bf16_f32 v65, v66, v67\n"
    "v_mfma_f32_16x16x32_bf16 v[100:103], a[140:143], a[16:19], v[100:103]\n"
    "ds_read_b64_tr_b16 a[224:225], v12 offset:5632\n"
    "ds_read_b64_tr_b16 a[226:227], v12 offset:5888\n"
    "v_mfma_f32_16x16x32_bf16 v[100:103], a[144:147], a[20:23], v[100:103]\n"
    "v_cvt_pk_bf16_f32 v66, v68, v69\n"
    "v_cvt_pk_bf16_f32 v67, v70, v71\n"
    "v_mfma_f32_16x16x32_bf16 v[100:103], a[148:151], a[24:27], v[100:103]\n"
    "v_cvt_pk_bf16_f32 v68, v72, v73\n"
    "v_cvt_pk_bf16_f32 v69, v74, v75\n"
    "v_mfma_f32_16x16x32_bf16 v[100:103], a[152:155], a[28:31], v[100:103]\n"
    "v_cvt_pk_bf16_f32 v70, v76, v77\n"
    "v_cvt_pk_bf16_f32 v71, v78, v79\n"
    "v_mfma_f32_16x16x32_bf16 v[104:107], a[128:131], a[8:11], v[104:107]\n"
    "ds_read_b128 a[72:75], v18 offset:51408\n"
    "v_mfma_f32_16x16x32_bf16 v[104:107], a[132:135], a[12:15], v[104:107]\n"
    "ds_read_b128 a[76:79], v18 offset:51984\n"
    "v_mfma_f32_16x16x32_bf16 v[104:107], a[136:139], a[16:19], v[104:107]\n"
    "ds_read_b128 a[80:83], v18 offset:52560\n"
    "v_mfma_f32_16x16x32_bf16 v[104:107], a[140:143], a[20:23], v[104:107]\n"
    "v_mfma_f32_16x16x32_bf16 v[104:107], a[144:147], a[24:27], v[104:107]\n"
    "s_waitcnt lgkmcnt(3)\n"
    "v_mfma_f32_32x32x16_bf16 v[48:63], a[224:227], a[228:231], 0\n"
    "ds_write_b128 v15, v[64:67] offset:1152\n"
    "v_mfma_f32_16x16x32_bf16 v[104:107], a[148:151], a[28:31], v[104:107]\n"
    "v_mfma_f32_16x16x32_bf16 v[104:107], a[152:155], a[32:35], v[104:107]\n"
    "v_mfma_f32_16x16x32_bf16 v[108:111], a[128:131], a[12:15], v[108:111]\n"
    "buffer_load_dwordx4 a[184:187], v5, s[28:31], s50 offen offset:2048 sc1\n"
    "ds_read_b128 a[84:87], v18 offset:53136\n"
    "v_mfma_f32_16x16x32_bf16 v[108:111], a[132:135], a[16:19], v[108:111]\n"
    "ds_read_b64_tr_b16 a[220:221], v12 offset:6016\n"
    "ds_read_b64_tr_b16 a[222:223], v12 offset:6272\n"
    "v_mfma_f32_16x16x32_bf16 v[108:111], a[136:139], a[20:23], v[108:111]\n"
    "ds_read_b128 a[88:91], v18 offset:53712\n"
    "v_mfma_f32_16x16x32_bf16 v[108:111], a[140:143], a[24:27], v[108:111]\n"
    "v_mfma_f32_16x16x32_bf16 v[108:111], a[144:147], a[28:31], v[108:111]\n"
    "v_mfma_f32_16x16x32_bf16 v[108:111], a[148:151], a[32:35], v[108:111]\n"
    "ds_read_b128 a[92:95], v18 offset:54288\n"
    "v_mfma_f32_16x16x32_bf16 v[108:111], a[152:155], a[36:39], v[108:111]\n"
    "v_mfma_f32_16x16x32_bf16 v[112:115], a[128:131], a[16:19], v[112:115]\n"
    "buffer_load_dwordx4 a[188:191], v5, s[28:31], s50 offen offset:3072 sc1\n"
    "s_add_u32 s50, s50, 4096\n"
    "v_mfma_f32_16x16x32_bf16 v[112:115], a[132:135], a[20:23], v[112:115]\n"
    "ds_read_b128 a[96:99], v18 offset:54864\n"
    "v_mfma_f32_16x16x32_bf16 v[112:115], a[136:139], a[24:27], v[112:115]\n"
    "s_waitcnt lgkmcnt(3)\n"
    "v_mfma_f32_32x32x16_bf16 v[48:63], a[220:223], a[232:235], v[48:63]\n"
    "ds_write_b128 v15, v[68:71] offset:1728\n"
    "v_mfma_f32_16x16x32_bf16 v[112:115], a[140:143], a[28:31], v[112:115]\n"
    "v_mfma_f32_16x16x32_bf16 v[112:115], a[144:147], a[32:35], v[112:115]\n"
    "v_mfma_f32_16x16x32_bf16 v[112:115], a[148:151], a[36:39], v[112:115]\n"
    "v_cvt_pk_bf16_f32 v48, v48, v49\n"
    "v_cvt_pk_bf16_f32 v49, v50, v51\n"
    "v_mfma_f32_16x16x32_bf16 v[112:115], a[152:155], a[40:43], v[112:115]\n"
    "s_waitcnt vmcnt(7) lgkmcnt(3)\n"
    "ds_read_b64_tr_b16 a[224:225], v31\n"
    "ds_read_b64_tr_b16 a[226:227], v32\n"
    "v_mfma_f32_16x16x32_bf16 v[116:119], a[128:131], a[20:23], v[116:119]\n"
    "v_cvt_pk_bf16_f32 v50, v52, v53\n"
    "v_cvt_pk_bf16_f32 v51, v54, v55\n"
    "v_mfma_f32_16x16x32_bf16 v[116:119], a[132:135], a[24:27], v[116:119]\n"
    "v_cvt_pk_bf16_f32 v52, v56, v57\n"
    "v_cvt_pk_bf16_f32 v53, v58, v59\n"
    "v_mfma_f32_16x16x32_bf16 v[116:119], a[136:139], a[28:31], v[116:119]\n"
    "v_cvt_pk_bf16_f32 v54, v60, v61\n"
    "v_cvt_pk_bf16_f32 v55, v62, v63\n"
    "v_mfma_f32_16x16x32_bf16 v[116:119], a[140:143], a[32:35], v[116:119]\n"
    "buffer_load_dwordx4 a[192:195], v5, s[28:31], s50 offen offset:0 sc1\n"
    "ds_read_b128 a[100:103], v18 offset:55440\n"
    "v_mfma_f32_16x16x32_bf16 v[116:119], a[144:147], a[36:39], v[116:119]\n"
    "ds_read_b128 a[104:107], v18 offset:56016\n"
    "v_mfma_f32_16x16x32_bf16 v[116:119], a[148:151], a[40:43], v[116:119]\n"
    "ds_read_b128 a[108:111], v18 offset:56592\n"
    "v_mfma_f32_16x16x32_bf16 v[116:119], a[152:155], a[44:47], v[116:119]\n"
    "v_mfma_f32_16x16x32_bf16 v[120:123], a[128:131], a[24:27], v[120:123]\n"
    "s_waitcnt lgkmcnt(3)\n"
    "buffer_load_dwordx4 a[196:199], v5, s[28:31], s50 offen offset:1024 sc1\n"
    "v_mfma_f32_32x32x16_bf16 v[80:95], a[224:227], a[240:243], v[80:95]\n"
    "ds_write_b128 v15, v[48:51] offset:2304\n"
    "v_mfma_f32_16x16x32_bf16 v[120:123], a[132:135], a[28:31], v[120:123]\n"
    "v_mfma_f32_16x16x32_bf16 v[120:123], a[136:139], a[32:35], v[120:123]\n"
    "v_mfma_f32_16x16x32_bf16 v[120:123], a[140:143], a[36:39], v[120:123]\n"
    "v_cvt_pk_bf16_f32 v80, v80, v81\n"
    "v_cvt_pk_bf16_f32 v81, v82, v83\n"
    "v_mfma_f32_16x16x32_bf16 v[120:123], a[144:147], a[40:43], v[120:123]\n"
    "ds_read_b64_tr_b16 a[220:221], v12 offset:8448\n"
    "ds_read_b64_tr_b16 a[222:223], v12 offset:8704\n"
    "v_mfma_f32_16x16x32_bf16 v[120:123], a[148:151], a[44:47], v[120:123]\n"
    "v_cvt_pk_bf16_f32 v82, v84, v85\n"
    "v_cvt_pk_bf16_f32 v83, v86, v87\n"
    "v_mfma_f32_16x16x32_bf16 v[120:123], a[152:155], a[48:51], v[120:123]\n"
    "v_cvt_pk_bf16_f32 v84, v88, v89\n"
    "v_cvt_pk_bf16_f32 v85, v90, v91\n"
    "v_mfma_f32_16x16x32_bf16 v[124:127], a[128:131], a[28:31], v[124:127]\n"
    "v_cvt_pk_bf16_f32 v86, v92, v93\n"
    "v_cvt_pk_bf16_f32 v87, v94, v95\n"
    "v_mfma_f32_16x16x32_bf16 v[124:127], a[132:135], a[32:35], v[124:127]\n"
    "buffer_load_dwordx4 a[200:203], v5, s[28:31], s50 offen offset:2048 sc1\n"
    "ds_read_b128 a[112:115], v18 offset:57168\n"
    "v_mfma_f32_16x16x32_bf16 v[124:127], a[136:139], a[36:39], v[124:127]\n"
    "ds_read_b128 a[116:119], v18 offset:57744\n"
    "v_mfma_f32_16x16x32_bf16 v[124:127], a[140:143], a[40:43], v[124:127]\n"
    "ds_read_b128 a[120:123], v18 offset:58320\n"
    "v_mfma_f32_16x16x32_bf16 v[124:127], a[144:147], a[44:47], v[124:127]\n"
    "v_mfma_f32_16x16x32_bf16 v[124:127], a[148:151], a[48:51], v[124:127]\n"
    "s_waitcnt lgkmcnt(3)\n"
    "buffer_load_dwordx2 v[22:23], v27, s[28:31], s74 offen offset:128 sc1\n"
    "v_mfma_f32_32x32x16_bf16 v[64:79], a[220:223], a[228:231], 0\n"
    "ds_write_b128 v15, v[52:55] offset:2880\n"
    "v_mfma_f32_16x16x32_bf16 v[124:127], a[152:155], a[52:55], v[124:127]\n"
    "v_mfma_f32_16x16x32_bf16 v[128:131], a[128:131], a[32:35], v[128:131]\n"
    "buffer_load_dwordx4 a[204:207], v5, s[28:31], s50 offen offset:3072 sc1\n"
    "s_add_u32 s50, s50, 4096\n"
    "v_mfma_f32_16x16x32_bf16 v[128:131], a[132:135], a[36:39], v[128:131]\n"
    "v_mfma_f32_16x16x32_bf16 v[128:131], a[136:139], a[40:43], v[128:131]\n"
    "ds_read_b64_tr_b16 a[224:225], v12 offset:8832\n"
    "ds_read_b64_tr_b16 a[226:227], v12 offset:9088\n"
    "v_mfma_f32_16x16x32_bf16 v[128:131], a[140:143], a[44:47], v[128:131]\n"
    "v_mfma_f32_16x16x32_bf16 v[128:131], a[144:147], a[48:51], v[128:131]\n"
    "v_mfma_f32_16x16x32_bf16 v[128:131], a[148:151], a[52:55], v[128:131]\n"
    "v_mfma_f32_16x16x32_bf16 v[128:131], a[152:155], a[56:59], v[128:131]\n"
    "s_waitcnt vmcnt(7) lgkmcnt(3)\n"
    "v_mfma_f32_16x16x32_bf16 v[136:139], a[156:159], a[64:67], v[136:139]\n"
    "buffer_load_dwordx4 a[208:211], v5, s[28:31], s50 offen offset:0 sc1\n"
    "v_mfma_f32_16x16x32_bf16 v[136:139], a[160:163], a[68:71], v[136:139]\n"
    "ds_read_b128 a[64:67], v19 offset:50176\n"
    "v_mfma_f32_16x16x32_bf16 v[136:139], a[164:167], a[72:75], v[136:139]\n"
    "ds_read_b128 a[0:3], v18 offset:50336\n"
    "v_mfma_f32_16x16x32_bf16 v[136:139], a[168:171], a[76:79], v[136:139]\n"
    "s_waitcnt lgkmcnt(2)\n"
    "v_mfma_f32_32x32x16_bf16 v[64:79], a[224:227], a[232:235], v[64:79]\n"
    "ds_write_b128 v16, v[80:83] offset:0\n"
    "v_mfma_f32_16x16x32_bf16 v[136:139], a[172:175], a[80:83], v[136:139]\n"
    "v_mfma_f32_16x16x32_bf16 v[136:139], a[176:179], a[84:87], v[136:139]\n"
    "v_mfma_f32_16x16x32_bf16 v[136:139], a[180:183], a[88:91], v[136:139]\n"
    "v_cvt_pk_bf16_f32 v64, v64, v65\n"
    "v_cvt_pk_bf16_f32 v65, v66, v67\n"
    "v_mfma_f32_16x16x32_bf16 v[140:143], a[156:159], a[68:71], v[140:143]\n"
    "ds_read_b64_tr_b16 a[220:221], v12 offset:11264\n"
    "ds_read_b64_tr_b16 a[222:223], v12 offset:11520\n"
    "v_mfma_f32_16x16x32_bf16 v[140:143], a[160:163], a[72:75], v[140:143]\n"
    "v_cvt_pk_bf16_f32 v66, v68, v69\n"
    "v_cvt_pk_bf16_f32 v67, v70, v71\n"
    "v_mfma_f32_16x16x32_bf16 v[140:143], a[164:167], a[76:79], v[140:143]\n"
    "v_cvt_pk_bf16_f32 v68, v72, v73\n"
    "v_cvt_pk_bf16_f32 v69, v74, v75\n"
    "v_mfma_f32_16x16x32_bf16 v[140:143], a[168:171], a[80:83], v[140:143]\n"
    "v_cvt_pk_bf16_f32 v70, v76, v77\n"
    "v_cvt_pk_bf16_f32 v71, v78, v79\n"
    "v_mfma_f32_16x16x32_bf16 v[140:143], a[172:175], a[84:87], v[140:143]\n"
    "buffer_load_dwordx4 a[212:215], v5, s[28:31], s50 offen offset:1024 sc1\n"
    "ds_read_b128 a[68:71], v19 offset:50752\n"
    "v_mfma_f32_16x16x32_bf16 v[140:143], a[176:179], a[88:91], v[140:143]\n"
    "ds_read_b128 a[4:7], v18 offset:50912\n"
    "v_mfma_f32_16x16x32_bf16 v[140:143], a[180:183], a[92:95], v[140:143]\n"
    "ds_read_b128 a[8:11], v18 offset:51488\n"
    "v_mfma_f32_16x16x32_bf16 v[144:147], a[156:159], a[72:75], v[144:147]\n"
    "buffer_load_dwordx4 a[216:219], v5, s[28:31], s50 offen offset:2048 sc1\n"
    "s_add_u32 s50, s50, s62\n"
    "v_mfma_f32_16x16x32_bf16 v[144:147], a[160:163], a[76:79], v[144:147]\n"
    "s_waitcnt lgkmcnt(3)\n"
    "s_movk_i32 s62, 3072\n"
    "v_mfma_f32_32x32x16_bf16 v[48:63], a[220:223], a[228:231], 0\n"
    "ds_write_b128 v15, v[64:67] offset:3456\n"
    "v_mfma_f32_16x16x32_bf16 v[144:147], a[164:167], a[80:83], v[144:147]\n"
    "v_mfma_f32_16x16x32_bf16 v[144:147], a[168:171], a[84:87], v[144:147]\n"
    "v_mfma_f32_16x16x32_bf16 v[144:147], a[172:175], a[88:91], v[144:147]\n"
    "ds_read_b128 a[72:75], v19 offset:51328\n"
    "v_mfma_f32_16x16x32_bf16 v[144:147], a[176:179], a[92:95], v[144:147]\n"
    "ds_read_b64_tr_b16 a[224:225], v12 offset:11648\n"
    "ds_read_b64_tr_b16 a[226:227], v12 offset:11904\n"
    "v_mfma_f32_16x16x32_bf16 v[144:147], a[180:183], a[96:99], v[144:147]\n"
    "ds_read_b128 a[12:15], v18 offset:52064\n"
    "v_mfma_f32_16x16x32_bf16 v[148:151], a[156:159], a[76:79], v[148:151]\n"
    "ds_read_b128 a[16:19], v18 offset:52640\n"
    "v_mfma_f32_16x16x32_bf16 v[148:151], a[160:163], a[80:83], v[148:151]\n"
    "ds_read_b128 a[76:79], v19 offset:51904\n"
    "v_mfma_f32_16x16x32_bf16 v[148:151], a[164:167], a[84:87], v[148:151]\n"
    "s_waitcnt vmcnt(4) lgkmcnt(7)\n"
    "v_min_u32 v22, v22, v23\n"
    "ds_read_b128 a[20:23], v18 offset:53216\n"
    "v_mfma_f32_16x16x32_bf16 v[148:151], a[168:171], a[88:91], v[148:151]\n"
    "v_cmp_eq_u32 vcc, 0, v22\n"
    "s_cbranch_vccnz 152f\n"
    "162:\n"
    "v_mfma_f32_16x16x32_bf16 v[148:151], a[172:175], a[92:95], v[148:151]\n"
    "ds_read_b128 a[24:27], v18 offset:53792\n"
    "v_mfma_f32_16x16x32_bf16 v[148:151], a[176:179], a[96:99], v[148:151]\n"
    "ds_read_b128 a[28:31], v18 offset:54368\n"
    "v_mfma_f32_16x16x32_bf16 v[148:151], a[180:183], a[100:103], v[148:151]\n"
    "s_waitcnt lgkmcnt(6)\n"
    "buffer_load_dwordx4 a[128:131], v5, s[28:31], s50 offen offset:0 sc1\n"
    "v_mfma_f32_32x32x16_bf16 v[48:63], a[224:227], a[232:235], v[48:63]\n"
    "ds_write_b128 v15, v[68:71] offset:4032\n"
    "v_mfma_f32_16x16x32_bf16 v[152:155], a[156:159], a[80:83], v[152:155]\n"
    "v_mfma_f32_16x16x32_bf16 v[152:155], a[160:163], a[84:87], v[152:155]\n"
    "v_mfma_f32_16x16x32_bf16 v[152:155], a[164:167], a[88:91], v[152:155]\n"
    "v_cvt_pk_bf16_f32 v48, v48, v49\n"
    "v_cvt_pk_bf16_f32 v49, v50, v51\n"
    "v_mfma_f32_16x16x32_bf16 v[152:155], a[168:171], a[92:95], v[152:155]\n"
    "ds_read_b64_tr_b16 a[220:221], v37 offset:14080\n"
    "ds_read_b64_tr_b16 a[222:223], v37 offset:14336\n"
    "v_mfma_f32_16x16x32_bf16 v[152:155], a[172:175], a[96:99], v[152:155]\n"
    "v_cvt_pk_bf16_f32 v50, v52, v53\n"
    "v_cvt_pk_bf16_f32 v51, v54, v55\n"
    "v_mfma_f32_16x16x32_bf16 v[152:155], a[176:179], a[100:103], v[152:155]\n"
    "v_cvt_pk_bf16_f32 v52, v56, v57\n"
    "v_cvt_pk_bf16_f32 v53, v58, v59\n"
    "v_mfma_f32_16x16x32_bf16 v[152:155], a[180:183], a[104:107], v[152:155]\n"
    "v_cvt_pk_bf16_f32 v54, v60, v61\n"
    "v_cvt_pk_bf16_f32 v55, v62, v63\n"
    "v_mfma_f32_16x16x32_bf16 v[156:159], a[156:159], a[84:87], v[156:159]\n"
    "buffer_load_dwordx4 a[132:135], v5, s[28:31], s50 offen offset:1024 sc1\n"
    "ds_read_b128 a[80:83], v19 offset:52480\n"
    "v_mfma_f32_16x16x32_bf16 v[156:159], a[160:163], a[88:91], v[156:159]\n"
    "ds_read_b128 a[32:35], v18 offset:54944\n"
    "v_mfma_f32_16x16x32_bf16 v[156:159], a[164:167], a[92:95], v[156:159]\n"
    "ds_read_b128 a[84:87], v19 offset:53056\n"
    "v_mfma_f32_16x16x32_bf16 v[156:159], a[168:171], a[96:99], v[156:159]\n"
    "ds_read_b128 a[36:39], v18 offset:55520\n"
    "v_mfma_f32_16x16x32_bf16 v[156:159], a[172:175], a[100:103], v[156:159]\n"
    "s_waitcnt lgkmcnt(4)\n"
    "v_mfma_f32_32x32x16_bf16 v[64:79], a[220:223], a[228:231], 0\n"
    "ds_write_b128 v15, v[48:51] offset:4608\n"
    "v_mfma_f32_16x16x32_bf16 v[156:159], a[176:179], a[104:107], v[156:159]\n"
    "v_mfma_f32_16x16x32_bf16 v[156:159], a[180:183], a[108:111], v[156:159]\n"
    "buffer_load_dwordx4 a[136:139], v5, s[28:31], s50 offen offset:2048 sc1\n"
    "v_mfma_f32_16x16x32_bf16 v[160:163], a[156:159], a[88:91], v[160:163]\n"
    "ds_read_b128 a[40:43], v18 offset:56096\n"
    "v_mfma_f32_16x16x32_bf16 v[160:163], a[160:163], a[92:95], v[160:163]\n"
    "ds_read_b64_tr_b16 a[224:225], v37 offset:14464\n"
    "ds_read_b64_tr_b16 a[226:227], v37 offset:14720\n"
    "v_mfma_f32_16x16x32_bf16 v[160:163], a[164:167], a[96:99], v[160:163]\n"
    "v_xor_b32 v12, 32, v12\n"
    "v_xor_b32 v13, 32, v13\n"
    "v_mfma_f32_16x16x32_bf16 v[160:163], a[168:171], a[100:103], v[160:163]\n"
    "v_xor_b32 v14, 32, v14\n"
    "v_xor_b32 v31, 32, v31\n"
    "v_mfma_f32_16x16x32_bf16 v[160:163], a[172:175], a[104:107], v[160:163]\n"
    "v_xor_b32 v32, 32, v32\n"
    "v_xor_b32 v37, 32, v37\n"
    "v_mfma_f32_16x16x32_bf16 v[160:163], a[176:179], a[108:111], v[160:163]\n"
    "ds_read_b128 a[88:91], v19 offset:53632\n"
    "v_mfma_f32_16x16x32_bf16 v[160:163], a[180:183], a[112:115], v[160:163]\n"
    "buffer_load_dwordx4 a[140:143], v5, s[28:31], s50 offen offset:3072 sc1\n"
    "s_add_u32 s50, s50, 4096\n"
    "v_mfma_f32_16x16x32_bf16 v[164:167], a[156:159], a[92:95], v[164:167]\n"
    "ds_read_b128 a[44:47], v18 offset:56672\n"
    "v_mfma_f32_16x16x32_bf16 v[164:167], a[160:163], a[96:99], v[164:167]\n"
    "ds_read_b128 a[92:95], v19 offset:54208\n"
    "v_mfma_f32_16x16x32_bf16 v[164:167], a[164:167], a[100:103], v[164:167]\n"
    "s_waitcnt lgkmcnt(3)\n"
    "v_mfma_f32_32x32x16_bf16 v[64:79], a[224:227], a[232:235], v[64:79]\n"
    "ds_write_b128 v15, v[52:55] offset:5184\n"
    "v_mfma_f32_16x16x32_bf16 v[164:167], a[168:171], a[104:107], v[164:167]\n"
    "v_mfma_f32_16x16x32_bf16 v[164:167], a[172:175], a[108:111], v[164:167]\n"
    "v_mfma_f32_16x16x32_bf16 v[164:167], a[176:179], a[112:115], v[164:167]\n"
    "v_cvt_pk_bf16_f32 v64, v64, v65\n"
    "v_cvt_pk_bf16_f32 v65, v66, v67\n"
    "v_mfma_f32_16x16x32_bf16 v[164:167], a[180:183], a[116:119], v[164:167]\n"
    "v_cvt_pk_bf16_f32 v66, v68, v69\n"
    "v_cvt_pk_bf16_f32 v67, v70, v71\n"
    "v_mfma_f32_16x16x32_bf16 v[168:171], a[156:159], a[96:99], v[168:171]\n"
    "v_cvt_pk_bf16_f32 v68, v72, v73\n"
    "v_cvt_pk_bf16_f32 v69, v74, v75\n"
    "v_mfma_f32_16x16x32_bf16 v[168:171], a[160:163], a[100:103], v[168:171]\n"
    "v_cvt_pk_bf16_f32 v70, v76, v77\n"
    "v_cvt_pk_bf16_f32 v71, v78, v79\n"
    "v_mfma_f32_16x16x32_bf16 v[168:171], a[164:167], a[104:107], v[168:171]\n"
    "ds_read_b64_tr_b16 a[220:221], v12 offset:0\n"
    "ds_read_b64_tr_b16 a[222:223], v12 offset:256\n"
    "v_mfma_f32_16x16x32_bf16 v[168:171], a[168:171], a[108:111], v[168:171]\n"
    "buffer_load_dwordx4 a[144:147], v5, s[28:31], s50 offen offset:0 sc1\n"
    "ds_read_b128 a[96:99], v19 offset:54784\n"
    "v_mfma_f32_16x16x32_bf16 v[168:171], a[172:175], a[112:115], v[168:171]\n"
    "ds_read_b128 a[48:51], v18 offset:57248\n"
    "v_mfma_f32_16x16x32_bf16 v[168:171], a[176:179], a[116:119], v[168:171]\n"
    "ds_read_b128 a[100:103], v19 offset:55360\n"
    "v_mfma_f32_16x16x32_bf16 v[168:171], a[180:183], a[120:123], v[168:171]\n"
    "s_waitcnt vmcnt(7) lgkmcnt(5)\n"
    "buffer_load_dwordx4 a[148:151], v5, s[28:31], s50 offen offset:1024 sc1\n"
    "ds_read_b128 a[52:55], v18 offset:57824\n"
    "v_mfma_f32_16x16x32_bf16 v[176:179], a[184:187], a[0:3], v[176:179]\n"
    "s_waitcnt lgkmcnt(4)\n"
    "v_mfma_f32_32x32x16_bf16 v[48:63], a[220:223], a[228:231], 0\n"
    "ds_write_b128 v16, v[84:87] offset:576\n"
    "v_mfma_f32_16x16x32_bf16 v[176:179], a[188:191], a[4:7], v[176:179]\n"
    "v_mfma_f32_16x16x32_bf16 v[176:179], a[192:195], a[8:11], v[176:179]\n"
    "v_mfma_f32_16x16x32_bf16 v[176:179], a[196:199], a[12:15], v[176:179]\n"
    "ds_read_b128 a[104:107], v19 offset:55936\n"
    "v_mfma_f32_16x16x32_bf16 v[176:179], a[200:203], a[16:19], v[176:179]\n"
    "ds_read_b64_tr_b16 a[224:225], v12 offset:384\n"
    "ds_read_b64_tr_b16 a[226:227], v12 offset:640\n"
    "v_mfma_f32_16x16x32_bf16 v[176:179], a[204:207], a[20:23], v[176:179]\n"
    "ds_read_b128 a[56:59], v18 offset:58400\n"
    "v_mfma_f32_16x16x32_bf16 v[176:179], a[208:211], a[24:27], v[176:179]\n"
    "buffer_load_dwordx4 a[152:155], v5, s[28:31], s50 offen offset:2048 sc1\n"
    "v_mfma_f32_16x16x32_bf16 v[180:183], a[184:187], a[4:7], v[180:183]\n"
    "v_mfma_f32_16x16x32_bf16 v[180:183], a[188:191], a[8:11], v[180:183]\n"
    "v_mfma_f32_16x16x32_bf16 v[180:183], a[192:195], a[12:15], v[180:183]\n"
    "v_mfma_f32_16x16x32_bf16 v[180:183], a[196:199], a[16:19], v[180:183]\n"
    "v_mfma_f32_16x16x32_bf16 v[180:183], a[200:203], a[20:23], v[180:183]\n"
    "v_mfma_f32_16x16x32_bf16 v[180:183], a[204:207], a[24:27], v[180:183]\n"
    "s_waitcnt lgkmcnt(1)\n"
    "v_mfma_f32_32x32x16_bf16 v[48:63], a[224:227], a[232:235], v[48:63]\n"
    "ds_write_b128 v33, v[64:67] offset:5760\n"
    "v_mfma_f32_16x16x32_bf16 v[180:183], a[208:211], a[28:31], v[180:183]\n"
    "buffer_load_dwordx4 a[156:159], v5, s[28:31], s50 offen offset:3072 sc1\n"
    "s_add_u32 s50, s50, 4096\n"
    "v_mfma_f32_16x16x32_bf16 v[184:187], a[184:187], a[8:11], v[184:187]\n"
    "v_mfma_f32_16x16x32_bf16 v[184:187], a[188:191], a[12:15], v[184:187]\n"
    "v_cvt_pk_bf16_f32 v48, v48, v49\n"
    "v_cvt_pk_bf16_f32 v49, v50, v51\n"
    "v_mfma_f32_16x16x32_bf16 v[184:187], a[192:195], a[16:19], v[184:187]\n"
    "v_cvt_pk_bf16_f32 v50, v52, v53\n"
    "v_cvt_pk_bf16_f32 v51, v54, v55\n"
    "v_mfma_f32_16x16x32_bf16 v[184:187], a[196:199], a[20:23], v[184:187]\n"
    "v_cvt_pk_bf16_f32 v52, v56, v57\n"
    "v_cvt_pk_bf16_f32 v53, v58, v59\n"
    "v_mfma_f32_16x16x32_bf16 v[184:187], a[200:203], a[24:27], v[184:187]\n"
    "v_cvt_pk_bf16_f32 v54, v60, v61\n"
    "v_cvt_pk_bf16_f32 v55, v62, v63\n"
    "v_mfma_f32_16x16x32_bf16 v[184:187], a[204:207], a[28:31], v[184:187]\n"
    "ds_read_b64_tr_b16 a[220:221], v13\n"
    "ds_read_b64_tr_b16 a[222:223], v14\n"
    "v_mfma_f32_16x16x32_bf16 v[184:187], a[208:211], a[32:35], v[184:187]\n"
    "buffer_load_dwordx4 a[160:163], v5, s[28:31], s50 offen offset:0 sc1\n"
    "v_mfma_f32_16x16x32_bf16 v[188:191], a[184:187], a[12:15], v[188:191]\n"
    "v_mfma_f32_16x16x32_bf16 v[188:191], a[188:191], a[16:19], v[188:191]\n"
    "v_mfma_f32_16x16x32_bf16 v[188:191], a[192:195], a[20:23], v[188:191]\n"
    "v_mfma_f32_16x16x32_bf16 v[188:191], a[196:199], a[24:27], v[188:191]\n"
    "s_waitcnt lgkmcnt(0)\n"
    "v_mfma_f32_32x32x16_bf16 v[80:95], a[220:223], a[236:239], 0\n"
    "ds_write_b128 v33, v[68:71] offset:6336\n"
    "v_mfma_f32_16x16x32_bf16 v[188:191], a[200:203], a[28:31], v[188:191]\n"
    "v_mfma_f32_16x16x32_bf16 v[188:191], a[204:207], a[32:35], v[188:191]\n"
    "v_mfma_f32_16x16x32_bf16 v[188:191], a[208:211], a[36:39], v[188:191]\n"
    "buffer_load_dwordx4 a[164:167], v5, s[28:31], s50 offen offset:1024 sc1\n"
    "v_mfma_f32_16x16x32_bf16 v[192:195], a[184:187], a[16:19], v[192:195]\n"
    "ds_read_b64_tr_b16 a[224:225], v12 offset:2816\n"
    "ds_read_b64_tr_b16 a[226:227], v12 offset:3072\n"
    "v_mfma_f32_16x16x32_bf16 v[192:195], a[188:191], a[20:23], v[192:195]\n"
    "v_mfma_f32_16x16x32_bf16 v[192:195], a[192:195], a[24:27], v[192:195]\n"
    "v_mfma_f32_16x16x32_bf16 v[192:195], a[196:199], a[28:31], v[192:195]\n"
    "v_mfma_f32_16x16x32_bf16 v[192:195], a[200:203], a[32:35], v[192:195]\n"
    "v_mfma_f32_16x16x32_bf16 v[192:195], a[204:207], a[36:39], v[192:195]\n"
    "v_mfma_f32_16x16x32_bf16 v[192:195], a[208:211], a[40:43], v[192:195]\n"
    "buffer_load_dwordx4 a[168:171], v5, s[28:31], s50 offen offset:2048 sc1\n"
    "v_mfma_f32_16x16x32_bf16 v[196:199], a[184:187], a[20:23], v[196:199]\n"
    "v_mfma_f32_16x16x32_bf16 v[196:199], a[188:191], a[24:27], v[196:199]\n"
    "s_waitcnt lgkmcnt(0)\n"
    "v_mfma_f32_32x32x16_bf16 v[64:79], a[224:227], a[228:231], 0\n"
    "s_waitcnt lgkmcnt(0)\n"
    "s_barrier\n"
    "v_mfma_f32_16x16x32_bf16 v[196:199], a[192:195], a[28:31], v[196:199]\n"
    "ds_write_b128 v15, v[48:51] offset:50176\n"
    "v_mfma_f32_16x16x32_bf16 v[196:199], a[196:199], a[32:35], v[196:199]\n"
    "v_mfma_f32_16x16x32_bf16 v[196:199], a[200:203], a[36:39], v[196:199]\n"
    "v_mfma_f32_16x16x32_bf16 v[196:199], a[204:207], a[40:43], v[196:199]\n"
    "ds_read_b128 a[0:3], v18 offset:0\n"
    "v_mfma_f32_16x16x32_bf16 v[196:199], a[208:211], a[44:47], v[196:199]\n"
    "buffer_load_dwordx4 a[172:175], v5, s[28:31], s50 offen offset:3072 sc1\n"
    "s_add_u32 s50, s50, 4096\n"
    "v_mfma_f32_16x16x32_bf16 v[200:203], a[184:187], a[24:27], v[200:203]\n"
    "ds_read_b128 a[4:7], v18 offset:576\n"
    "v_mfma_f32_16x16x32_bf16 v[200:203], a[188:191], a[28:31], v[200:203]\n"
    "v_mfma_f32_16x16x32_bf16 v[200:203], a[192:195], a[32:35], v[200:203]\n"
    "ds_read_b128 a[8:11], v18 offset:1152\n"
    "v_mfma_f32_16x16x32_bf16 v[200:203], a[196:199], a[36:39], v[200:203]\n"
    "v_mfma_f32_16x16x32_bf16 v[200:203], a[200:203], a[40:43], v[200:203]\n"
    "ds_read_b128 a[12:15], v18 offset:1728\n"
    "v_mfma_f32_16x16x32_bf16 v[200:203], a[204:207], a[44:47], v[200:203]\n"
    "v_mfma_f32_16x16x32_bf16 v[200:203], a[208:211], a[48:51], v[200:203]\n"
    "buffer_load_dwordx4 a[176:179], v5, s[28:31], s50 offen offset:0 sc1\n"
    "ds_read_b128 a[16:19], v18 offset:2304\n"
    "v_mfma_f32_16x16x32_bf16 v[204:207], a[184:187], a[28:31], v[204:207]\n"
    "v_mfma_f32_16x16x32_bf16 v[204:207], a[188:191], a[32:35], v[204:207]\n"
    "ds_read_b128 a[20:23], v18 offset:2880\n"
    "v_mfma_f32_16x16x32_bf16 v[204:207], a[192:195], a[36:39], v[204:207]\n"
    "v_mfma_f32_16x16x32_bf16 v[204:207], a[196:199], a[40:43], v[204:207]\n"
    "ds_read_b128 a[24:27], v18 offset:3456\n"
    "v_mfma_f32_16x16x32_bf16 v[204:207], a[200:203], a[44:47], v[204:207]\n"
    "v_mfma_f32_16x16x32_bf16 v[204:207], a[204:207], a[48:51], v[204:207]\n"
    "ds_read_b128 a[28:31], v18 offset:4032\n"
    "v_mfma_f32_16x16x32_bf16 v[204:207], a[208:211], a[52:55], v[204:207]\n"
    "buffer_load_dwordx4 a[180:183], v5, s[28:31], s50 offen offset:1024 sc1\n"
    "v_mfma_f32_16x16x32_bf16 v[208:211], a[184:187], a[32:35], v[208:211]\n"
    "v_mfma_f32_16x16x32_bf16 v[208:211], a[188:191], a[36:39], v[208:211]\n"
    "ds_read_b128 a[32:35], v18 offset:4608\n"
    "v_mfma_f32_16x16x32_bf16 v[208:211], a[192:195], a[40:43], v[208:211]\n"
    "v_mfma_f32_16x16x32_bf16 v[208:211], a[196:199], a[44:47], v[208:211]\n"
    "ds_read_b128 a[36:39], v18 offset:5184\n"
    "v_mfma_f32_16x16x32_bf16 v[208:211], a[200:203], a[48:51], v[208:211]\n"
    "v_mfma_f32_16x16x32_bf16 v[208:211], a[204:207], a[52:55], v[208:211]\n"
    "ds_read_b128 a[40:43], v18 offset:5760\n"
    "v_mfma_f32_16x16x32_bf16 v[208:211], a[208:211], a[56:59], v[208:211]\n"
    "s_waitcnt vmcnt(14) lgkmcnt(4)\n"
    "buffer_load_dwordx4 a[184:187], v5, s[28:31], s50 offen offset:2048 sc1\n"
    "v_mfma_f32_16x16x32_bf16 v[216:219], a[212:215], a[64:67], v[216:219]\n"
    "ds_read_b128 a[44:47], v18 offset:6336\n"
    "v_mfma_f32_16x16x32_bf16 v[216:219], a[216:219], a[72:75], v[216:219]\n"
    "v_mfma_f32_16x16x32_bf16 v[220:223], a[212:215], a[68:71], v[220:223]\n"
    "ds_read_b128 a[48:51], v18 offset:6912\n"
    "v_mfma_f32_16x16x32_bf16 v[220:223], a[216:219], a[76:79], v[220:223]\n"
    "v_mfma_f32_16x16x32_bf16 v[224:227], a[212:215], a[72:75], v[224:227]\n"
    "ds_read_b128 a[52:55], v18 offset:7488\n"
    "v_mfma_f32_16x16x32_bf16 v[224:227], a[216:219], a[80:83], v[224:227]\n"
    "v_mfma_f32_16x16x32_bf16 v[228:231], a[212:215], a[76:79], v[228:231]\n"
    "buffer_load_dwordx4 a[188:191], v5, s[28:31], s50 offen offset:3072 sc1\n"
    "s_add_u32 s50, s50, 4096\n"
    "v_mfma_f32_16x16x32_bf16 v[228:231], a[216:219], a[84:87], v[228:231]\n"
    "ds_read_b128 a[56:59], v18 offset:8064\n"
    "v_mfma_f32_16x16x32_bf16 v[232:235], a[212:215], a[80:83], v[232:235]\n"
    "v_mfma_f32_16x16x32_bf16 v[232:235], a[216:219], a[88:91], v[232:235]\n"
    "v_mfma_f32_16x16x32_bf16 v[236:239], a[212:215], a[84:87], v[236:239]\n"
    "v_mfma_f32_16x16x32_bf16 v[236:239], a[216:219], a[92:95], v[236:239]\n"
    "v_mfma_f32_16x16x32_bf16 v[240:243], a[212:215], a[88:91], v[240:243]\n"
    "v_mfma_f32_16x16x32_bf16 v[240:243], a[216:219], a[96:99], v[240:243]\n"
    "v_mfma_f32_16x16x32_bf16 v[244:247], a[212:215], a[92:95], v[244:247]\n"
    "v_mfma_f32_16x16x32_bf16 v[244:247], a[216:219], a[100:103], v[244:247]\n"
    "v_mfma_f32_16x16x32_bf16 v[248:251], a[212:215], a[96:99], v[248:251]\n"
    "v_mfma_f32_16x16x32_bf16 v[248:251], a[216:219], a[104:107], v[248:251]\n"
    "s_waitcnt vmcnt(9) lgkmcnt(0)\n"
    "s_cmp_eq_u32 s52, 1\n"
    "s_cbranch_scc1 12f\n"
    "v_mfma_f32_16x16x32_bf16 v[96:99], a[128:131], a[0:3], v[96:99]\n"
    "s_add_u32 s58, s51, 0\n"
    "ds_read_b64_tr_b16 a[220:221], v12 offset:3200\n"
    "ds_read_b64_tr_b16 a[222:223], v12 offset:3456\n"
    "v_mfma_f32_16x16x32_bf16 v[96:99], a[132:135], a[4:7], v[96:99]\n"
    "s_add_u32 m0, s48, 0\n"
    "buffer_load_dwordx4 a[192:195], v5, s[28:31], s50 offen offset:0 sc1\n"
    "ds_read_b128 a[64:67], v18 offset:80\n"
    "v_mfma_f32_16x16x32_bf16 v[96:99], a[136:139], a[8:11], v[96:99]\n"
    "v_add_u32 v10, s58, v6\n"
    "v_mfma_f32_16x16x32_bf16 v[96:99], a[140:143], a[12:15], v[96:99]\n"
    "buffer_load_dwordx4 v10, s[16:19], 0 offen lds\n"
    "v_mfma_f32_16x16x32_bf16 v[96:99], a[144:147], a[16:19], v[96:99]\n"
    "ds_read_b128 a[68:71], v18 offset:656\n"
    "v_mfma_f32_16x16x32_bf16 v[96:99], a[148:151], a[20:23], v[96:99]\n"
    "v_mfma_f32_16x16x32_bf16 v[96:99], a[152:155], a[24:27], v[96:99]\n"
    "s_waitcnt lgkmcnt(2)\n"
    "v_mfma_f32_32x32x16_bf16 v[64:79], a[220:223], a[232:235], v[64:79]\n"
    "ds_write_b128 v15, v[52:55] offset:50752\n"
    "v_mfma_f32_16x16x32_bf16 v[100:103], a[128:131], a[4:7], v[100:103]\n"
    "v_mfma_f32_16x16x32_bf16 v[100:103], a[132:135], a[8:11], v[100:103]\n"
    "buffer_load_dwordx4 a[196:199], v5, s[28:31], s50 offen offset:1024 sc1\n"
    "v_mfma_f32_16x16x32_bf16 v[100:103], a[136:139], a[12:15], v[100:103]\n"
    "s_add_u32 m0, s48, 1024\n"
    "v_cvt_pk_bf16_f32 v64, v64, v65\n"
    "v_cvt_pk_bf16_f32 v65, v66, v67\n"
    "v_mfma_f32_16x16x32_bf16 v[100:103], a[140:143], a[16:19], v[100:103]\n"
    "ds_read_b64_tr_b16 a[224:225], v12 offset:5632\n"
    "ds_read_b64_tr_b16 a[226:227], v12 offset:5888\n"
    "v_mfma_f32_16x16x32_bf16 v[100:103], a[144:147], a[20:23], v[100:103]\n"
    "v_cvt_pk_bf16_f32 v66, v68, v69\n"
    "v_cvt_pk_bf16_f32 v67, v70, v71\n"
    "v_mfma_f32_16x16x32_bf16 v[100:103], a[148:151], a[24:27], v[100:103]\n"
    "v_cvt_pk_bf16_f32 v68, v72, v73\n"
    "v_cvt_pk_bf16_f32 v69, v74, v75\n"
    "v_mfma_f32_16x16x32_bf16 v[100:103], a[152:155], a[28:31], v[100:103]\n"
    "v_cvt_pk_bf16_f32 v70, v76, v77\n"
    "v_cvt_pk_bf16_f32 v71, v78, v79\n"
    "v_mfma_f32_16x16x32_bf16 v[104:107], a[128:131], a[8:11], v[104:107]\n"
    "ds_read_b128 a[72:75], v18 offset:1232\n"
    "v_add_u32 v10, s58, v7\n"
    "v_mfma_f32_16x16x32_bf16 v[104:107], a[132:135], a[12:15], v[104:107]\n"
    "ds_read_b128 a[76:79], v18 offset:1808\n"
    "buffer_load_dwordx4 v10, s[16:19], 0 offen lds\n"
    "v_mfma_f32_16x16x32_bf16 v[104:107], a[136:139], a[16:19], v[104:107]\n"
    "buffer_load_dwordx4 a[200:203], v5, s[28:31], s50 offen offset:2048 sc1\n"
    "ds_read_b128 a[80:83], v18 offset:2384\n"
    "v_mfma_f32_16x16x32_bf16 v[104:107], a[140:143], a[20:23], v[104:107]\n"
    "v_mfma_f32_16x16x32_bf16 v[104:107], a[144:147], a[24:27], v[104:107]\n"
    "s_waitcnt lgkmcnt(3)\n"
    "v_mfma_f32_32x32x16_bf16 v[48:63], a[224:227], a[228:231], 0\n"
    "s_add_u32 m0, s48, 2048\n"
    "ds_write_b128 v15, v[64:67] offset:51328\n"
    "v_add_u32 v10, s58, v8\n"
    "v_mfma_f32_16x16x32_bf16 v[104:107], a[148:151], a[28:31], v[104:107]\n"
    "s_mov_b32 exec_hi, 0xffff\n"
    "buffer_load_dwordx4 v10, s[16:19], 0 offen lds\n"
    "s_mov_b32 exec_hi, -1\n"
    "v_mfma_f32_16x16x32_bf16 v[104:107], a[152:155], a[32:35], v[104:107]\n"
    "v_mfma_f32_16x16x32_bf16 v[108:111], a[128:131], a[12:15], v[108:111]\n"
    "ds_read_b128 a[84:87], v18 offset:2960\n"
    "v_mfma_f32_16x16x32_bf16 v[108:111], a[132:135], a[16:19], v[108:111]\n"
    "ds_read_b64_tr_b16 a[220:221], v12 offset:6016\n"
    "ds_read_b64_tr_b16 a[222:223], v12 offset:6272\n"
    "v_mfma_f32_16x16x32_bf16 v[108:111], a[136:139], a[20:23], v[108:111]\n"
    "ds_read_b128 a[88:91], v18 offset:3536\n"
    "v_mfma_f32_16x16x32_bf16 v[108:111], a[140:143], a[24:27], v[108:111]\n"
    "buffer_load_dwordx4 a[204:207], v5, s[28:31], s50 offen offset:3072 sc1\n"
    "s_add_u32 s50, s50, 4096\n"
    "v_mfma_f32_16x16x32_bf16 v[108:111], a[144:147], a[28:31], v[108:111]\n"
    "v_mfma_f32_16x16x32_bf16 v[108:111], a[148:151], a[32:35], v[108:111]\n"
    "ds_read_b128 a[92:95], v18 offset:4112\n"
    "v_mfma_f32_16x16x32_bf16 v[108:111], a[152:155], a[36:39], v[108:111]\n"
    "v_mfma_f32_16x16x32_bf16 v[112:115], a[128:131], a[16:19], v[112:115]\n"
    "s_add_u32 s58, s51, 172032\n"
    "s_add_u32 m0, s48, 2816\n"
    "v_add_u32 v10, s58, v6\n"
    "v_mfma_f32_16x16x32_bf16 v[112:115], a[132:135], a[20:23], v[112:115]\n"
    "ds_read_b128 a[96:99], v18 offset:4688\n"
    "buffer_load_dwordx4 v10, s[16:19], 0 offen lds\n"
    "v_mfma_f32_16x16x32_bf16 v[112:115], a[136:139], a[24:27], v[112:115]\n"
    "s_waitcnt lgkmcnt(3)\n"
    "v_mfma_f32_32x32x16_bf16 v[48:63], a[220:223], a[232:235], v[48:63]\n"
    "ds_write_b128 v15, v[68:71] offset:51904\n"
    "v_mfma_f32_16x16x32_bf16 v[112:115], a[140:143], a[28:31], v[112:115]\n"
    "v_mfma_f32_16x16x32_bf16 v[112:115], a[144:147], a[32:35], v[112:115]\n"
    "buffer_load_dwordx4 a[208:211], v5, s[28:31], s50 offen offset:0 sc1\n"
    "v_mfma_f32_16x16x32_bf16 v[112:115], a[148:151], a[36:39], v[112:115]\n"
    "v_cvt_pk_bf16_f32 v48, v48, v49\n"
    "v_cvt_pk_bf16_f32 v49, v50, v51\n"
    "v_mfma_f32_16x16x32_bf16 v[112:115], a[152:155], a[40:43], v[112:115]\n"
    "ds_read_b64_tr_b16 a[224:225], v31\n"
    "ds_read_b64_tr_b16 a[226:227], v32\n"
    "v_mfma_f32_16x16x32_bf16 v[116:119], a[128:131], a[20:23], v[116:119]\n"
    "v_cvt_pk_bf16_f32 v50, v52, v53\n"
    "v_cvt_pk_bf16_f32 v51, v54, v55\n"
    "v_mfma_f32_16x16x32_bf16 v[116:119], a[132:135], a[24:27], v[116:119]\n"
    "v_cvt_pk_bf16_f32 v52, v56, v57\n"
    "v_cvt_pk_bf16_f32 v53, v58, v59\n"
    "v_mfma_f32_16x16x32_bf16 v[116:119], a[136:139], a[28:31], v[116:119]\n"
    "s_add_u32 m0, s48, 3840\n"
    "v_cvt_pk_bf16_f32 v54, v60, v61\n"
    "v_cvt_pk_bf16_f32 v55, v62, v63\n"
    "v_mfma_f32_16x16x32_bf16 v[116:119], a[140:143], a[32:35], v[116:119]\n"
    "ds_read_b128 a[100:103], v18 offset:5264\n"
    "v_add_u32 v10, s58, v7\n"
    "v_mfma_f32_16x16x32_bf16 v[116:119], a[144:147], a[36:39], v[116:119]\n"
    "ds_read_b128 a[104:107], v18 offset:5840\n"
    "buffer_load_dwordx4 v10, s[16:19], 0 offen lds\n"
    "v_mfma_f32_16x16x32_bf16 v[116:119], a[148:151], a[40:43], v[116:119]\n"
    "buffer_load_dwordx4 a[212:215], v5, s[28:31], s50 offen offset:1024 sc1\n"
    "ds_read_b128 a[108:111], v18 offset:6416\n"
    "v_mfma_f32_16x16x32_bf16 v[116:119], a[152:155], a[44:47], v[116:119]\n"
    "v_mfma_f32_16x16x32_bf16 v[120:123], a[128:131], a[24:27], v[120:123]\n"
    "s_waitcnt lgkmcnt(3)\n"
    "v_mfma_f32_32x32x16_bf16 v[80:95], a[224:227], a[240:243], v[80:95]\n"
    "ds_write_b128 v15, v[48:51] offset:52480\n"
    "v_mfma_f32_16x16x32_bf16 v[120:123], a[132:135], a[28:31], v[120:123]\n"
    "v_mfma_f32_16x16x32_bf16 v[120:123], a[136:139], a[32:35], v[120:123]\n"
    "v_mfma_f32_16x16x32_bf16 v[120:123], a[140:143], a[36:39], v[120:123]\n"
    "v_cvt_pk_bf16_f32 v80, v80, v81\n"
    "v_cvt_pk_bf16_f32 v81, v82, v83\n"
    "v_mfma_f32_16x16x32_bf16 v[120:123], a[144:147], a[40:43], v[120:123]\n"
    "s_add_u32 m0, s48, 4864\n"
    "ds_read_b64_tr_b16 a[220:221], v12 offset:8448\n"
    "ds_read_b64_tr_b16 a[222:223], v12 offset:8704\n"
    "v_mfma_f32_16x16x32_bf16 v[120:123], a[148:151], a[44:47], v[120:123]\n"
    "v_cvt_pk_bf16_f32 v82, v84, v85\n"
    "v_cvt_pk_bf16_f32 v83, v86, v87\n"
    "v_mfma_f32_16x16x32_bf16 v[120:123], a[152:155], a[48:51], v[120:123]\n"
    "v_cvt_pk_bf16_f32 v84, v88, v89\n"
    "v_cvt_pk_bf16_f32 v85, v90, v91\n"
    "v_mfma_f32_16x16x32_bf16 v[124:127], a[128:131], a[28:31], v[124:127]\n"
    "v_cvt_pk_bf16_f32 v86, v92, v93\n"
    "v_cvt_pk_bf16_f32 v87, v94, v95\n"
    "v_mfma_f32_16x16x32_bf16 v[124:127], a[132:135], a[32:35], v[124:127]\n"
    "buffer_load_dwordx4 a[216:219], v5, s[28:31], s50 offen offset:2048 sc1\n"
    "s_add_u32 s50, s50, 3072\n"
    "v_mfma_f32_16x16x32_bf16 v[124:127], a[136:139], a[36:39], v[124:127]\n"
    "ds_read_b128 a[112:115], v18 offset:6992\n"
    "v_add_u32 v10, s58, v8\n"
    "v_mfma_f32_16x16x32_bf16 v[124:127], a[140:143], a[40:43], v[124:127]\n"
    "ds_read_b128 a[116:119], v18 offset:7568\n"
    "v_mfma_f32_16x16x32_bf16 v[124:127], a[144:147], a[44:47], v[124:127]\n"
    "ds_read_b128 a[120:123], v18 offset:8144\n"
    "v_mfma_f32_16x16x32_bf16 v[124:127], a[148:151], a[48:51], v[124:127]\n"
    "s_waitcnt lgkmcnt(3)\n"
    "v_mfma_f32_32x32x16_bf16 v[64:79], a[220:223], a[228:231], 0\n"
    "ds_write_b128 v15, v[52:55] offset:53056\n"
    "v_mfma_f32_16x16x32_bf16 v[124:127], a[152:155], a[52:55], v[124:127]\n"
    "s_mov_b32 exec_hi, 0xffff\n"
    "buffer_load_dwordx4 v10, s[16:19], 0 offen lds\n"
    "s_mov_b32 exec_hi, -1\n"
    "v_mfma_f32_16x16x32_bf16 v[128:131], a[128:131], a[32:35], v[128:131]\n"
    "s_add_u32 s58, s51, 344064\n"
    "s_add_u32 m0, s48, 5632\n"
    "v_mfma_f32_16x16x32_bf16 v[128:131], a[132:135], a[36:39], v[128:131]\n"
    "buffer_load_dwordx4 v6, s[16:19], s58 offen lds\n"
    "v_mfma_f32_16x16x32_bf16 v[128:131], a[136:139], a[40:43], v[128:131]\n"
    "ds_read_b64_tr_b16 a[224:225], v12 offset:8832\n"
    "ds_read_b64_tr_b16 a[226:227], v12 offset:9088\n"
    "v_mfma_f32_16x16x32_bf16 v[128:131], a[140:143], a[44:47], v[128:131]\n"
    "v_mfma_f32_16x16x32_bf16 v[128:131], a[144:147], a[48:51], v[128:131]\n"
    "v_mfma_f32_16x16x32_bf16 v[128:131], a[148:151], a[52:55], v[128:131]\n"
    "v_mfma_f32_16x16x32_bf16 v[128:131], a[152:155], a[56:59], v[128:131]\n"
    "s_waitcnt vmcnt(16) lgkmcnt(3)\n"
    "v_mfma_f32_16x16x32_bf16 v[136:139], a[156:159], a[64:67], v[136:139]\n"
    "buffer_load_dwordx4 a[128:131], v5, s[28:31], s50 offen offset:0 sc1\n"
    "v_mfma_f32_16x16x32_bf16 v[136:139], a[160:163], a[68:71], v[136:139]\n"
    "ds_read_b128 a[64:67], v19 offset:0\n"
    "v_mfma_f32_16x16x32_bf16 v[136:139], a[164:167], a[72:75], v[136:139]\n"
    "ds_read_b128 a[0:3], v18 offset:160\n"
    "v_mfma_f32_16x16x32_bf16 v[136:139], a[168:171], a[76:79], v[136:139]\n"
    "s_waitcnt lgkmcnt(2)\n"
    "s_add_u32 m0, s48, 6656\n"
    "v_mfma_f32_32x32x16_bf16 v[64:79], a[224:227], a[232:235], v[64:79]\n"
    "ds_write_b128 v16, v[80:83] offset:50176\n"
    "buffer_load_dwordx4 v7, s[16:19], s58 offen lds\n"
    "v_mfma_f32_16x16x32_bf16 v[136:139], a[172:175], a[80:83], v[136:139]\n"
    "v_mfma_f32_16x16x32_bf16 v[136:139], a[176:179], a[84:87], v[136:139]\n"
    "v_mfma_f32_16x16x32_bf16 v[136:139], a[180:183], a[88:91], v[136:139]\n"
    "v_cvt_pk_bf16_f32 v64, v64, v65\n"
    "v_cvt_pk_bf16_f32 v65, v66, v67\n"
    "v_mfma_f32_16x16x32_bf16 v[140:143], a[156:159], a[68:71], v[140:143]\n"
    "ds_read_b64_tr_b16 a[220:221], v12 offset:11264\n"
    "ds_read_b64_tr_b16 a[222:223], v12 offset:11520\n"
    "v_mfma_f32_16x16x32_bf16 v[140:143], a[160:163], a[72:75], v[140:143]\n"
    "v_cvt_pk_bf16_f32 v66, v68, v69\n"
    "v_cvt_pk_bf16_f32 v67, v70, v71\n"
    "v_mfma_f32_16x16x32_bf16 v[140:143], a[164:167], a[76:79], v[140:143]\n"
    "v_cvt_pk_bf16_f32 v68, v72, v73\n"
    "v_cvt_pk_bf16_f32 v69, v74, v75\n"
    "v_mfma_f32_16x16x32_bf16 v[140:143], a[168:171], a[80:83], v[140:143]\n"
    "v_cvt_pk_bf16_f32 v70, v76, v77\n"
    "v_cvt_pk_bf16_f32 v71, v78, v79\n"
    "v_mfma_f32_16x16x32_bf16 v[140:143], a[172:175], a[84:87], v[140:143]\n"
    "ds_read_b128 a[68:71], v19 offset:576\n"
    "v_mfma_f32_16x16x32_bf16 v[140:143], a[176:179], a[88:91], v[140:143]\n"
    "s_add_u32 m0, s48, 7680\n"
    "buffer_load_dwordx4 a[132:135], v5, s[28:31], s50 offen offset:1024 sc1\n"
    "ds_read_b128 a[4:7], v18 offset:736\n"
    "v_mfma_f32_16x16x32_bf16 v[140:143], a[180:183], a[92:95], v[140:143]\n"
    "ds_read_b128 a[8:11], v18 offset:1312\n"
    "v_mfma_f32_16x16x32_bf16 v[144:147], a[156:159], a[72:75], v[144:147]\n"
    "ds_read_b128 a[12:15], v18 offset:1888\n"
    "v_mfma_f32_16x16x32_bf16 v[144:147], a[160:163], a[76:79], v[144:147]\n"
    "s_waitcnt lgkmcnt(4)\n"
    "v_mfma_f32_32x32x16_bf16 v[48:63], a[220:223], a[228:231], 0\n"
    "ds_write_b128 v15, v[64:67] offset:53632\n"
    "v_mfma_f32_16x16x32_bf16 v[144:147], a[164:167], a[80:83], v[144:147]\n"
    "s_mov_b32 exec_hi, 0xffff\n"
    "buffer_load_dwordx4 v8, s[16:19], s58 offen lds\n"
    "s_mov_b32 exec_hi, -1\n"
    "v_mfma_f32_16x16x32_bf16 v[144:147], a[168:171], a[84:87], v[144:147]\n"
    "v_mfma_f32_16x16x32_bf16 v[144:147], a[172:175], a[88:91], v[144:147]\n"
    "ds_read_b128 a[72:75], v19 offset:1152\n"
    "v_mfma_f32_16x16x32_bf16 v[144:147], a[176:179], a[92:95], v[144:147]\n"
    "ds_read_b64_tr_b16 a[224:225], v12 offset:11648\n"
    "ds_read_b64_tr_b16 a[226:227], v12 offset:11904\n"
    "v_mfma_f32_16x16x32_bf16 v[144:147], a[180:183], a[96:99], v[144:147]\n"
    "ds_read_b128 a[16:19], v18 offset:2464\n"
    "v_mfma_f32_16x16x32_bf16 v[148:151], a[156:159], a[76:79], v[148:151]\n"
    "s_add_u32 s58, s51, 516096\n"
    "ds_read_b128 a[20:23], v18 offset:3040\n"
    "s_add_u32 m0, s48, 8448\n"
    "v_mfma_f32_16x16x32_bf16 v[148:151], a[160:163], a[80:83], v[148:151]\n"
    "ds_read_b128 a[76:79], v19 offset:1728\n"
    "buffer_load_dwordx4 v6, s[16:19], s58 offen lds\n"
    "v_mfma_f32_16x16x32_bf16 v[148:151], a[164:167], a[84:87], v[148:151]\n"
    "ds_read_b128 a[24:27], v18 offset:3616\n"
    "v_mfma_f32_16x16x32_bf16 v[148:151], a[168:171], a[88:91], v[148:151]\n"
    "v_mfma_f32_16x16x32_bf16 v[148:151], a[172:175], a[92:95], v[148:151]\n"
    "buffer_load_dwordx4 a[136:139], v5, s[28:31], s50 offen offset:2048 sc1\n"
    "v_mfma_f32_16x16x32_bf16 v[148:151], a[176:179], a[96:99], v[148:151]\n"
    "ds_read_b128 a[28:31], v18 offset:4192\n"
    "v_mfma_f32_16x16x32_bf16 v[148:151], a[180:183], a[100:103], v[148:151]\n"
    "s_waitcnt lgkmcnt(5)\n"
    "v_mfma_f32_32x32x16_bf16 v[48:63], a[224:227], a[232:235], v[48:63]\n"
    "ds_write_b128 v15, v[68:71] offset:54208\n"
    "v_mfma_f32_16x16x32_bf16 v[152:155], a[156:159], a[80:83], v[152:155]\n"
    "v_mfma_f32_16x16x32_bf16 v[152:155], a[160:163], a[84:87], v[152:155]\n"
    "v_mfma_f32_16x16x32_bf16 v[152:155], a[164:167], a[88:91], v[152:155]\n"
    "s_add_u32 m0, s48, 9472\n"
    "v_cvt_pk_bf16_f32 v48, v48, v49\n"
    "v_cvt_pk_bf16_f32 v49, v50, v51\n"
    "v_mfma_f32_16x16x32_bf16 v[152:155], a[168:171], a[92:95], v[152:155]\n"
    "ds_read_b64_tr_b16 a[220:221], v37 offset:14080\n"
    "ds_read_b64_tr_b16 a[222:223], v37 offset:14336\n"
    "v_mfma_f32_16x16x32_bf16 v[152:155], a[172:175], a[96:99], v[152:155]\n"
    "v_cvt_pk_bf16_f32 v50, v52, v53\n"
    "v_cvt_pk_bf16_f32 v51, v54, v55\n"
    "v_mfma_f32_16x16x32_bf16 v[152:155], a[176:179], a[100:103], v[152:155]\n"
    "v_cvt_pk_bf16_f32 v52, v56, v57\n"
    "v_cvt_pk_bf16_f32 v53, v58, v59\n"
    "v_mfma_f32_16x16x32_bf16 v[152:155], a[180:183], a[104:107], v[152:155]\n"
    "v_cvt_pk_bf16_f32 v54, v60, v61\n"
    "v_cvt_pk_bf16_f32 v55, v62, v63\n"
    "v_mfma_f32_16x16x32_bf16 v[156:159], a[156:159], a[84:87], v[156:159]\n"
    "ds_read_b128 a[80:83], v19 offset:2304\n"
    "buffer_load_dwordx4 v7, s[16:19], s58 offen lds\n"
    "v_mfma_f32_16x16x32_bf16 v[156:159], a[160:163], a[88:91], v[156:159]\n"
    "ds_read_b128 a[32:35], v18 offset:4768\n"
    "v_mfma_f32_16x16x32_bf16 v[156:159], a[164:167], a[92:95], v[156:159]\n"
    "ds_read_b128 a[84:87], v19 offset:2880\n"
    "v_mfma_f32_16x16x32_bf16 v[156:159], a[168:171], a[96:99], v[156:159]\n"
    "buffer_load_dwordx4 a[140:143], v5, s[28:31], s50 offen offset:3072 sc1\n"
    "s_add_u32 s50, s50, 4096\n"
    "v_mfma_f32_16x16x32_bf16 v[156:159], a[172:175], a[100:103], v[156:159]\n"
    "s_waitcnt lgkmcnt(3)\n"
    "v_mfma_f32_32x32x16_bf16 v[64:79], a[220:223], a[228:231], 0\n"
    "s_add_u32 m0, s48, 10496\n"
    "ds_write_b128 v15, v[48:51] offset:54784\n"
    "v_mfma_f32_16x16x32_bf16 v[156:159], a[176:179], a[104:107], v[156:159]\n"
    "s_mov_b32 exec_hi, 0xffff\n"
    "buffer_load_dwordx4 v8, s[16:19], s58 offen lds\n"
    "s_mov_b32 exec_hi, -1\n"
    "v_mfma_f32_16x16x32_bf16 v[156:159], a[180:183], a[108:111], v[156:159]\n"
    "v_mfma_f32_16x16x32_bf16 v[160:163], a[156:159], a[88:91], v[160:163]\n"
    "ds_read_b128 a[36:39], v18 offset:5344\n"
    "v_mfma_f32_16x16x32_bf16 v[160:163], a[160:163], a[92:95], v[160:163]\n"
    "ds_read_b64_tr_b16 a[224:225], v37 offset:14464\n"
    "ds_read_b64_tr_b16 a[226:227], v37 offset:14720\n"
    "v_mfma_f32_16x16x32_bf16 v[160:163], a[164:167], a[96:99], v[160:163]\n"
    "v_xor_b32 v12, 32, v12\n"
    "v_xor_b32 v13, 32, v13\n"
    "v_mfma_f32_16x16x32_bf16 v[160:163], a[168:171], a[100:103], v[160:163]\n"
    "v_xor_b32 v14, 32, v14\n"
    "v_xor_b32 v31, 32, v31\n"
    "v_mfma_f32_16x16x32_bf16 v[160:163], a[172:175], a[104:107], v[160:163]\n"
    "v_xor_b32 v32, 32, v32\n"
    "v_xor_b32 v37, 32, v37\n"
    "v_mfma_f32_16x16x32_bf16 v[160:163], a[176:179], a[108:111], v[160:163]\n"
    "ds_read_b128 a[88:91], v19 offset:3456\n"
    "v_mfma_f32_16x16x32_bf16 v[160:163], a[180:183], a[112:115], v[160:163]\n"
    "ds_read_b128 a[40:43], v18 offset:5920\n"
    "v_mfma_f32_16x16x32_bf16 v[164:167], a[156:159], a[92:95], v[164:167]\n"
    "s_add_u32 s58, s51, 688128\n"
    "ds_read_b128 a[44:47], v18 offset:6496\n"
    "s_add_u32 m0, s48, 11264\n"
    "v_mfma_f32_16x16x32_bf16 v[164:167], a[160:163], a[96:99], v[164:167]\n"
    "buffer_load_dwordx4 a[144:147], v5, s[28:31], s50 offen offset:0 sc1\n"
    "ds_read_b128 a[92:95], v19 offset:4032\n"
    "v_mfma_f32_16x16x32_bf16 v[164:167], a[164:167], a[100:103], v[164:167]\n"
    "s_waitcnt lgkmcnt(4)\n"
    "buffer_load_dwordx4 v6, s[16:19], s58 offen lds\n"
    "v_mfma_f32_32x32x16_bf16 v[64:79], a[224:227], a[232:235], v[64:79]\n"
    "ds_write_b128 v15, v[52:55] offset:55360\n"
    "v_mfma_f32_16x16x32_bf16 v[164:167], a[168:171], a[104:107], v[164:167]\n"
    "v_mfma_f32_16x16x32_bf16 v[164:167], a[172:175], a[108:111], v[164:167]\n"
    "v_mfma_f32_16x16x32_bf16 v[164:167], a[176:179], a[112:115], v[164:167]\n"
    "v_cvt_pk_bf16_f32 v64, v64, v65\n"
    "v_cvt_pk_bf16_f32 v65, v66, v67\n"
    "v_mfma_f32_16x16x32_bf16 v[164:167], a[180:183], a[116:119], v[164:167]\n"
    "v_cvt_pk_bf16_f32 v66, v68, v69\n"
    "v_cvt_pk_bf16_f32 v67, v70, v71\n"
    "v_mfma_f32_16x16x32_bf16 v[168:171], a[156:159], a[96:99], v[168:171]\n"
    "v_cvt_pk_bf16_f32 v68, v72, v73\n"
    "v_cvt_pk_bf16_f32 v69, v74, v75\n"
    "v_mfma_f32_16x16x32_bf16 v[168:171], a[160:163], a[100:103], v[168:171]\n"
    "v_cvt_pk_bf16_f32 v70, v76, v77\n"
    "v_cvt_pk_bf16_f32 v71, v78, v79\n"
    "v_mfma_f32_16x16x32_bf16 v[168:171], a[164:167], a[104:107], v[168:171]\n"
    "s_waitcnt vmcnt(19) lgkmcnt(2)\n"
    "s_add_u32 m0, s48, 12288\n"
    "ds_read_b64_tr_b16 a[220:221], v12 offset:0\n"
    "ds_read_b64_tr_b16 a[222:223], v12 offset:256\n"
    "v_mfma_f32_16x16x32_bf16 v[168:171], a[168:171], a[108:111], v[168:171]\n"
    "ds_read_b128 a[96:99], v19 offset:4608\n"
    "buffer_load_dwordx4 v7, s[16:19], s58 offen lds\n"
    "v_mfma_f32_16x16x32_bf16 v[168:171], a[172:175], a[112:115], v[168:171]\n"
    "ds_read_b128 a[48:51], v18 offset:7072\n"
    "v_mfma_f32_16x16x32_bf16 v[168:171], a[176:179], a[116:119], v[168:171]\n"
    "ds_read_b128 a[100:103], v19 offset:5184\n"
    "v_mfma_f32_16x16x32_bf16 v[168:171], a[180:183], a[120:123], v[168:171]\n"
    "s_waitcnt vmcnt(17) lgkmcnt(5)\n"
    "ds_read_b128 a[52:55], v18 offset:7648\n"
    "v_mfma_f32_16x16x32_bf16 v[176:179], a[184:187], a[0:3], v[176:179]\n"
    "s_waitcnt lgkmcnt(4)\n"
    "buffer_load_dwordx4 a[148:151], v5, s[28:31], s50 offen offset:1024 sc1\n"
    "v_mfma_f32_32x32x16_bf16 v[48:63], a[220:223], a[228:231], 0\n"
    "ds_write_b128 v16, v[84:87] offset:50752\n"
    "v_mfma_f32_16x16x32_bf16 v[176:179], a[188:191], a[4:7], v[176:179]\n"
    "v_mfma_f32_16x16x32_bf16 v[176:179], a[192:195], a[8:11], v[176:179]\n"
    "v_mfma_f32_16x16x32_bf16 v[176:179], a[196:199], a[12:15], v[176:179]\n"
    "ds_read_b128 a[104:107], v19 offset:5760\n"
    "v_mfma_f32_16x16x32_bf16 v[176:179], a[200:203], a[16:19], v[176:179]\n"
    "s_add_u32 m0, s48, 13312\n"
    "ds_read_b64_tr_b16 a[224:225], v12 offset:384\n"
    "ds_read_b64_tr_b16 a[226:227], v12 offset:640\n"
    "v_mfma_f32_16x16x32_bf16 v[176:179], a[204:207], a[20:23], v[176:179]\n"
    "ds_read_b128 a[56:59], v18 offset:8224\n"
    "v_mfma_f32_16x16x32_bf16 v[176:179], a[208:211], a[24:27], v[176:179]\n"
    "s_mov_b32 exec_hi, 0xffff\n"
    "buffer_load_dwordx4 v8, s[16:19], s58 offen lds\n"
    "s_mov_b32 exec_hi, -1\n"
    "v_mfma_f32_16x16x32_bf16 v[180:183], a[184:187], a[4:7], v[180:183]\n"
    "v_mfma_f32_16x16x32_bf16 v[180:183], a[188:191], a[8:11], v[180:183]\n"
    "v_mfma_f32_16x16x32_bf16 v[180:183], a[192:195], a[12:15], v[180:183]\n"
    "v_mfma_f32_16x16x32_bf16 v[180:183], a[196:199], a[16:19], v[180:183]\n"
    "v_mfma_f32_16x16x32_bf16 v[180:183], a[200:203], a[20:23], v[180:183]\n"
    "v_mfma_f32_16x16x32_bf16 v[180:183], a[204:207], a[24:27], v[180:183]\n"
    "s_waitcnt lgkmcnt(1)\n"
    "v_mfma_f32_32x32x16_bf16 v[48:63], a[224:227], a[232:235], v[48:63]\n"
    "ds_write_b128 v33, v[64:67] offset:55936\n"
    "v_mfma_f32_16x16x32_bf16 v[180:183], a[208:211], a[28:31], v[180:183]\n"
    "s_mov_b32 m0, s54\n"
    "buffer_load_dwordx4 a[152:155], v5, s[28:31], s50 offen offset:2048 sc1\n"
    "v_mfma_f32_16x16x32_bf16 v[184:187], a[184:187], a[8:11], v[184:187]\n"
    "buffer_load_dwordx4 v6, s[16:19], s57 offen lds\n"
    "v_mfma_f32_16x16x32_bf16 v[184:187], a[188:191], a[12:15], v[184:187]\n"
    "v_cvt_pk_bf16_f32 v48, v48, v49\n"
    "v_cvt_pk_bf16_f32 v49, v50, v51\n"
    "v_mfma_f32_16x16x32_bf16 v[184:187], a[192:195], a[16:19], v[184:187]\n"
    "v_cvt_pk_bf16_f32 v50, v52, v53\n"
    "v_cvt_pk_bf16_f32 v51, v54, v55\n"
    "v_mfma_f32_16x16x32_bf16 v[184:187], a[196:199], a[20:23], v[184:187]\n"
    "v_cvt_pk_bf16_f32 v52, v56, v57\n"
    "v_cvt_pk_bf16_f32 v53, v58, v59\n"
    "v_mfma_f32_16x16x32_bf16 v[184:187], a[200:203], a[24:27], v[184:187]\n"
    "v_cvt_pk_bf16_f32 v54, v60, v61\n"
    "v_cvt_pk_bf16_f32 v55, v62, v63\n"
    "v_mfma_f32_16x16x32_bf16 v[184:187], a[204:207], a[28:31], v[184:187]\n"
    "s_waitcnt vmcnt(12) lgkmcnt(1)\n"
    "ds_read_b64_tr_b16 a[220:221], v13\n"
    "ds_read_b64_tr_b16 a[222:223], v14\n"
    "v_mfma_f32_16x16x32_bf16 v[184:187], a[208:211], a[32:35], v[184:187]\n"
    "v_mfma_f32_16x16x32_bf16 v[188:191], a[184:187], a[12:15], v[188:191]\n"
    "v_mfma_f32_16x16x32_bf16 v[188:191], a[188:191], a[16:19], v[188:191]\n"
    "v_mfma_f32_16x16x32_bf16 v[188:191], a[192:195], a[20:23], v[188:191]\n"
    "s_mov_b32 m0, s55\n"
    "v_mfma_f32_16x16x32_bf16 v[188:191], a[196:199], a[24:27], v[188:191]\n"
    "s_waitcnt lgkmcnt(0)\n"
    "buffer_load_dwordx4 v7, s[16:19], s57 offen lds\n"
    "v_mfma_f32_32x32x16_bf16 v[80:95], a[220:223], a[236:239], 0\n"
    "ds_write_b128 v33, v[68:71] offset:56512\n"
    "v_mfma_f32_16x16x32_bf16 v[188:191], a[200:203], a[28:31], v[188:191]\n"
    "v_mfma_f32_16x16x32_bf16 v[188:191], a[204:207], a[32:35], v[188:191]\n"
    "buffer_load_dwordx4 a[156:159], v5, s[28:31], s50 offen offset:3072 sc1\n"
    "s_add_u32 s50, s50, 4096\n"
    "v_mfma_f32_16x16x32_bf16 v[188:191], a[208:211], a[36:39], v[188:191]\n"
    "v_mfma_f32_16x16x32_bf16 v[192:195], a[184:187], a[16:19], v[192:195]\n"
    "ds_read_b64_tr_b16 a[224:225], v12 offset:2816\n"
    "ds_read_b64_tr_b16 a[226:227], v12 offset:3072\n"
    "v_mfma_f32_16x16x32_bf16 v[192:195], a[188:191], a[20:23], v[192:195]\n"
    "v_mfma_f32_16x16x32_bf16 v[192:195], a[192:195], a[24:27], v[192:195]\n"
    "v_mfma_f32_16x16x32_bf16 v[192:195], a[196:199], a[28:31], v[192:195]\n"
    "v_mfma_f32_16x16x32_bf16 v[192:195], a[200:203], a[32:35], v[192:195]\n"
    "s_mov_b32 m0, s56\n"
    "v_mfma_f32_16x16x32_bf16 v[192:195], a[204:207], a[36:39], v[192:195]\n"
    "s_mov_b32 exec_hi, 0xffff\n"
    "buffer_load_dwordx4 v8, s[16:19], s57 offen lds\n"
    "s_mov_b32 exec_hi, -1\n"
    "v_mfma_f32_16x16x32_bf16 v[192:195], a[208:211], a[40:43], v[192:195]\n"
    "s_add_u32 s51, s51, 64\n"
    "s_add_u32 s57, s57, s59\n"
    "v_mfma_f32_16x16x32_bf16 v[196:199], a[184:187], a[20:23], v[196:199]\n"
    "v_mfma_f32_16x16x32_bf16 v[196:199], a[188:191], a[24:27], v[196:199]\n"
    "s_waitcnt lgkmcnt(0)\n"
    "v_mfma_f32_32x32x16_bf16 v[64:79], a[224:227], a[228:231], 0\n"
    "s_waitcnt lgkmcnt(0)\n"
    "s_barrier\n"
    "v_mfma_f32_16x16x32_bf16 v[196:199], a[192:195], a[28:31], v[196:199]\n"
    "ds_write_b128 v15, v[48:51] offset:0\n"
    "v_mfma_f32_16x16x32_bf16 v[196:199], a[196:199], a[32:35], v[196:199]\n"
    "buffer_load_dwordx4 a[160:163], v5, s[28:31], s50 offen offset:0 sc1\n"
    "v_mfma_f32_16x16x32_bf16 v[196:199], a[200:203], a[36:39], v[196:199]\n"
    "v_mfma_f32_16x16x32_bf16 v[196:199], a[204:207], a[40:43], v[196:199]\n"
    "ds_read_b128 a[0:3], v18 offset:50176\n"
    "v_mfma_f32_16x16x32_bf16 v[196:199], a[208:211], a[44:47], v[196:199]\n"
    "v_mfma_f32_16x16x32_bf16 v[200:203], a[184:187], a[24:27], v[200:203]\n"
    "ds_read_b128 a[4:7], v18 offset:50752\n"
    "v_mfma_f32_16x16x32_bf16 v[200:203], a[188:191], a[28:31], v[200:203]\n"
    "v_mfma_f32_16x16x32_bf16 v[200:203], a[192:195], a[32:35], v[200:203]\n"
    "ds_read_b128 a[8:11], v18 offset:51328\n"
    "v_mfma_f32_16x16x32_bf16 v[200:203], a[196:199], a[36:39], v[200:203]\n"
    "v_mfma_f32_16x16x32_bf16 v[200:203], a[200:203], a[40:43], v[200:203]\n"
    "ds_read_b128 a[12:15], v18 offset:51904\n"
    "v_mfma_f32_16x16x32_bf16 v[200:203], a[204:207], a[44:47], v[200:203]\n"
    "v_mfma_f32_16x16x32_bf16 v[200:203], a[208:211], a[48:51], v[200:203]\n"
    "ds_read_b128 a[16:19], v18 offset:52480\n"
    "v_mfma_f32_16x16x32_bf16 v[204:207], a[184:187], a[28:31], v[204:207]\n"
    "v_mfma_f32_16x16x32_bf16 v[204:207], a[188:191], a[32:35], v[204:207]\n"
    "ds_read_b128 a[20:23], v18 offset:53056\n"
    "v_mfma_f32_16x16x32_bf16 v[204:207], a[192:195], a[36:39], v[204:207]\n"
    "buffer_load_dwordx4 a[164:167], v5, s[28:31], s50 offen offset:1024 sc1\n"
    "v_mfma_f32_16x16x32_bf16 v[204:207], a[196:199], a[40:43], v[204:207]\n"
    "ds_read_b128 a[24:27], v18 offset:53632\n"
    "v_mfma_f32_16x16x32_bf16 v[204:207], a[200:203], a[44:47], v[204:207]\n"
    "v_mfma_f32_16x16x32_bf16 v[204:207], a[204:207], a[48:51], v[204:207]\n"
    "ds_read_b128 a[28:31], v18 offset:54208\n"
    "v_mfma_f32_16x16x32_bf16 v[204:207], a[208:211], a[52:55], v[204:207]\n"
    "v_mfma_f32_16x16x32_bf16 v[208:211], a[184:187], a[32:35], v[208:211]\n"
    "v_mfma_f32_16x16x32_bf16 v[208:211], a[188:191], a[36:39], v[208:211]\n"
    "ds_read_b128 a[32:35], v18 offset:54784\n"
    "v_mfma_f32_16x16x32_bf16 v[208:211], a[192:195], a[40:43], v[208:211]\n"
    "v_mfma_f32_16x16x32_bf16 v[208:211], a[196:199], a[44:47], v[208:211]\n"
    "ds_read_b128 a[36:39], v18 offset:55360\n"
    "v_mfma_f32_16x16x32_bf16 v[208:211], a[200:203], a[48:51], v[208:211]\n"
    "v_mfma_f32_16x16x32_bf16 v[208:211], a[204:207], a[52:55], v[208:211]\n"
    "ds_read_b128 a[40:43], v18 offset:55936\n"
    "v_mfma_f32_16x16x32_bf16 v[208:211], a[208:211], a[56:59], v[208:211]\n"
    "v_mfma_f32_16x16x32_bf16 v[216:219], a[212:215], a[64:67], v[216:219]\n"
    "ds_read_b128 a[44:47], v18 offset:56512\n"
    "v_mfma_f32_16x16x32_bf16 v[216:219], a[216:219], a[72:75], v[216:219]\n"
    "buffer_load_dwordx4 a[168:171], v5, s[28:31], s50 offen offset:2048 sc1\n"
    "v_mfma_f32_16x16x32_bf16 v[220:223], a[212:215], a[68:71], v[220:223]\n"
    "ds_read_b128 a[48:51], v18 offset:57088\n"
    "v_mfma_f32_16x16x32_bf16 v[220:223], a[216:219], a[76:79], v[220:223]\n"
    "v_mfma_f32_16x16x32_bf16 v[224:227], a[212:215], a[72:75], v[224:227]\n"
    "ds_read_b128 a[52:55], v18 offset:57664\n"
    "v_mfma_f32_16x16x32_bf16 v[224:227], a[216:219], a[80:83], v[224:227]\n"
    "v_mfma_f32_16x16x32_bf16 v[228:231], a[212:215], a[76:79], v[228:231]\n"
    "ds_read_b128 a[56:59], v18 offset:58240\n"
    "v_mfma_f32_16x16x32_bf16 v[228:231], a[216:219], a[84:87], v[228:231]\n"
    "v_mfma_f32_16x16x32_bf16 v[232:235], a[212:215], a[80:83], v[232:235]\n"
    "v_mfma_f32_16x16x32_bf16 v[232:235], a[216:219], a[88:91], v[232:235]\n"
    "v_mfma_f32_16x16x32_bf16 v[236:239], a[212:215], a[84:87], v[236:239]\n"
    "v_mfma_f32_16x16x32_bf16 v[236:239], a[216:219], a[92:95], v[236:239]\n"
    "v_mfma_f32_16x16x32_bf16 v[240:243], a[212:215], a[88:91], v[240:243]\n"
    "v_mfma_f32_16x16x32_bf16 v[240:243], a[216:219], a[96:99], v[240:243]\n"
    "v_mfma_f32_16x16x32_bf16 v[244:247], a[212:215], a[92:95], v[244:247]\n"
    "v_mfma_f32_16x16x32_bf16 v[244:247], a[216:219], a[100:103], v[244:247]\n"
    "v_mfma_f32_16x16x32_bf16 v[248:251], a[212:215], a[96:99], v[248:251]\n"
    "v_mfma_f32_16x16x32_bf16 v[248:251], a[216:219], a[104:107], v[248:251]\n"
    "s_waitcnt vmcnt(7) lgkmcnt(0)\n"
    "s_mov_b32 s52, 1\n"
    "s_branch 11b\n"
    "12:\n"
    "v_mfma_f32_16x16x32_bf16 v[96:99], a[128:131], a[0:3], v[96:99]\n"
    "ds_read_b64_tr_b16 a[220:221], v12 offset:3200\n"
    "ds_read_b64_tr_b16 a[222:223], v12 offset:3456\n"
    "v_mfma_f32_16x16x32_bf16 v[96:99], a[132:135], a[4:7], v[96:99]\n"
    "buffer_load_dwordx4 a[192:195], v5, s[28:31], s50 offen offset:0 sc1\n"
    "ds_read_b128 a[64:67], v18 offset:80\n"
    "v_mfma_f32_16x16x32_bf16 v[96:99], a[136:139], a[8:11], v[96:99]\n"
    "v_mfma_f32_16x16x32_bf16 v[96:99], a[140:143], a[12:15], v[96:99]\n"
    "v_mfma_f32_16x16x32_bf16 v[96:99], a[144:147], a[16:19], v[96:99]\n"
    "ds_read_b128 a[68:71], v18 offset:656\n"
    "v_mfma_f32_16x16x32_bf16 v[96:99], a[148:151], a[20:23], v[96:99]\n"
    "v_mfma_f32_16x16x32_bf16 v[96:99], a[152:155], a[24:27], v[96:99]\n"
    "s_waitcnt lgkmcnt(2)\n"
    "v_mfma_f32_32x32x16_bf16 v[64:79], a[220:223], a[232:235], v[64:79]\n"
    "ds_write_b128 v15, v[52:55] offset:50752\n"
    "v_mfma_f32_16x16x32_bf16 v[100:103], a[128:131], a[4:7], v[100:103]\n"
    "v_mfma_f32_16x16x32_bf16 v[100:103], a[132:135], a[8:11], v[100:103]\n"
    "buffer_load_dwordx4 a[196:199], v5, s[28:31], s50 offen offset:1024 sc1\n"
    "v_mfma_f32_16x16x32_bf16 v[100:103], a[136:139], a[12:15], v[100:103]\n"
    "v_cvt_pk_bf16_f32 v64, v64, v65\n"
    "v_cvt_pk_bf16_f32 v65, v66, v67\n"
    "v_mfma_f32_16x16x32_bf16 v[100:103], a[140:143], a[16:19], v[100:103]\n"
    "v_cvt_pk_bf16_f32 v66, v68, v69\n"
    "v_cvt_pk_bf16_f32 v67, v70, v71\n"
    "v_mfma_f32_16x16x32_bf16 v[100:103], a[144:147], a[20:23], v[100:103]\n"
    "v_cvt_pk_bf16_f32 v68, v72, v73\n"
    "v_cvt_pk_bf16_f32 v69, v74, v75\n"
    "v_mfma_f32_16x16x32_bf16 v[100:103], a[148:151], a[24:27], v[100:103]\n"
    "ds_read_b64_tr_b16 a[224:225], v12 offset:5632\n"
    "ds_read_b64_tr_b16 a[226:227], v12 offset:5888\n"
    "v_mfma_f32_16x16x32_bf16 v[100:103], a[152:155], a[28:31], v[100:103]\n"
    "v_cvt_pk_bf16_f32 v70, v76, v77\n"
    "v_cvt_pk_bf16_f32 v71, v78, v79\n"
    "v_mfma_f32_16x16x32_bf16 v[104:107], a[128:131], a[8:11], v[104:107]\n"
    "ds_read_b128 a[72:75], v18 offset:1232\n"
    "v_mfma_f32_16x16x32_bf16 v[104:107], a[132:135], a[12:15], v[104:107]\n"
    "ds_read_b128 a[76:79], v18 offset:1808\n"
    "v_mfma_f32_16x16x32_bf16 v[104:107], a[136:139], a[16:19], v[104:107]\n"
    "buffer_load_dwordx4 a[200:203], v5, s[28:31], s50 offen offset:2048 sc1\n"
    "ds_read_b128 a[80:83], v18 offset:2384\n"
    "v_mfma_f32_16x16x32_bf16 v[104:107], a[140:143], a[20:23], v[104:107]\n"
    "v_mfma_f32_16x16x32_bf16 v[104:107], a[144:147], a[24:27], v[104:107]\n"
    "v_mfma_f32_16x16x32_bf16 v[104:107], a[148:151], a[28:31], v[104:107]\n"
    "ds_read_b128 a[84:87], v18 offset:2960\n"
    "v_mfma_f32_16x16x32_bf16 v[104:107], a[152:155], a[32:35], v[104:107]\n"
    "s_waitcnt lgkmcnt(4)\n"
    "v_mfma_f32_32x32x16_bf16 v[48:63], a[224:227], a[228:231], 0\n"
    "ds_write_b128 v15, v[64:67] offset:51328\n"
    "v_mfma_f32_16x16x32_bf16 v[108:111], a[128:131], a[12:15], v[108:111]\n"
    "v_mfma_f32_16x16x32_bf16 v[108:111], a[132:135], a[16:19], v[108:111]\n"
    "v_mfma_f32_16x16x32_bf16 v[108:111], a[136:139], a[20:23], v[108:111]\n"
    "ds_read_b128 a[88:91], v18 offset:3536\n"
    "v_mfma_f32_16x16x32_bf16 v[108:111], a[140:143], a[24:27], v[108:111]\n"
    "buffer_load_dwordx4 a[204:207], v5, s[28:31], s50 offen offset:3072 sc1\n"
    "s_add_u32 s50, s50, 4096\n"
    "v_mfma_f32_16x16x32_bf16 v[108:111], a[144:147], a[28:31], v[108:111]\n"
    "v_mfma_f32_16x16x32_bf16 v[108:111], a[148:151], a[32:35], v[108:111]\n"
    "ds_read_b128 a[92:95], v18 offset:4112\n"
    "v_mfma_f32_16x16x32_bf16 v[108:111], a[152:155], a[36:39], v[108:111]\n"
    "ds_read_b64_tr_b16 a[220:221], v12 offset:6016\n"
    "ds_read_b64_tr_b16 a[222:223], v12 offset:6272\n"
    "v_mfma_f32_16x16x32_bf16 v[112:115], a[128:131], a[16:19], v[112:115]\n"
    "v_mfma_f32_16x16x32_bf16 v[112:115], a[132:135], a[20:23], v[112:115]\n"
    "ds_read_b128 a[96:99], v18 offset:4688\n"
    "v_mfma_f32_16x16x32_bf16 v[112:115], a[136:139], a[24:27], v[112:115]\n"
    "v_mfma_f32_16x16x32_bf16 v[112:115], a[140:143], a[28:31], v[112:115]\n"
    "v_mfma_f32_16x16x32_bf16 v[112:115], a[144:147], a[32:35], v[112:115]\n"
    "buffer_load_dwordx4 a[208:211], v5, s[28:31], s50 offen offset:0 sc1\n"
    "v_mfma_f32_16x16x32_bf16 v[112:115], a[148:151], a[36:39], v[112:115]\n"
    "ds_read_b128 a[100:103], v18 offset:5264\n"
    "v_mfma_f32_16x16x32_bf16 v[112:115], a[152:155], a[40:43], v[112:115]\n"
    "v_mfma_f32_16x16x32_bf16 v[116:119], a[128:131], a[20:23], v[116:119]\n"
    "s_waitcnt lgkmcnt(2)\n"
    "v_mfma_f32_32x32x16_bf16 v[48:63], a[220:223], a[232:235], v[48:63]\n"
    "ds_write_b128 v15, v[68:71] offset:51904\n"
    "v_mfma_f32_16x16x32_bf16 v[116:119], a[132:135], a[24:27], v[116:119]\n"
    "v_mfma_f32_16x16x32_bf16 v[116:119], a[136:139], a[28:31], v[116:119]\n"
    "v_mfma_f32_16x16x32_bf16 v[116:119], a[140:143], a[32:35], v[116:119]\n"
    "v_cvt_pk_bf16_f32 v48, v48, v49\n"
    "v_cvt_pk_bf16_f32 v49, v50, v51\n"
    "v_mfma_f32_16x16x32_bf16 v[116:119], a[144:147], a[36:39], v[116:119]\n"
    "v_cvt_pk_bf16_f32 v50, v52, v53\n"
    "v_cvt_pk_bf16_f32 v51, v54, v55\n"
    "v_mfma_f32_16x16x32_bf16 v[116:119], a[148:151], a[40:43], v[116:119]\n"
    "v_cvt_pk_bf16_f32 v52, v56, v57\n"
    "v_cvt_pk_bf16_f32 v53, v58, v59\n"
    "v_mfma_f32_16x16x32_bf16 v[116:119], a[152:155], a[44:47], v[116:119]\n"
    "v_cvt_pk_bf16_f32 v54, v60, v61\n"
    "v_cvt_pk_bf16_f32 v55, v62, v63\n"
    "v_mfma_f32_16x16x32_bf16 v[120:123], a[128:131], a[24:27], v[120:123]\n"
    "ds_read_b64_tr_b16 a[224:225], v31\n"
    "ds_read_b64_tr_b16 a[226:227], v32\n"
    "v_mfma_f32_16x16x32_bf16 v[120:123], a[132:135], a[28:31], v[120:123]\n"
    "buffer_load_dwordx4 a[212:215], v5, s[28:31], s50 offen offset:1024 sc1\n"
    "ds_read_b128 a[104:107], v18 offset:5840\n"
    "v_mfma_f32_16x16x32_bf16 v[120:123], a[136:139], a[32:35], v[120:123]\n"
    "ds_read_b128 a[108:111], v18 offset:6416\n"
    "v_mfma_f32_16x16x32_bf16 v[120:123], a[140:143], a[36:39], v[120:123]\n"
    "ds_read_b128 a[112:115], v18 offset:6992\n"
    "v_mfma_f32_16x16x32_bf16 v[120:123], a[144:147], a[40:43], v[120:123]\n"
    "v_mfma_f32_16x16x32_bf16 v[120:123], a[148:151], a[44:47], v[120:123]\n"
    "ds_read_b128 a[116:119], v18 offset:7568\n"
    "v_mfma_f32_16x16x32_bf16 v[120:123], a[152:155], a[48:51], v[120:123]\n"
    "buffer_load_dwordx4 a[216:219], v5, s[28:31], s50 offen offset:2048 sc1\n"
    "s_add_u32 s50, s50, 3072\n"
    "v_mfma_f32_16x16x32_bf16 v[124:127], a[128:131], a[28:31], v[124:127]\n"
    "v_mfma_f32_16x16x32_bf16 v[124:127], a[132:135], a[32:35], v[124:127]\n"
    "s_waitcnt lgkmcnt(4)\n"
    "v_mfma_f32_32x32x16_bf16 v[80:95], a[224:227], a[240:243], v[80:95]\n"
    "ds_write_b128 v15, v[48:51] offset:52480\n"
    "v_mfma_f32_16x16x32_bf16 v[124:127], a[136:139], a[36:39], v[124:127]\n"
    "v_mfma_f32_16x16x32_bf16 v[124:127], a[140:143], a[40:43], v[124:127]\n"
    "v_mfma_f32_16x16x32_bf16 v[124:127], a[144:147], a[44:47], v[124:127]\n"
    "v_cvt_pk_bf16_f32 v80, v80, v81\n"
    "v_cvt_pk_bf16_f32 v81, v82, v83\n"
    "v_mfma_f32_16x16x32_bf16 v[124:127], a[148:151], a[48:51], v[124:127]\n"
    "v_cvt_pk_bf16_f32 v82, v84, v85\n"
    "v_cvt_pk_bf16_f32 v83, v86, v87\n"
    "v_mfma_f32_16x16x32_bf16 v[124:127], a[152:155], a[52:55], v[124:127]\n"
    "v_cvt_pk_bf16_f32 v84, v88, v89\n"
    "v_cvt_pk_bf16_f32 v85, v90, v91\n"
    "v_mfma_f32_16x16x32_bf16 v[128:131], a[128:131], a[32:35], v[128:131]\n"
    "v_cvt_pk_bf16_f32 v86, v92, v93\n"
    "v_cvt_pk_bf16_f32 v87, v94, v95\n"
    "v_mfma_f32_16x16x32_bf16 v[128:131], a[132:135], a[36:39], v[128:131]\n"
    "ds_read_b64_tr_b16 a[220:221], v12 offset:8448\n"
    "ds_read_b64_tr_b16 a[222:223], v12 offset:8704\n"
    "v_mfma_f32_16x16x32_bf16 v[128:131], a[136:139], a[40:43], v[128:131]\n"
    "ds_read_b128 a[120:123], v18 offset:8144\n"
    "v_mfma_f32_16x16x32_bf16 v[128:131], a[140:143], a[44:47], v[128:131]\n"
    "v_mfma_f32_16x16x32_bf16 v[128:131], a[144:147], a[48:51], v[128:131]\n"
    "v_mfma_f32_16x16x32_bf16 v[128:131], a[148:151], a[52:55], v[128:131]\n"
    "v_mfma_f32_16x16x32_bf16 v[128:131], a[152:155], a[56:59], v[128:131]\n"
    "s_waitcnt vmcnt(9) lgkmcnt(3)\n"
    "buffer_load_dwordx4 a[128:131], v5, s[28:31], s50 offen offset:0 sc1\n"
    "v_mfma_f32_16x16x32_bf16 v[136:139], a[156:159], a[64:67], v[136:139]\n"
    "v_mfma_f32_16x16x32_bf16 v[136:139], a[160:163], a[68:71], v[136:139]\n"
    "ds_read_b128 a[64:67], v19 offset:0\n"
    "v_mfma_f32_16x16x32_bf16 v[136:139], a[164:167], a[72:75], v[136:139]\n"
    "s_waitcnt lgkmcnt(2)\n"
    "v_mfma_f32_32x32x16_bf16 v[64:79], a[220:223], a[228:231], 0\n"
    "ds_write_b128 v15, v[52:55] offset:53056\n"
    "v_mfma_f32_16x16x32_bf16 v[136:139], a[168:171], a[76:79], v[136:139]\n"
    "v_mfma_f32_16x16x32_bf16 v[136:139], a[172:175], a[80:83], v[136:139]\n"
    "v_mfma_f32_16x16x32_bf16 v[136:139], a[176:179], a[84:87], v[136:139]\n"
    "ds_read_b128 a[0:3], v18 offset:160\n"
    "v_mfma_f32_16x16x32_bf16 v[136:139], a[180:183], a[88:91], v[136:139]\n"
    "ds_read_b128 a[4:7], v18 offset:736\n"
    "v_mfma_f32_16x16x32_bf16 v[140:143], a[156:159], a[68:71], v[140:143]\n"
    "v_mfma_f32_16x16x32_bf16 v[140:143], a[160:163], a[72:75], v[140:143]\n"
    "ds_read_b64_tr_b16 a[224:225], v12 offset:8832\n"
    "ds_read_b64_tr_b16 a[226:227], v12 offset:9088\n"
    "v_mfma_f32_16x16x32_bf16 v[140:143], a[164:167], a[76:79], v[140:143]\n"
    "buffer_load_dwordx4 a[132:135], v5, s[28:31], s50 offen offset:1024 sc1\n"
    "ds_read_b128 a[68:71], v19 offset:576\n"
    "v_mfma_f32_16x16x32_bf16 v[140:143], a[168:171], a[80:83], v[140:143]\n"
    "ds_read_b128 a[8:11], v18 offset:1312\n"
    "v_mfma_f32_16x16x32_bf16 v[140:143], a[172:175], a[84:87], v[140:143]\n"
    "ds_read_b128 a[12:15], v18 offset:1888\n"
    "v_mfma_f32_16x16x32_bf16 v[140:143], a[176:179], a[88:91], v[140:143]\n"
    "v_mfma_f32_16x16x32_bf16 v[140:143], a[180:183], a[92:95], v[140:143]\n"
    "v_mfma_f32_16x16x32_bf16 v[144:147], a[156:159], a[72:75], v[144:147]\n"
    "v_mfma_f32_16x16x32_bf16 v[144:147], a[160:163], a[76:79], v[144:147]\n"
    "ds_read_b128 a[72:75], v19 offset:1152\n"
    "v_mfma_f32_16x16x32_bf16 v[144:147], a[164:167], a[80:83], v[144:147]\n"
    "s_waitcnt lgkmcnt(4)\n"
    "v_mfma_f32_32x32x16_bf16 v[64:79], a[224:227], a[232:235], v[64:79]\n"
    "ds_write_b128 v16, v[80:83] offset:50176\n"
    "v_mfma_f32_16x16x32_bf16 v[144:147], a[168:171], a[84:87], v[144:147]\n"
    "buffer_load_dwordx4 a[136:139], v5, s[28:31], s50 offen offset:2048 sc1\n"
    "v_mfma_f32_16x16x32_bf16 v[144:147], a[172:175], a[88:91], v[144:147]\n"
    "v_mfma_f32_16x16x32_bf16 v[144:147], a[176:179], a[92:95], v[144:147]\n"
    "v_cvt_pk_bf16_f32 v64, v64, v65\n"
    "v_cvt_pk_bf16_f32 v65, v66, v67\n"
    "v_mfma_f32_16x16x32_bf16 v[144:147], a[180:183], a[96:99], v[144:147]\n"
    "v_cvt_pk_bf16_f32 v66, v68, v69\n"
    "v_cvt_pk_bf16_f32 v67, v70, v71\n"
    "v_mfma_f32_16x16x32_bf16 v[148:151], a[156:159], a[76:79], v[148:151]\n"
    "v_cvt_pk_bf16_f32 v68, v72, v73\n"
    "v_cvt_pk_bf16_f32 v69, v74, v75\n"
    "v_mfma_f32_16x16x32_bf16 v[148:151], a[160:163], a[80:83], v[148:151]\n"
    "v_cvt_pk_bf16_f32 v70, v76, v77\n"
    "v_cvt_pk_bf16_f32 v71, v78, v79\n"
    "v_mfma_f32_16x16x32_bf16 v[148:151], a[164:167], a[84:87], v[148:151]\n"
    "ds_read_b64_tr_b16 a[220:221], v12 offset:11264\n"
    "ds_read_b64_tr_b16 a[222:223], v12 offset:11520\n"
    "v_mfma_f32_16x16x32_bf16 v[148:151], a[168:171], a[88:91], v[148:151]\n"
    "ds_read_b128 a[76:79], v19 offset:1728\n"
    "v_mfma_f32_16x16x32_bf16 v[148:151], a[172:175], a[92:95], v[148:151]\n"
    "ds_read_b128 a[16:19], v18 offset:2464\n"
    "v_mfma_f32_16x16x32_bf16 v[148:151], a[176:179], a[96:99], v[148:151]\n"
    "buffer_load_dwordx4 a[140:143], v5, s[28:31], s50 offen offset:3072 sc1\n"
    "s_add_u32 s50, s50, 4096\n"
    "v_mfma_f32_16x16x32_bf16 v[148:151], a[180:183], a[100:103], v[148:151]\n"
    "ds_read_b128 a[20:23], v18 offset:3040\n"
    "v_mfma_f32_16x16x32_bf16 v[152:155], a[156:159], a[80:83], v[152:155]\n"
    "ds_read_b128 a[24:27], v18 offset:3616\n"
    "v_mfma_f32_16x16x32_bf16 v[152:155], a[160:163], a[84:87], v[152:155]\n"
    "ds_read_b128 a[80:83], v19 offset:2304\n"
    "v_mfma_f32_16x16x32_bf16 v[152:155], a[164:167], a[88:91], v[152:155]\n"
    "ds_read_b128 a[28:31], v18 offset:4192\n"
    "v_mfma_f32_16x16x32_bf16 v[152:155], a[168:171], a[92:95], v[152:155]\n"
    "s_waitcnt lgkmcnt(6)\n"
    "v_mfma_f32_32x32x16_bf16 v[48:63], a[220:223], a[228:231], 0\n"
    "ds_write_b128 v15, v[64:67] offset:53632\n"
    "v_mfma_f32_16x16x32_bf16 v[152:155], a[172:175], a[96:99], v[152:155]\n"
    "v_mfma_f32_16x16x32_bf16 v[152:155], a[176:179], a[100:103], v[152:155]\n"
    "v_mfma_f32_16x16x32_bf16 v[152:155], a[180:183], a[104:107], v[152:155]\n"
    "ds_read_b128 a[32:35], v18 offset:4768\n"
    "v_mfma_f32_16x16x32_bf16 v[156:159], a[156:159], a[84:87], v[156:159]\n"
    "buffer_load_dwordx4 a[144:147], v5, s[28:31], s50 offen offset:0 sc1\n"
    "ds_read_b128 a[36:39], v18 offset:5344\n"
    "v_mfma_f32_16x16x32_bf16 v[156:159], a[160:163], a[88:91], v[156:159]\n"
    "ds_read_b128 a[84:87], v19 offset:2880\n"
    "v_mfma_f32_16x16x32_bf16 v[156:159], a[164:167], a[92:95], v[156:159]\n"
    "ds_read_b128 a[40:43], v18 offset:5920\n"
    "v_mfma_f32_16x16x32_bf16 v[156:159], a[168:171], a[96:99], v[156:159]\n"
    "ds_read_b64_tr_b16 a[224:225], v12 offset:11648\n"
    "ds_read_b64_tr_b16 a[226:227], v12 offset:11904\n"
    "v_mfma_f32_16x16x32_bf16 v[156:159], a[172:175], a[100:103], v[156:159]\n"
    "v_mfma_f32_16x16x32_bf16 v[156:159], a[176:179], a[104:107], v[156:159]\n"
    "ds_read_b128 a[44:47], v18 offset:6496\n"
    "v_mfma_f32_16x16x32_bf16 v[156:159], a[180:183], a[108:111], v[156:159]\n"
    "v_mfma_f32_16x16x32_bf16 v[160:163], a[156:159], a[88:91], v[160:163]\n"
    "v_mfma_f32_16x16x32_bf16 v[160:163], a[160:163], a[92:95], v[160:163]\n"
    "ds_read_b128 a[88:91], v19 offset:3456\n"
    "v_mfma_f32_16x16x32_bf16 v[160:163], a[164:167], a[96:99], v[160:163]\n"
    "buffer_load_dwordx4 a[148:151], v5, s[28:31], s50 offen offset:1024 sc1\n"
    "ds_read_b128 a[48:51], v18 offset:7072\n"
    "v_mfma_f32_16x16x32_bf16 v[160:163], a[168:171], a[100:103], v[160:163]\n"
    "v_mfma_f32_16x16x32_bf16 v[160:163], a[172:175], a[104:107], v[160:163]\n"
    "s_waitcnt lgkmcnt(3)\n"
    "v_mfma_f32_32x32x16_bf16 v[48:63], a[224:227], a[232:235], v[48:63]\n"
    "ds_write_b128 v15, v[68:71] offset:54208\n"
    "v_mfma_f32_16x16x32_bf16 v[160:163], a[176:179], a[108:111], v[160:163]\n"
    "v_mfma_f32_16x16x32_bf16 v[160:163], a[180:183], a[112:115], v[160:163]\n"
    "v_mfma_f32_16x16x32_bf16 v[164:167], a[156:159], a[92:95], v[164:167]\n"
    "v_cvt_pk_bf16_f32 v48, v48, v49\n"
    "v_cvt_pk_bf16_f32 v49, v50, v51\n"
    "v_mfma_f32_16x16x32_bf16 v[164:167], a[160:163], a[96:99], v[164:167]\n"
    "v_cvt_pk_bf16_f32 v50, v52, v53\n"
    "v_cvt_pk_bf16_f32 v51, v54, v55\n"
    "v_mfma_f32_16x16x32_bf16 v[164:167], a[164:167], a[100:103], v[164:167]\n"
    "v_cvt_pk_bf16_f32 v52, v56, v57\n"
    "v_cvt_pk_bf16_f32 v53, v58, v59\n"
    "v_mfma_f32_16x16x32_bf16 v[164:167], a[168:171], a[104:107], v[164:167]\n"
    "v_cvt_pk_bf16_f32 v54, v60, v61\n"
    "v_cvt_pk_bf16_f32 v55, v62, v63\n"
    "v_mfma_f32_16x16x32_bf16 v[164:167], a[172:175], a[108:111], v[164:167]\n"
    "ds_read_b64_tr_b16 a[220:221], v37 offset:14080\n"
    "ds_read_b64_tr_b16 a[222:223], v37 offset:14336\n"
    "v_mfma_f32_16x16x32_bf16 v[164:167], a[176:179], a[112:115], v[164:167]\n"
    "buffer_load_dwordx4 a[152:155], v5, s[28:31], s50 offen offset:2048 sc1\n"
    "ds_read_b128 a[92:95], v19 offset:4032\n"
    "v_mfma_f32_16x16x32_bf16 v[164:167], a[180:183], a[116:119], v[164:167]\n"
    "ds_read_b128 a[52:55], v18 offset:7648\n"
    "v_mfma_f32_16x16x32_bf16 v[168:171], a[156:159], a[96:99], v[168:171]\n"
    "ds_read_b128 a[56:59], v18 offset:8224\n"
    "v_mfma_f32_16x16x32_bf16 v[168:171], a[160:163], a[100:103], v[168:171]\n"
    "ds_read_b128 a[96:99], v19 offset:4608\n"
    "v_mfma_f32_16x16x32_bf16 v[168:171], a[164:167], a[104:107], v[168:171]\n"
    "v_mfma_f32_16x16x32_bf16 v[168:171], a[168:171], a[108:111], v[168:171]\n"
    "ds_read_b128 a[100:103], v19 offset:5184\n"
    "v_mfma_f32_16x16x32_bf16 v[168:171], a[172:175], a[112:115], v[168:171]\n"
    "v_mfma_f32_16x16x32_bf16 v[168:171], a[176:179], a[116:119], v[168:171]\n"
    "s_waitcnt lgkmcnt(5)\n"
    "v_mfma_f32_32x32x16_bf16 v[64:79], a[220:223], a[228:231], 0\n"
    "ds_write_b128 v15, v[48:51] offset:54784\n"
    "v_mfma_f32_16x16x32_bf16 v[168:171], a[180:183], a[120:123], v[168:171]\n"
    "s_waitcnt vmcnt(9)\n"
    "buffer_load_dwordx4 a[156:159], v5, s[28:31], s50 offen offset:3072 sc1\n"
    "s_add_u32 s50, s50, 4096\n"
    "v_mfma_f32_16x16x32_bf16 v[176:179], a[184:187], a[0:3], v[176:179]\n"
    "v_mfma_f32_16x16x32_bf16 v[176:179], a[188:191], a[4:7], v[176:179]\n"
    "ds_read_b128 a[104:107], v19 offset:5760\n"
    "v_mfma_f32_16x16x32_bf16 v[176:179], a[192:195], a[8:11], v[176:179]\n"
    "v_mfma_f32_16x16x32_bf16 v[176:179], a[196:199], a[12:15], v[176:179]\n"
    "v_mfma_f32_16x16x32_bf16 v[176:179], a[200:203], a[16:19], v[176:179]\n"
    "v_mfma_f32_16x16x32_bf16 v[176:179], a[204:207], a[20:23], v[176:179]\n"
    "ds_read_b64_tr_b16 a[224:225], v37 offset:14464\n"
    "ds_read_b64_tr_b16 a[226:227], v37 offset:14720\n"
    "v_mfma_f32_16x16x32_bf16 v[176:179], a[208:211], a[24:27], v[176:179]\n"
    "v_mfma_f32_16x16x32_bf16 v[180:183], a[184:187], a[4:7], v[180:183]\n"
    "v_mfma_f32_16x16x32_bf16 v[180:183], a[188:191], a[8:11], v[180:183]\n"
    "buffer_load_dwordx4 a[160:163], v5, s[28:31], s50 offen offset:0 sc1\n"
    "v_mfma_f32_16x16x32_bf16 v[180:183], a[192:195], a[12:15], v[180:183]\n"
    "v_mfma_f32_16x16x32_bf16 v[180:183], a[196:199], a[16:19], v[180:183]\n"
    "v_mfma_f32_16x16x32_bf16 v[180:183], a[200:203], a[20:23], v[180:183]\n"
    "v_mfma_f32_16x16x32_bf16 v[180:183], a[204:207], a[24:27], v[180:183]\n"
    "v_mfma_f32_16x16x32_bf16 v[180:183], a[208:211], a[28:31], v[180:183]\n"
    "s_waitcnt lgkmcnt(0)\n"
    "v_mfma_f32_32x32x16_bf16 v[64:79], a[224:227], a[232:235], v[64:79]\n"
    "ds_write_b128 v15, v[52:55] offset:55360\n"
    "v_mfma_f32_16x16x32_bf16 v[184:187], a[184:187], a[8:11], v[184:187]\n"
    "v_mfma_f32_16x16x32_bf16 v[184:187], a[188:191], a[12:15], v[184:187]\n"
    "v_mfma_f32_16x16x32_bf16 v[184:187], a[192:195], a[16:19], v[184:187]\n"
    "v_cvt_pk_bf16_f32 v64, v64, v65\n"
    "v_cvt_pk_bf16_f32 v65, v66, v67\n"
    "v_mfma_f32_16x16x32_bf16 v[184:187], a[196:199], a[20:23], v[184:187]\n"
    "v_cvt_pk_bf16_f32 v66, v68, v69\n"
    "v_cvt_pk_bf16_f32 v67, v70, v71\n"
    "v_mfma_f32_16x16x32_bf16 v[184:187], a[200:203], a[24:27], v[184:187]\n"
    "v_cvt_pk_bf16_f32 v68, v72, v73\n"
    "v_cvt_pk_bf16_f32 v69, v74, v75\n"
    "v_mfma_f32_16x16x32_bf16 v[184:187], a[204:207], a[28:31], v[184:187]\n"
    "v_cvt_pk_bf16_f32 v70, v76, v77\n"
    "v_cvt_pk_bf16_f32 v71, v78, v79\n"
    "v_mfma_f32_16x16x32_bf16 v[184:187], a[208:211], a[32:35], v[184:187]\n"
    "ds_write_b128 v16, v[84:87] offset:50752\n"
    "v_mfma_f32_16x16x32_bf16 v[188:191], a[184:187], a[12:15], v[188:191]\n"
    "buffer_load_dwordx4 a[164:167], v5, s[28:31], s50 offen offset:1024 sc1\n"
    "v_mfma_f32_16x16x32_bf16 v[188:191], a[188:191], a[16:19], v[188:191]\n"
    "v_mfma_f32_16x16x32_bf16 v[188:191], a[192:195], a[20:23], v[188:191]\n"
    "ds_write_b128 v33, v[64:67] offset:55936\n"
    "v_mfma_f32_16x16x32_bf16 v[188:191], a[196:199], a[24:27], v[188:191]\n"
    "v_mfma_f32_16x16x32_bf16 v[188:191], a[200:203], a[28:31], v[188:191]\n"
    "v_mfma_f32_16x16x32_bf16 v[188:191], a[204:207], a[32:35], v[188:191]\n"
    "ds_write_b128 v33, v[68:71] offset:56512\n"
    "v_mfma_f32_16x16x32_bf16 v[188:191], a[208:211], a[36:39], v[188:191]\n"
    "buffer_load_dwordx4 a[168:171], v5, s[28:31], s50 offen offset:2048 sc1\n"
    "v_mfma_f32_16x16x32_bf16 v[192:195], a[184:187], a[16:19], v[192:195]\n"
    "v_mfma_f32_16x16x32_bf16 v[192:195], a[188:191], a[20:23], v[192:195]\n"
    "v_mfma_f32_16x16x32_bf16 v[192:195], a[192:195], a[24:27], v[192:195]\n"
    "v_mfma_f32_16x16x32_bf16 v[192:195], a[196:199], a[28:31], v[192:195]\n"
    "v_mfma_f32_16x16x32_bf16 v[192:195], a[200:203], a[32:35], v[192:195]\n"
    "v_mfma_f32_16x16x32_bf16 v[192:195], a[204:207], a[36:39], v[192:195]\n"
    "v_mfma_f32_16x16x32_bf16 v[192:195], a[208:211], a[40:43], v[192:195]\n"
    "v_mfma_f32_16x16x32_bf16 v[196:199], a[184:187], a[20:23], v[196:199]\n"
    "buffer_load_dwordx4 a[172:175], v5, s[28:31], s50 offen offset:3072 sc1\n"
    "s_add_u32 s50, s50, 4096\n"
    "v_mfma_f32_16x16x32_bf16 v[196:199], a[188:191], a[24:27], v[196:199]\n"
    "s_waitcnt lgkmcnt(0)\n"
    "s_barrier\n"
    "v_mfma_f32_16x16x32_bf16 v[196:199], a[192:195], a[28:31], v[196:199]\n"
    "v_mfma_f32_16x16x32_bf16 v[196:199], a[196:199], a[32:35], v[196:199]\n"
    "ds_read_b128 a[0:3], v18 offset:50176\n"
    "v_mfma_f32_16x16x32_bf16 v[196:199], a[200:203], a[36:39], v[196:199]\n"
    "v_mfma_f32_16x16x32_bf16 v[196:199], a[204:207], a[40:43], v[196:199]\n"
    "ds_read_b128 a[4:7], v18 offset:50752\n"
    "v_mfma_f32_16x16x32_bf16 v[196:199], a[208:211], a[44:47], v[196:199]\n"
    "v_mfma_f32_16x16x32_bf16 v[200:203], a[184:187], a[24:27], v[200:203]\n"
    "ds_read_b128 a[8:11], v18 offset:51328\n"
    "v_mfma_f32_16x16x32_bf16 v[200:203], a[188:191], a[28:31], v[200:203]\n"
    "v_mfma_f32_16x16x32_bf16 v[200:203], a[192:195], a[32:35], v[200:203]\n"
    "buffer_load_dwordx4 a[176:179], v5, s[28:31], s50 offen offset:0 sc1\n"
    "ds_read_b128 a[12:15], v18 offset:51904\n"
    "v_mfma_f32_16x16x32_bf16 v[200:203], a[196:199], a[36:39], v[200:203]\n"
    "v_mfma_f32_16x16x32_bf16 v[200:203], a[200:203], a[40:43], v[200:203]\n"
    "ds_read_b128 a[16:19], v18 offset:52480\n"
    "v_mfma_f32_16x16x32_bf16 v[200:203], a[204:207], a[44:47], v[200:203]\n"
    "v_mfma_f32_16x16x32_bf16 v[200:203], a[208:211], a[48:51], v[200:203]\n"
    "ds_read_b128 a[20:23], v18 offset:53056\n"
    "v_mfma_f32_16x16x32_bf16 v[204:207], a[184:187], a[28:31], v[204:207]\n"
    "v_mfma_f32_16x16x32_bf16 v[204:207], a[188:191], a[32:35], v[204:207]\n"
    "ds_read_b128 a[24:27], v18 offset:53632\n"
    "v_mfma_f32_16x16x32_bf16 v[204:207], a[192:195], a[36:39], v[204:207]\n"
    "v_mfma_f32_16x16x32_bf16 v[204:207], a[196:199], a[40:43], v[204:207]\n"
    "ds_read_b128 a[28:31], v18 offset:54208\n"
    "v_mfma_f32_16x16x32_bf16 v[204:207], a[200:203], a[44:47], v[204:207]\n"
    "buffer_load_dwordx4 a[180:183], v5, s[28:31], s50 offen offset:1024 sc1\n"
    "v_mfma_f32_16x16x32_bf16 v[204:207], a[204:207], a[48:51], v[204:207]\n"
    "v_mfma_f32_16x16x32_bf16 v[204:207], a[208:211], a[52:55], v[204:207]\n"
    "v_mfma_f32_16x16x32_bf16 v[208:211], a[184:187], a[32:35], v[208:211]\n"
    "v_mfma_f32_16x16x32_bf16 v[208:211], a[188:191], a[36:39], v[208:211]\n"
    "ds_read_b128 a[32:35], v18 offset:54784\n"
    "v_mfma_f32_16x16x32_bf16 v[208:211], a[192:195], a[40:43], v[208:211]\n"
    "v_mfma_f32_16x16x32_bf16 v[208:211], a[196:199], a[44:47], v[208:211]\n"
    "ds_read_b128 a[36:39], v18 offset:55360\n"
    "v_mfma_f32_16x16x32_bf16 v[208:211], a[200:203], a[48:51], v[208:211]\n"
    "v_mfma_f32_16x16x32_bf16 v[208:211], a[204:207], a[52:55], v[208:211]\n"
    "ds_read_b128 a[40:43], v18 offset:55936\n"
    "v_mfma_f32_16x16x32_bf16 v[208:211], a[208:211], a[56:59], v[208:211]\n"
    "s_waitcnt vmcnt(14) lgkmcnt(3)\n"
    "buffer_load_dwordx4 a[184:187], v5, s[28:31], s50 offen offset:2048 sc1\n"
    "v_mfma_f32_16x16x32_bf16 v[216:219], a[212:215], a[64:67], v[216:219]\n"
    "ds_read_b128 a[44:47], v18 offset:56512\n"
    "v_mfma_f32_16x16x32_bf16 v[216:219], a[216:219], a[72:75], v[216:219]\n"
    "buffer_load_dwordx4 a[188:191], v5, s[28:31], s50 offen offset:3072 sc1\n"
    "s_add_u32 s50, s50, 4096\n"
    "v_mfma_f32_16x16x32_bf16 v[220:223], a[212:215], a[68:71], v[220:223]\n"
    "ds_read_b128 a[48:51], v18 offset:57088\n"
    "v_mfma_f32_16x16x32_bf16 v[220:223], a[216:219], a[76:79], v[220:223]\n"
    "buffer_load_dwordx4 a[192:195], v5, s[28:31], s50 offen offset:0 sc1\n"
    "v_mfma_f32_16x16x32_bf16 v[224:227], a[212:215], a[72:75], v[224:227]\n"
    "ds_read_b128 a[52:55], v18 offset:57664\n"
    "v_mfma_f32_16x16x32_bf16 v[224:227], a[216:219], a[80:83], v[224:227]\n"
    "buffer_load_dwordx4 a[196:199], v5, s[28:31], s50 offen offset:1024 sc1\n"
    "v_mfma_f32_16x16x32_bf16 v[228:231], a[212:215], a[76:79], v[228:231]\n"
    "ds_read_b128 a[56:59], v18 offset:58240\n"
    "v_mfma_f32_16x16x32_bf16 v[228:231], a[216:219], a[84:87], v[228:231]\n"
    "buffer_load_dwordx4 a[200:203], v5, s[28:31], s50 offen offset:2048 sc1\n"
    "v_mfma_f32_16x16x32_bf16 v[232:235], a[212:215], a[80:83], v[232:235]\n"
    "v_mfma_f32_16x16x32_bf16 v[232:235], a[216:219], a[88:91], v[232:235]\n"
    "buffer_load_dwordx4 a[204:207], v5, s[28:31], s50 offen offset:3072 sc1\n"
    "s_add_u32 s50, s50, 4096\n"
    "v_mfma_f32_16x16x32_bf16 v[236:239], a[212:215], a[84:87], v[236:239]\n"
    "v_mfma_f32_16x16x32_bf16 v[236:239], a[216:219], a[92:95], v[236:239]\n"
    "buffer_load_dwordx4 a[208:211], v5, s[28:31], s50 offen offset:0 sc1\n"
    "v_mfma_f32_16x16x32_bf16 v[240:243], a[212:215], a[88:91], v[240:243]\n"
    "v_mfma_f32_16x16x32_bf16 v[240:243], a[216:219], a[96:99], v[240:243]\n"
    "v_mfma_f32_16x16x32_bf16 v[244:247], a[212:215], a[92:95], v[244:247]\n"
    "v_mfma_f32_16x16x32_bf16 v[244:247], a[216:219], a[100:103], v[244:247]\n"
    "v_mfma_f32_16x16x32_bf16 v[248:251], a[212:215], a[96:99], v[248:251]\n"
    "v_mfma_f32_16x16x32_bf16 v[248:251], a[216:219], a[104:107], v[248:251]\n"
    "s_waitcnt vmcnt(14) lgkmcnt(0)\n"
    "s_cmp_eq_u32 s44, 1\n"
    "s_cbranch_scc1 13f\n"
    "v_mfma_f32_16x16x32_bf16 v[100:103], a[128:131], a[4:7], v[100:103]\n"
    "buffer_load_dwordx4 a[212:215], v5, s[28:31], s50 offen offset:1024 sc1\n"
    "ds_read_b128 a[60:63], v18 offset:50832\n"
    "v_mfma_f32_16x16x32_bf16 v[100:103], a[132:135], a[8:11], v[100:103]\n"
    "v_add_u32 v46, s48, v5\n"
    "v_add_u32 v47, s49, v5\n"
    "v_mfma_f32_16x16x32_bf16 v[100:103], a[136:139], a[12:15], v[100:103]\n"
    "s_mov_b32 s76, 0x3f360b61\n"
    "buffer_load_dwordx4 a[216:219], v5, s[28:31], s50 offen offset:2048 sc1\n"
    "s_mov_b32 s78, 0x3eb60b61\n"
    "v_mfma_f32_16x16x32_bf16 v[100:103], a[140:143], a[16:19], v[100:103]\n"
    "s_mov_b32 s80, 0x3e360b61\n"
    "s_mov_b32 s82, 0x3db60b61\n"
    "s_mov_b32 s84, 0x41000000\n"
    "v_mfma_f32_16x16x32_bf16 v[100:103], a[144:147], a[20:23], v[100:103]\n"
    "s_lshl_b32 s72, s43, 5\n"
    "v_xor_b32 v40, s72, v40\n"
    "s_lshl_b32 s72, s42, 10\n"
    "v_mfma_f32_16x16x32_bf16 v[100:103], a[148:151], a[24:27], v[100:103]\n"
    "v_add_u32 v41, s72, v41\n"
    "ds_read_b128 a[64:67], v18 offset:51408\n"
    "v_mfma_f32_16x16x32_bf16 v[100:103], a[152:155], a[28:31], v[100:103]\n"
    "ds_read_b128 a[68:71], v18 offset:51984\n"
    "v_mfma_f32_16x16x32_bf16 v[96:99], a[128:131], a[0:3], v[96:99]\n"
    "ds_read_b128 a[72:75], v18 offset:52560\n"
    "v_mfma_f32_16x16x32_bf16 v[96:99], a[132:135], a[4:7], v[96:99]\n"
    "ds_read_b128 a[76:79], v18 offset:53136\n"
    "v_mfma_f32_16x16x32_bf16 v[96:99], a[136:139], a[8:11], v[96:99]\n"
    "ds_read_b128 a[80:83], v18 offset:53712\n"
    "v_mfma_f32_16x16x32_bf16 v[96:99], a[140:143], a[12:15], v[96:99]\n"
    "ds_read_b128 a[84:87], v18 offset:54288\n"
    "v_mfma_f32_16x16x32_bf16 v[96:99], a[144:147], a[16:19], v[96:99]\n"
    "ds_read_b128 a[88:91], v18 offset:50256\n"
    "v_mfma_f32_16x16x32_bf16 v[96:99], a[148:151], a[20:23], v[96:99]\n"
    "ds_read_b128 a[92:95], v18 offset:54864\n"
    "v_mfma_f32_16x16x32_bf16 v[96:99], a[152:155], a[24:27], v[96:99]\n"
    "ds_read_b128 a[96:99], v19 offset:50752\n"
    "v_mfma_f32_16x16x32_bf16 v[104:107], a[128:131], a[8:11], v[104:107]\n"
    "ds_read_b128 a[100:103], v19 offset:51904\n"
    "v_mfma_f32_16x16x32_bf16 v[104:107], a[132:135], a[12:15], v[104:107]\n"
    "ds_read_b128 a[104:107], v18 offset:50912\n"
    "v_mfma_f32_16x16x32_bf16 v[104:107], a[136:139], a[16:19], v[104:107]\n"
    "ds_read_b128 a[108:111], v18 offset:51488\n"
    "v_mfma_f32_16x16x32_bf16 v[104:107], a[140:143], a[20:23], v[104:107]\n"
    "ds_read_b128 a[112:115], v18 offset:52064\n"
    "v_mfma_f32_16x16x32_bf16 v[104:107], a[144:147], a[24:27], v[104:107]\n"
    "ds_read_b128 a[116:119], v18 offset:52640\n"
    "v_mfma_f32_16x16x32_bf16 v[104:107], a[148:151], a[28:31], v[104:107]\n"
    "ds_read_b128 a[120:123], v18 offset:53216\n"
    "v_mfma_f32_16x16x32_bf16 v[104:107], a[152:155], a[32:35], v[104:107]\n"
    "ds_read_b128 a[124:127], v18 offset:53792\n"
    "v_mfma_f32_16x16x32_bf16 v[108:111], a[128:131], a[12:15], v[108:111]\n"
    "ds_read_b128 a[220:223], v18 offset:54368\n"
    "v_mfma_f32_16x16x32_bf16 v[108:111], a[132:135], a[16:19], v[108:111]\n"
    "ds_read_b128 a[224:227], v19 offset:50176\n"
    "v_mfma_f32_16x16x32_bf16 v[108:111], a[136:139], a[20:23], v[108:111]\n"
    "ds_read_b128 a[228:231], v19 offset:51328\n"
    "v_mfma_f32_16x16x32_bf16 v[108:111], a[140:143], a[24:27], v[108:111]\n"
    "ds_read_b128 a[232:235], v18 offset:50336\n"
    "v_mfma_f32_16x16x32_bf16 v[108:111], a[144:147], a[28:31], v[108:111]\n"
    "ds_read_b128 a[236:239], v19 offset:52480\n"
    "v_mfma_f32_16x16x32_bf16 v[108:111], a[148:151], a[32:35], v[108:111]\n"
    "ds_read_b128 a[240:243], v18 offset:54944\n"
    "v_mfma_f32_16x16x32_bf16 v[108:111], a[152:155], a[36:39], v[108:111]\n"
    "s_waitcnt vmcnt(9) lgkmcnt(9)\n"
    "ds_read_b128 a[244:247], v18 offset:55440\n"
    "v_mfma_f32_16x16x32_bf16 v[140:143], a[156:159], a[60:63], v[140:143]\n"
    "ds_read_b128 a[248:251], v19 offset:53056\n"
    "v_mfma_f32_16x16x32_bf16 v[140:143], a[160:163], a[64:67], v[140:143]\n"
    "ds_read_b128 a[252:255], v18 offset:55520\n"
    "v_mfma_f32_16x16x32_bf16 v[140:143], a[164:167], a[68:71], v[140:143]\n"
    "v_mfma_f32_16x16x32_bf16 v[140:143], a[168:171], a[72:75], v[140:143]\n"
    "v_mfma_f32_16x16x32_bf16 v[140:143], a[172:175], a[76:79], v[140:143]\n"
    "v_mfma_f32_16x16x32_bf16 v[140:143], a[176:179], a[80:83], v[140:143]\n"
    "v_mfma_f32_16x16x32_bf16 v[140:143], a[180:183], a[84:87], v[140:143]\n"
    "v_mfma_f32_16x16x32_bf16 v[136:139], a[156:159], a[88:91], v[136:139]\n"
    "v_mfma_f32_16x16x32_bf16 v[136:139], a[160:163], a[60:63], v[136:139]\n"
    "v_mfma_f32_16x16x32_bf16 v[136:139], a[164:167], a[64:67], v[136:139]\n"
    "v_mul_f32 v100, s76, v100\n"
    "v_mul_f32 v101, s76, v101\n"
    "v_mfma_f32_16x16x32_bf16 v[136:139], a[168:171], a[68:71], v[136:139]\n"
    "v_mul_f32 v102, s76, v102\n"
    "v_mul_f32 v103, s76, v103\n"
    "v_mfma_f32_16x16x32_bf16 v[136:139], a[172:175], a[72:75], v[136:139]\n"
    "v_fma_f32 v140, -v140, s76, v100\n"
    "v_fma_f32 v141, -v141, s76, v101\n"
    "v_mfma_f32_16x16x32_bf16 v[136:139], a[176:179], a[76:79], v[136:139]\n"
    "v_fma_f32 v142, -v142, s76, v102\n"
    "v_fma_f32 v143, -v143, s76, v103\n"
    "v_mfma_f32_16x16x32_bf16 v[136:139], a[180:183], a[80:83], v[136:139]\n"
    "v_fma_f32 v100, v100, 2.0, -v140\n"
    "v_fma_f32 v101, v101, 2.0, -v141\n"
    "v_mfma_f32_16x16x32_bf16 v[144:147], a[156:159], a[64:67], v[144:147]\n"
    "v_fma_f32 v102, v102, 2.0, -v142\n"
    "v_fma_f32 v103, v103, 2.0, -v143\n"
    "v_mfma_f32_16x16x32_bf16 v[144:147], a[160:163], a[68:71], v[144:147]\n"
    "v_mfma_f32_16x16x32_bf16 v[144:147], a[164:167], a[72:75], v[144:147]\n"
    "ds_write_b128 v46, v[140:143] offset:1024\n"
    "v_mfma_f32_16x16x32_bf16 v[144:147], a[168:171], a[76:79], v[144:147]\n"
    "v_mul_f32 v96, s76, v96\n"
    "v_mul_f32 v97, s76, v97\n"
    "v_mfma_f32_16x16x32_bf16 v[144:147], a[172:175], a[80:83], v[144:147]\n"
    "v_mul_f32 v98, s76, v98\n"
    "v_mul_f32 v99, s76, v99\n"
    "v_mfma_f32_16x16x32_bf16 v[144:147], a[176:179], a[84:87], v[144:147]\n"
    "ds_write_b128 v46, v[100:103] offset:0\n"
    "v_mfma_f32_16x16x32_bf16 v[144:147], a[180:183], a[92:95], v[144:147]\n"
    "s_waitcnt vmcnt(0) lgkmcnt(2)\n"
    "v_fma_f32 v136, -v136, s76, v96\n"
    "v_fma_f32 v137, -v137, s76, v97\n"
    "v_mfma_f32_16x16x32_bf16 v[220:223], a[212:215], a[96:99], v[220:223]\n"
    "v_fma_f32 v138, -v138, s76, v98\n"
    "v_fma_f32 v139, -v139, s76, v99\n"
    "v_mfma_f32_16x16x32_bf16 v[220:223], a[216:219], a[100:103], v[220:223]\n"
    "v_fma_f32 v96, v96, 2.0, -v136\n"
    "v_fma_f32 v97, v97, 2.0, -v137\n"
    "v_mfma_f32_16x16x32_bf16 v[180:183], a[184:187], a[104:107], v[180:183]\n"
    "v_fma_f32 v98, v98, 2.0, -v138\n"
    "v_fma_f32 v99, v99, 2.0, -v139\n"
    "v_mfma_f32_16x16x32_bf16 v[180:183], a[188:191], a[108:111], v[180:183]\n"
    "v_mul_f32 v104, s76, v104\n"
    "v_mul_f32 v105, s76, v105\n"
    "v_mfma_f32_16x16x32_bf16 v[180:183], a[192:195], a[112:115], v[180:183]\n"
    "v_fma_f32 v220, v220, s76, v140\n"
    "v_fma_f32 v221, v221, s76, v141\n"
    "v_mfma_f32_16x16x32_bf16 v[180:183], a[196:199], a[116:119], v[180:183]\n"
    "v_fma_f32 v222, v222, s76, v142\n"
    "v_fma_f32 v223, v223, s76, v143\n"
    "v_mfma_f32_16x16x32_bf16 v[180:183], a[200:203], a[120:123], v[180:183]\n"
    "v_mul_f32 v106, s76, v106\n"
    "v_mul_f32 v107, s76, v107\n"
    "v_mfma_f32_16x16x32_bf16 v[180:183], a[204:207], a[124:127], v[180:183]\n"
    "ds_write_b128 v46, v[220:223] offset:3072\n"
    "v_mfma_f32_16x16x32_bf16 v[180:183], a[208:211], a[220:223], v[180:183]\n"
    "v_fma_f32 v144, -v144, s76, v104\n"
    "v_fma_f32 v145, -v145, s76, v105\n"
    "v_mfma_f32_16x16x32_bf16 v[216:219], a[212:215], a[224:227], v[216:219]\n"
    "v_fma_f32 v146, -v146, s76, v106\n"
    "v_fma_f32 v147, -v147, s76, v107\n"
    "v_mfma_f32_16x16x32_bf16 v[216:219], a[216:219], a[228:231], v[216:219]\n"
    "v_fma_f32 v104, v104, 2.0, -v144\n"
    "v_fma_f32 v105, v105, 2.0, -v145\n"
    "v_mfma_f32_16x16x32_bf16 v[176:179], a[184:187], a[232:235], v[176:179]\n"
    "v_fma_f32 v106, v106, 2.0, -v146\n"
    "v_fma_f32 v107, v107, 2.0, -v147\n"
    "v_mfma_f32_16x16x32_bf16 v[176:179], a[188:191], a[104:107], v[176:179]\n"
    "v_fma_f32 v180, v180, s76, v100\n"
    "v_fma_f32 v181, v181, s76, v101\n"
    "v_mfma_f32_16x16x32_bf16 v[176:179], a[192:195], a[108:111], v[176:179]\n"
    "v_fma_f32 v182, v182, s76, v102\n"
    "v_fma_f32 v183, v183, s76, v103\n"
    "v_mfma_f32_16x16x32_bf16 v[176:179], a[196:199], a[112:115], v[176:179]\n"
    "v_fma_f32 v216, v216, s76, v136\n"
    "v_fma_f32 v217, v217, s76, v137\n"
    "v_mfma_f32_16x16x32_bf16 v[176:179], a[200:203], a[116:119], v[176:179]\n"
    "ds_write_b128 v46, v[180:183] offset:2048\n"
    "v_mfma_f32_16x16x32_bf16 v[176:179], a[204:207], a[120:123], v[176:179]\n"
    "v_fma_f32 v218, v218, s76, v138\n"
    "v_fma_f32 v219, v219, s76, v139\n"
    "v_mfma_f32_16x16x32_bf16 v[176:179], a[208:211], a[124:127], v[176:179]\n"
    "v_mfma_f32_16x16x32_bf16 v[224:227], a[212:215], a[228:231], v[224:227]\n"
    "v_mfma_f32_16x16x32_bf16 v[224:227], a[216:219], a[236:239], v[224:227]\n"
    "v_mfma_f32_16x16x32_bf16 v[184:187], a[184:187], a[108:111], v[184:187]\n"
    "v_mfma_f32_16x16x32_bf16 v[184:187], a[188:191], a[112:115], v[184:187]\n"
    "v_mfma_f32_16x16x32_bf16 v[184:187], a[192:195], a[116:119], v[184:187]\n"
    "v_fma_f32 v224, v224, s76, v144\n"
    "v_fma_f32 v225, v225, s76, v145\n"
    "v_mfma_f32_16x16x32_bf16 v[184:187], a[196:199], a[120:123], v[184:187]\n"
    "v_fma_f32 v226, v226, s76, v146\n"
    "v_fma_f32 v227, v227, s76, v147\n"
    "v_mfma_f32_16x16x32_bf16 v[184:187], a[200:203], a[124:127], v[184:187]\n"
    "v_mfma_f32_16x16x32_bf16 v[184:187], a[204:207], a[220:223], v[184:187]\n"
    "v_mfma_f32_16x16x32_bf16 v[184:187], a[208:211], a[240:243], v[184:187]\n"
    "s_waitcnt lgkmcnt(0)\n"
    "s_barrier\n"
    "v_mfma_f32_16x16x32_bf16 v[148:151], a[156:159], a[68:71], v[148:151]\n"
    "ds_read_b128 v[100:103], v47 offset:0\n"
    "ds_read_b128 v[140:143], v47 offset:1024\n"
    "v_mfma_f32_16x16x32_bf16 v[148:151], a[160:163], a[72:75], v[148:151]\n"
    "ds_read_b128 v[180:183], v47 offset:2048\n"
    "ds_read_b128 v[220:223], v47 offset:3072\n"
    "v_mfma_f32_16x16x32_bf16 v[148:151], a[164:167], a[76:79], v[148:151]\n"
    "ds_read_b128 v[132:135], v47 offset:4096\n"
    "ds_read_b128 v[172:175], v47 offset:5120\n"
    "v_mfma_f32_16x16x32_bf16 v[148:151], a[168:171], a[80:83], v[148:151]\n"
    "ds_read_b128 v[212:215], v47 offset:6144\n"
    "ds_read_b128 v[252:255], v47 offset:7168\n"
    "v_mfma_f32_16x16x32_bf16 v[148:151], a[172:175], a[84:87], v[148:151]\n"
    "ds_read_b128 a[0:3], v18 offset:56016\n"
    "v_mfma_f32_16x16x32_bf16 v[148:151], a[176:179], a[92:95], v[148:151]\n"
    "ds_read_b128 a[4:7], v19 offset:53632\n"
    "v_mfma_f32_16x16x32_bf16 v[148:151], a[180:183], a[244:247], v[148:151]\n"
    "ds_read_b128 a[8:11], v18 offset:56096\n"
    "v_mfma_f32_16x16x32_bf16 v[228:231], a[212:215], a[100:103], v[228:231]\n"
    "s_waitcnt lgkmcnt(7)\n"
    "ds_read_b128 a[12:15], v18 offset:56592\n"
    "v_pk_fma_f32 v[216:217], v[220:221], s[84:85], v[216:217] op_sel_hi:[1,0,1]\n"
    "v_pk_fma_f32 v[218:219], v[222:223], s[84:85], v[218:219] op_sel_hi:[1,0,1]\n"
    "v_pk_fma_f32 v[136:137], v[140:141], 2.0, v[136:137] op_sel_hi:[1,0,1]\n"
    "v_pk_fma_f32 v[138:139], v[142:143], 2.0, v[138:139] op_sel_hi:[1,0,1]\n"
    "v_pk_fma_f32 v[140:141], v[100:101], 4.0, v[96:97] op_sel_hi:[1,0,1]\n"
    "v_pk_fma_f32 v[142:143], v[102:103], 4.0, v[98:99] op_sel_hi:[1,0,1]\n"
    "v_pk_add_f32 v[96:97], v[96:97], v[100:101]\n"
    "v_pk_add_f32 v[98:99], v[98:99], v[102:103]\n"
    "v_pk_fma_f32 v[216:217], v[180:181], s[82:83], v[216:217] op_sel_hi:[1,0,1]\n"
    "v_pk_fma_f32 v[218:219], v[182:183], s[82:83], v[218:219] op_sel_hi:[1,0,1]\n"
    "v_pk_fma_f32 v[136:137], v[180:181], s[78:79], v[136:137] op_sel_hi:[1,0,1]\n"
    "v_pk_fma_f32 v[138:139], v[182:183], s[78:79], v[138:139] op_sel_hi:[1,0,1]\n"
    "v_pk_fma_f32 v[140:141], v[180:181], s[80:81], v[140:141] op_sel_hi:[1,0,1]\n"
    "v_pk_fma_f32 v[142:143], v[182:183], s[80:81], v[142:143] op_sel_hi:[1,0,1]\n"
    "v_pk_fma_f32 v[96:97], v[180:181], s[76:77], v[96:97] op_sel_hi:[1,0,1]\n"
    "v_pk_fma_f32 v[98:99], v[182:183], s[76:77], v[98:99] op_sel_hi:[1,0,1]\n"
    "v_fmac_f32 v96, s76, v176\n"
    "v_mfma_f32_16x16x32_bf16 v[228:231], a[216:219], a[248:251], v[228:231]\n"
    "ds_read_b128 a[88:91], v19 offset:54208\n"
    "v_fmac_f32 v97, s76, v177\n"
    "v_mfma_f32_16x16x32_bf16 v[188:191], a[184:187], a[112:115], v[188:191]\n"
    "s_waitcnt lgkmcnt(5)\n"
    "v_pk_mul_f32 v[108:109], v[108:109], s[76:77] op_sel_hi:[1,0]\n"
    "v_pk_mul_f32 v[110:111], v[110:111], s[76:77] op_sel_hi:[1,0]\n"
    "v_pk_fma_f32 v[148:149], v[148:149], s[76:77], v[108:109] op_sel_hi:[1,0,1] neg_lo:[1,0,0] neg_hi:[1,0,0]\n"
    "v_pk_fma_f32 v[150:151], v[150:151], s[76:77], v[110:111] op_sel_hi:[1,0,1] neg_lo:[1,0,0] neg_hi:[1,0,0]\n"
    "v_pk_fma_f32 v[108:109], v[108:109], 2.0, v[148:149] op_sel_hi:[1,0,1] neg_lo:[0,0,1] neg_hi:[0,0,1]\n"
    "v_pk_fma_f32 v[110:111], v[110:111], 2.0, v[150:151] op_sel_hi:[1,0,1] neg_lo:[0,0,1] neg_hi:[0,0,1]\n"
    "v_fmac_f32 v98, s76, v178\n"
    "v_fmac_f32 v99, s76, v179\n"
    "v_pk_fma_f32 v[224:225], v[252:253], s[84:85], v[224:225] op_sel_hi:[1,0,1]\n"
    "v_pk_fma_f32 v[226:227], v[254:255], s[84:85], v[226:227] op_sel_hi:[1,0,1]\n"
    "v_pk_fma_f32 v[144:145], v[172:173], 2.0, v[144:145] op_sel_hi:[1,0,1]\n"
    "v_pk_fma_f32 v[146:147], v[174:175], 2.0, v[146:147] op_sel_hi:[1,0,1]\n"
    "v_pk_fma_f32 v[172:173], v[132:133], 4.0, v[104:105] op_sel_hi:[1,0,1]\n"
    "v_pk_fma_f32 v[174:175], v[134:135], 4.0, v[106:107] op_sel_hi:[1,0,1]\n"
    "v_pk_add_f32 v[104:105], v[104:105], v[132:133]\n"
    "v_pk_add_f32 v[106:107], v[106:107], v[134:135]\n"
    "v_pk_fma_f32 v[224:225], v[212:213], s[82:83], v[224:225] op_sel_hi:[1,0,1]\n"
    "v_pk_fma_f32 v[226:227], v[214:215], s[82:83], v[226:227] op_sel_hi:[1,0,1]\n"
    "v_pk_fma_f32 v[144:145], v[212:213], s[78:79], v[144:145] op_sel_hi:[1,0,1]\n"
    "v_pk_fma_f32 v[146:147], v[214:215], s[78:79], v[146:147] op_sel_hi:[1,0,1]\n"
    "v_pk_fma_f32 v[172:173], v[212:213], s[80:81], v[172:173] op_sel_hi:[1,0,1]\n"
    "v_pk_fma_f32 v[174:175], v[214:215], s[80:81], v[174:175] op_sel_hi:[1,0,1]\n"
    "v_pk_fma_f32 v[104:105], v[212:213], s[76:77], v[104:105] op_sel_hi:[1,0,1]\n"
    "v_pk_fma_f32 v[106:107], v[214:215], s[76:77], v[106:107] op_sel_hi:[1,0,1]\n"
    "v_pk_fma_f32 v[104:105], v[184:185], s[76:77], v[104:105] op_sel_hi:[1,0,1]\n"
    "v_pk_fma_f32 v[106:107], v[186:187], s[76:77], v[106:107] op_sel_hi:[1,0,1]\n"
    "v_mfma_f32_16x16x32_bf16 v[188:191], a[188:191], a[116:119], v[188:191]\n"
    "v_cvt_pk_bf16_f32 v136, v136, v137\n"
    "v_cvt_pk_bf16_f32 v137, v138, v139\n"
    "v_mfma_f32_16x16x32_bf16 v[188:191], a[192:195], a[120:123], v[188:191]\n"
    "ds_write_b128 v46, v[148:151] offset:5120\n"
    "v_mfma_f32_16x16x32_bf16 v[188:191], a[196:199], a[124:127], v[188:191]\n"
    "v_fma_f32 v228, v228, s76, v148\n"
    "v_fma_f32 v229, v229, s76, v149\n"
    "v_mfma_f32_16x16x32_bf16 v[188:191], a[200:203], a[220:223], v[188:191]\n"
    "v_fma_f32 v230, v230, s76, v150\n"
    "v_fma_f32 v231, v231, s76, v151\n"
    "v_mfma_f32_16x16x32_bf16 v[188:191], a[204:207], a[240:243], v[188:191]\n"
    "ds_write_b128 v46, v[108:111] offset:4096\n"
    "v_mfma_f32_16x16x32_bf16 v[188:191], a[208:211], a[252:255], v[188:191]\n"
    "v_cvt_pk_bf16_f32 v140, v140, v141\n"
    "v_cvt_pk_bf16_f32 v141, v142, v143\n"
    "v_mfma_f32_16x16x32_bf16 v[112:115], a[128:131], a[16:19], v[112:115]\n"
    "v_cvt_pk_bf16_f32 v216, v216, v217\n"
    "v_cvt_pk_bf16_f32 v217, v218, v219\n"
    "v_mfma_f32_16x16x32_bf16 v[112:115], a[132:135], a[20:23], v[112:115]\n"
    "ds_write_b128 v46, v[228:231] offset:7168\n"
    "v_mfma_f32_16x16x32_bf16 v[112:115], a[136:139], a[24:27], v[112:115]\n"
    "v_cvt_pk_bf16_f32 v96, v96, v97\n"
    "v_cvt_pk_bf16_f32 v97, v98, v99\n"
    "v_mfma_f32_16x16x32_bf16 v[112:115], a[140:143], a[28:31], v[112:115]\n"
    "v_cvt_pk_bf16_f32 v144, v144, v145\n"
    "v_cvt_pk_bf16_f32 v145, v146, v147\n"
    "v_mfma_f32_16x16x32_bf16 v[112:115], a[144:147], a[32:35], v[112:115]\n"
    "ds_write_b64 v40, v[136:137] offset:1024\n"
    "v_mfma_f32_16x16x32_bf16 v[112:115], a[148:151], a[36:39], v[112:115]\n"
    "ds_write_b64 v40, v[144:145] offset:9216\n"
    "v_mfma_f32_16x16x32_bf16 v[112:115], a[152:155], a[40:43], v[112:115]\n"
    "ds_write_b64 v40, v[140:141] offset:2048\n"
    "v_mfma_f32_16x16x32_bf16 v[152:155], a[156:159], a[72:75], v[152:155]\n"
    "v_cvt_pk_bf16_f32 v172, v172, v173\n"
    "v_cvt_pk_bf16_f32 v173, v174, v175\n"
    "v_mfma_f32_16x16x32_bf16 v[152:155], a[160:163], a[76:79], v[152:155]\n"
    "ds_write_b64 v40, v[216:217] offset:3072\n"
    "v_mfma_f32_16x16x32_bf16 v[152:155], a[164:167], a[80:83], v[152:155]\n"
    "ds_write_b64 v40, v[172:173] offset:10240\n"
    "v_mfma_f32_16x16x32_bf16 v[152:155], a[168:171], a[84:87], v[152:155]\n"
    "ds_write_b64 v40, v[96:97] offset:0\n"
    "v_mfma_f32_16x16x32_bf16 v[152:155], a[172:175], a[92:95], v[152:155]\n"
    "v_cvt_pk_bf16_f32 v224, v224, v225\n"
    "v_cvt_pk_bf16_f32 v225, v226, v227\n"
    "v_mfma_f32_16x16x32_bf16 v[152:155], a[176:179], a[244:247], v[152:155]\n"
    "s_waitcnt lgkmcnt(6)\n"
    "v_cvt_pk_bf16_f32 v104, v104, v105\n"
    "v_cvt_pk_bf16_f32 v105, v106, v107\n"
    "v_mfma_f32_16x16x32_bf16 v[152:155], a[180:183], a[0:3], v[152:155]\n"
    "ds_write_b64 v40, v[224:225] offset:11264\n"
    "v_mfma_f32_16x16x32_bf16 v[232:235], a[212:215], a[236:239], v[232:235]\n"
    "v_fma_f32 v188, v188, s76, v108\n"
    "v_fma_f32 v189, v189, s76, v109\n"
    "v_mfma_f32_16x16x32_bf16 v[232:235], a[216:219], a[4:7], v[232:235]\n"
    "ds_write_b64 v40, v[104:105] offset:8192\n"
    "v_mfma_f32_16x16x32_bf16 v[192:195], a[184:187], a[116:119], v[192:195]\n"
    "v_fma_f32 v190, v190, s76, v110\n"
    "v_fma_f32 v191, v191, s76, v111\n"
    "v_mfma_f32_16x16x32_bf16 v[192:195], a[188:191], a[120:123], v[192:195]\n"
    "v_mul_f32 v112, s76, v112\n"
    "v_mul_f32 v113, s76, v113\n"
    "v_mfma_f32_16x16x32_bf16 v[192:195], a[192:195], a[124:127], v[192:195]\n"
    "ds_write_b128 v46, v[188:191] offset:6144\n"
    "v_mfma_f32_16x16x32_bf16 v[192:195], a[196:199], a[220:223], v[192:195]\n"
    "v_mul_f32 v114, s76, v114\n"
    "v_mul_f32 v115, s76, v115\n"
    "v_mfma_f32_16x16x32_bf16 v[192:195], a[200:203], a[240:243], v[192:195]\n"
    "v_fma_f32 v152, -v152, s76, v112\n"
    "v_fma_f32 v153, -v153, s76, v113\n"
    "v_mfma_f32_16x16x32_bf16 v[192:195], a[204:207], a[252:255], v[192:195]\n"
    "v_fma_f32 v154, -v154, s76, v114\n"
    "v_fma_f32 v155, -v155, s76, v115\n"
    "v_mfma_f32_16x16x32_bf16 v[192:195], a[208:211], a[8:11], v[192:195]\n"
    "s_waitcnt lgkmcnt(0)\n"
    "s_barrier\n"
    "v_mfma_f32_16x16x32_bf16 v[116:119], a[128:131], a[20:23], v[116:119]\n"
    "ds_read_b128 v[108:111], v47 offset:8192\n"
    "ds_read_b128 v[148:151], v47 offset:9216\n"
    "v_mfma_f32_16x16x32_bf16 v[116:119], a[132:135], a[24:27], v[116:119]\n"
    "ds_read_b128 v[188:191], v47 offset:10240\n"
    "ds_read_b128 v[228:231], v47 offset:11264\n"
    "v_mfma_f32_16x16x32_bf16 v[116:119], a[136:139], a[28:31], v[116:119]\n"
    "ds_read_b128 v[48:51], v41 offset:0\n"
    "ds_read_b128 v[52:55], v41 offset:8192\n"
    "v_mfma_f32_16x16x32_bf16 v[116:119], a[140:143], a[32:35], v[116:119]\n"
    "ds_read_b128 v[56:59], v41 offset:4096\n"
    "v_fma_f32 v112, v112, 2.0, -v152\n"
    "v_mfma_f32_16x16x32_bf16 v[116:119], a[144:147], a[36:39], v[116:119]\n"
    "ds_read_b128 a[60:63], v18 offset:56672\n"
    "v_fma_f32 v113, v113, 2.0, -v153\n"
    "v_mfma_f32_16x16x32_bf16 v[116:119], a[148:151], a[40:43], v[116:119]\n"
    "ds_read_b128 a[64:67], v18 offset:57168\n"
    "v_fma_f32 v114, v114, 2.0, -v154\n"
    "v_mfma_f32_16x16x32_bf16 v[116:119], a[152:155], a[44:47], v[116:119]\n"
    "ds_read_b128 a[96:99], v19 offset:54784\n"
    "v_fma_f32 v115, v115, 2.0, -v155\n"
    "v_mfma_f32_16x16x32_bf16 v[156:159], a[156:159], a[76:79], v[156:159]\n"
    "ds_read_b128 a[224:227], v18 offset:57248\n"
    "v_fma_f32 v232, v232, s76, v152\n"
    "v_mfma_f32_16x16x32_bf16 v[156:159], a[160:163], a[80:83], v[156:159]\n"
    "s_waitcnt lgkmcnt(6)\n"
    "s_add_u32 s58, s53, 0\n"
    "buffer_store_dwordx4 v[48:51], v42, s[24:27], s58 offen sc0 sc1\n"
    "v_fma_f32 v233, v233, s76, v153\n"
    "v_mfma_f32_16x16x32_bf16 v[156:159], a[164:167], a[84:87], v[156:159]\n"
    "v_fma_f32 v234, v234, s76, v154\n"
    "v_fma_f32 v235, v235, s76, v155\n"
    "v_pk_fma_f32 v[232:233], v[228:229], s[84:85], v[232:233] op_sel_hi:[1,0,1]\n"
    "v_pk_fma_f32 v[234:235], v[230:231], s[84:85], v[234:235] op_sel_hi:[1,0,1]\n"
    "v_pk_fma_f32 v[152:153], v[148:149], 2.0, v[152:153] op_sel_hi:[1,0,1]\n"
    "v_pk_fma_f32 v[154:155], v[150:151], 2.0, v[154:155] op_sel_hi:[1,0,1]\n"
    "v_pk_fma_f32 v[148:149], v[108:109], 4.0, v[112:113] op_sel_hi:[1,0,1]\n"
    "v_pk_fma_f32 v[150:151], v[110:111], 4.0, v[114:115] op_sel_hi:[1,0,1]\n"
    "v_pk_add_f32 v[112:113], v[112:113], v[108:109]\n"
    "v_pk_add_f32 v[114:115], v[114:115], v[110:111]\n"
    "v_pk_fma_f32 v[232:233], v[188:189], s[82:83], v[232:233] op_sel_hi:[1,0,1]\n"
    "v_pk_fma_f32 v[234:235], v[190:191], s[82:83], v[234:235] op_sel_hi:[1,0,1]\n"
    "v_pk_fma_f32 v[152:153], v[188:189], s[78:79], v[152:153] op_sel_hi:[1,0,1]\n"
    "v_pk_fma_f32 v[154:155], v[190:191], s[78:79], v[154:155] op_sel_hi:[1,0,1]\n"
    "v_pk_fma_f32 v[148:149], v[188:189], s[80:81], v[148:149] op_sel_hi:[1,0,1]\n"
    "v_pk_fma_f32 v[150:151], v[190:191], s[80:81], v[150:151] op_sel_hi:[1,0,1]\n"
    "v_pk_fma_f32 v[112:113], v[188:189], s[76:77], v[112:113] op_sel_hi:[1,0,1]\n"
    "v_pk_fma_f32 v[114:115], v[190:191], s[76:77], v[114:115] op_sel_hi:[1,0,1]\n"
    "v_pk_fma_f32 v[112:113], v[192:193], s[76:77], v[112:113] op_sel_hi:[1,0,1]\n"
    "v_pk_fma_f32 v[114:115], v[194:195], s[76:77], v[114:115] op_sel_hi:[1,0,1]\n"
    "v_mfma_f32_16x16x32_bf16 v[156:159], a[168:171], a[92:95], v[156:159]\n"
    "s_waitcnt lgkmcnt(5)\n"
    "s_add_u32 s58, s53, 172032\n"
    "buffer_store_dwordx4 v[52:55], v42, s[24:27], s58 offen sc0 sc1\n"
    "v_cvt_pk_bf16_f32 v152, v152, v153\n"
    "v_mfma_f32_16x16x32_bf16 v[156:159], a[172:175], a[244:247], v[156:159]\n"
    "v_cvt_pk_bf16_f32 v153, v154, v155\n"
    "v_cvt_pk_bf16_f32 v148, v148, v149\n"
    "v_mfma_f32_16x16x32_bf16 v[156:159], a[176:179], a[0:3], v[156:159]\n"
    "s_waitcnt lgkmcnt(4)\n"
    "s_add_u32 s58, s53, 86016\n"
    "buffer_store_dwordx4 v[56:59], v42, s[24:27], s58 offen sc0 sc1\n"
    "v_cvt_pk_bf16_f32 v149, v150, v151\n"
    "v_mfma_f32_16x16x32_bf16 v[156:159], a[180:183], a[12:15], v[156:159]\n"
    "ds_write_b64 v40, v[152:153] offset:17408\n"
    "v_mfma_f32_16x16x32_bf16 v[236:239], a[212:215], a[248:251], v[236:239]\n"
    "v_cvt_pk_bf16_f32 v232, v232, v233\n"
    "v_cvt_pk_bf16_f32 v233, v234, v235\n"
    "v_mfma_f32_16x16x32_bf16 v[236:239], a[216:219], a[88:91], v[236:239]\n"
    "ds_write_b64 v40, v[148:149] offset:18432\n"
    "v_mfma_f32_16x16x32_bf16 v[196:199], a[184:187], a[120:123], v[196:199]\n"
    "v_mul_f32 v116, s76, v116\n"
    "v_mul_f32 v117, s76, v117\n"
    "v_mfma_f32_16x16x32_bf16 v[196:199], a[188:191], a[124:127], v[196:199]\n"
    "v_mul_f32 v118, s76, v118\n"
    "v_mul_f32 v119, s76, v119\n"
    "v_mfma_f32_16x16x32_bf16 v[196:199], a[192:195], a[220:223], v[196:199]\n"
    "v_fma_f32 v156, -v156, s76, v116\n"
    "v_fma_f32 v157, -v157, s76, v117\n"
    "v_mfma_f32_16x16x32_bf16 v[196:199], a[196:199], a[240:243], v[196:199]\n"
    "v_fma_f32 v158, -v158, s76, v118\n"
    "v_fma_f32 v159, -v159, s76, v119\n"
    "v_mfma_f32_16x16x32_bf16 v[196:199], a[200:203], a[252:255], v[196:199]\n"
    "v_fma_f32 v116, v116, 2.0, -v156\n"
    "v_fma_f32 v117, v117, 2.0, -v157\n"
    "v_mfma_f32_16x16x32_bf16 v[196:199], a[204:207], a[8:11], v[196:199]\n"
    "s_waitcnt lgkmcnt(2)\n"
    "v_fma_f32 v118, v118, 2.0, -v158\n"
    "v_fma_f32 v119, v119, 2.0, -v159\n"
    "v_mfma_f32_16x16x32_bf16 v[196:199], a[208:211], a[60:63], v[196:199]\n"
    "ds_write_b64 v40, v[232:233] offset:19456\n"
    "v_mfma_f32_16x16x32_bf16 v[120:123], a[128:131], a[24:27], v[120:123]\n"
    "ds_write_b128 v46, v[156:159] offset:9216\n"
    "v_mfma_f32_16x16x32_bf16 v[120:123], a[132:135], a[28:31], v[120:123]\n"
    "v_cvt_pk_bf16_f32 v112, v112, v113\n"
    "v_cvt_pk_bf16_f32 v113, v114, v115\n"
    "v_mfma_f32_16x16x32_bf16 v[120:123], a[136:139], a[32:35], v[120:123]\n"
    "v_fma_f32 v236, v236, s76, v156\n"
    "v_fma_f32 v237, v237, s76, v157\n"
    "v_mfma_f32_16x16x32_bf16 v[120:123], a[140:143], a[36:39], v[120:123]\n"
    "ds_write_b128 v46, v[116:119] offset:8192\n"
    "v_mfma_f32_16x16x32_bf16 v[120:123], a[144:147], a[40:43], v[120:123]\n"
    "v_fma_f32 v238, v238, s76, v158\n"
    "v_fma_f32 v239, v239, s76, v159\n"
    "v_mfma_f32_16x16x32_bf16 v[120:123], a[148:151], a[44:47], v[120:123]\n"
    "v_fma_f32 v196, v196, s76, v116\n"
    "v_fma_f32 v197, v197, s76, v117\n"
    "v_mfma_f32_16x16x32_bf16 v[120:123], a[152:155], a[48:51], v[120:123]\n"
    "ds_write_b64 v40, v[112:113] offset:16384\n"
    "v_mfma_f32_16x16x32_bf16 v[160:163], a[156:159], a[80:83], v[160:163]\n"
    "ds_write_b128 v46, v[236:239] offset:11264\n"
    "v_mfma_f32_16x16x32_bf16 v[160:163], a[160:163], a[84:87], v[160:163]\n"
    "v_fma_f32 v198, v198, s76, v118\n"
    "v_fma_f32 v199, v199, s76, v119\n"
    "v_mfma_f32_16x16x32_bf16 v[160:163], a[164:167], a[92:95], v[160:163]\n"
    "v_mfma_f32_16x16x32_bf16 v[160:163], a[168:171], a[244:247], v[160:163]\n"
    "ds_write_b128 v46, v[196:199] offset:10240\n"
    "v_mfma_f32_16x16x32_bf16 v[160:163], a[172:175], a[0:3], v[160:163]\n"
    "v_mfma_f32_16x16x32_bf16 v[160:163], a[176:179], a[12:15], v[160:163]\n"
    "v_mfma_f32_16x16x32_bf16 v[160:163], a[180:183], a[64:67], v[160:163]\n"
    "v_mfma_f32_16x16x32_bf16 v[240:243], a[212:215], a[4:7], v[240:243]\n"
    "s_waitcnt lgkmcnt(0)\n"
    "s_barrier\n"
    "v_mfma_f32_16x16x32_bf16 v[240:243], a[216:219], a[96:99], v[240:243]\n"
    "ds_read_b128 v[116:119], v47 offset:0\n"
    "ds_read_b128 v[156:159], v47 offset:1024\n"
    "v_mfma_f32_16x16x32_bf16 v[200:203], a[184:187], a[124:127], v[200:203]\n"
    "ds_read_b128 v[196:199], v47 offset:2048\n"
    "ds_read_b128 v[236:239], v47 offset:3072\n"
    "v_mfma_f32_16x16x32_bf16 v[200:203], a[188:191], a[220:223], v[200:203]\n"
    "ds_read_b128 v[60:63], v41 offset:16384\n"
    "ds_read_b128 v[64:67], v41 offset:12288\n"
    "v_mfma_f32_16x16x32_bf16 v[200:203], a[192:195], a[240:243], v[200:203]\n"
    "ds_read_b128 a[232:235], v18 offset:57744\n"
    "v_mul_f32 v120, s76, v120\n"
    "v_mfma_f32_16x16x32_bf16 v[200:203], a[196:199], a[252:255], v[200:203]\n"
    "ds_read_b128 a[104:107], v19 offset:55360\n"
    "v_mul_f32 v121, s76, v121\n"
    "v_mfma_f32_16x16x32_bf16 v[200:203], a[200:203], a[8:11], v[200:203]\n"
    "ds_read_b128 a[228:231], v18 offset:57824\n"
    "v_mul_f32 v122, s76, v122\n"
    "v_mfma_f32_16x16x32_bf16 v[200:203], a[204:207], a[60:63], v[200:203]\n"
    "ds_read_b128 a[108:111], v18 offset:58320\n"
    "v_mul_f32 v123, s76, v123\n"
    "v_mfma_f32_16x16x32_bf16 v[200:203], a[208:211], a[224:227], v[200:203]\n"
    "s_waitcnt lgkmcnt(5)\n"
    "s_add_u32 s58, s53, 344064\n"
    "buffer_store_dwordx4 v[60:63], v42, s[24:27], s58 offen sc0 sc1\n"
    "ds_read_b128 a[68:71], v19 offset:55936\n"
    "v_mfma_f32_16x16x32_bf16 v[124:127], a[128:131], a[28:31], v[124:127]\n"
    "ds_read_b128 a[100:103], v18 offset:58400\n"
    "v_fma_f32 v160, -v160, s76, v120\n"
    "v_mfma_f32_16x16x32_bf16 v[124:127], a[132:135], a[32:35], v[124:127]\n"
    "s_waitcnt lgkmcnt(6)\n"
    "s_add_u32 s58, s53, 258048\n"
    "buffer_store_dwordx4 v[64:67], v42, s[24:27], s58 offen sc0 sc1\n"
    "v_fma_f32 v161, -v161, s76, v121\n"
    "v_mfma_f32_16x16x32_bf16 v[124:127], a[136:139], a[36:39], v[124:127]\n"
    "v_fma_f32 v162, -v162, s76, v122\n"
    "v_fma_f32 v163, -v163, s76, v123\n"
    "v_mfma_f32_16x16x32_bf16 v[124:127], a[140:143], a[40:43], v[124:127]\n"
    "v_fma_f32 v120, v120, 2.0, -v160\n"
    "v_fma_f32 v121, v121, 2.0, -v161\n"
    "v_mfma_f32_16x16x32_bf16 v[124:127], a[144:147], a[44:47], v[124:127]\n"
    "v_fma_f32 v122, v122, 2.0, -v162\n"
    "v_fma_f32 v123, v123, 2.0, -v163\n"
    "v_mfma_f32_16x16x32_bf16 v[124:127], a[148:151], a[48:51], v[124:127]\n"
    "v_fma_f32 v240, v240, s76, v160\n"
    "v_fma_f32 v241, v241, s76, v161\n"
    "v_mfma_f32_16x16x32_bf16 v[124:127], a[152:155], a[52:55], v[124:127]\n"
    "v_fma_f32 v242, v242, s76, v162\n"
    "v_fma_f32 v243, v243, s76, v163\n"
    "v_pk_fma_f32 v[240:241], v[236:237], s[84:85], v[240:241] op_sel_hi:[1,0,1]\n"
    "v_pk_fma_f32 v[242:243], v[238:239], s[84:85], v[242:243] op_sel_hi:[1,0,1]\n"
    "v_pk_fma_f32 v[160:161], v[156:157], 2.0, v[160:161] op_sel_hi:[1,0,1]\n"
    "v_pk_fma_f32 v[162:163], v[158:159], 2.0, v[162:163] op_sel_hi:[1,0,1]\n"
    "v_pk_fma_f32 v[156:157], v[116:117], 4.0, v[120:121] op_sel_hi:[1,0,1]\n"
    "v_pk_fma_f32 v[158:159], v[118:119], 4.0, v[122:123] op_sel_hi:[1,0,1]\n"
    "v_pk_add_f32 v[120:121], v[120:121], v[116:117]\n"
    "v_pk_add_f32 v[122:123], v[122:123], v[118:119]\n"
    "v_pk_fma_f32 v[240:241], v[196:197], s[82:83], v[240:241] op_sel_hi:[1,0,1]\n"
    "v_pk_fma_f32 v[242:243], v[198:199], s[82:83], v[242:243] op_sel_hi:[1,0,1]\n"
    "v_pk_fma_f32 v[160:161], v[196:197], s[78:79], v[160:161] op_sel_hi:[1,0,1]\n"
    "v_pk_fma_f32 v[162:163], v[198:199], s[78:79], v[162:163] op_sel_hi:[1,0,1]\n"
    "v_mfma_f32_16x16x32_bf16 v[164:167], a[156:159], a[84:87], v[164:167]\n"
    "v_fmac_f32 v156, s80, v196\n"
    "v_fmac_f32 v157, s80, v197\n"
    "v_mfma_f32_16x16x32_bf16 v[164:167], a[160:163], a[92:95], v[164:167]\n"
    "v_fmac_f32 v158, s80, v198\n"
    "v_fmac_f32 v159, s80, v199\n"
    "v_mfma_f32_16x16x32_bf16 v[164:167], a[164:167], a[244:247], v[164:167]\n"
    "v_fmac_f32 v120, s76, v196\n"
    "v_fmac_f32 v121, s76, v197\n"
    "v_mfma_f32_16x16x32_bf16 v[164:167], a[168:171], a[0:3], v[164:167]\n"
    "v_fmac_f32 v122, s76, v198\n"
    "v_fmac_f32 v123, s76, v199\n"
    "v_mfma_f32_16x16x32_bf16 v[164:167], a[172:175], a[12:15], v[164:167]\n"
    "v_fmac_f32 v120, s76, v200\n"
    "v_fmac_f32 v121, s76, v201\n"
    "v_mfma_f32_16x16x32_bf16 v[164:167], a[176:179], a[64:67], v[164:167]\n"
    "s_waitcnt lgkmcnt(0)\n"
    "v_fmac_f32 v122, s76, v202\n"
    "v_fmac_f32 v123, s76, v203\n"
    "v_mfma_f32_16x16x32_bf16 v[164:167], a[180:183], a[232:235], v[164:167]\n"
    "v_cvt_pk_bf16_f32 v160, v160, v161\n"
    "v_cvt_pk_bf16_f32 v161, v162, v163\n"
    "v_mfma_f32_16x16x32_bf16 v[244:247], a[212:215], a[88:91], v[244:247]\n"
    "v_cvt_pk_bf16_f32 v156, v156, v157\n"
    "v_cvt_pk_bf16_f32 v157, v158, v159\n"
    "v_mfma_f32_16x16x32_bf16 v[244:247], a[216:219], a[104:107], v[244:247]\n"
    "ds_write_b64 v40, v[160:161] offset:25600\n"
    "v_mfma_f32_16x16x32_bf16 v[204:207], a[184:187], a[220:223], v[204:207]\n"
    "v_pk_mul_f32 v[124:125], v[124:125], s[76:77] op_sel_hi:[1,0]\n"
    "v_pk_mul_f32 v[126:127], v[126:127], s[76:77] op_sel_hi:[1,0]\n"
    "v_pk_fma_f32 v[164:165], v[164:165], s[76:77], v[124:125] op_sel_hi:[1,0,1] neg_lo:[1,0,0] neg_hi:[1,0,0]\n"
    "v_pk_fma_f32 v[166:167], v[166:167], s[76:77], v[126:127] op_sel_hi:[1,0,1] neg_lo:[1,0,0] neg_hi:[1,0,0]\n"
    "v_pk_fma_f32 v[124:125], v[124:125], 2.0, v[164:165] op_sel_hi:[1,0,1] neg_lo:[0,0,1] neg_hi:[0,0,1]\n"
    "v_pk_fma_f32 v[126:127], v[126:127], 2.0, v[166:167] op_sel_hi:[1,0,1] neg_lo:[0,0,1] neg_hi:[0,0,1]\n"
    "v_cvt_pk_bf16_f32 v240, v240, v241\n"
    "v_cvt_pk_bf16_f32 v241, v242, v243\n"
    "v_mfma_f32_16x16x32_bf16 v[204:207], a[188:191], a[240:243], v[204:207]\n"
    "ds_write_b64 v40, v[156:157] offset:26624\n"
    "v_mfma_f32_16x16x32_bf16 v[204:207], a[192:195], a[252:255], v[204:207]\n"
    "ds_write_b128 v46, v[164:167] offset:1024\n"
    "v_mfma_f32_16x16x32_bf16 v[204:207], a[196:199], a[8:11], v[204:207]\n"
    "v_fma_f32 v244, v244, s76, v164\n"
    "v_fma_f32 v245, v245, s76, v165\n"
    "v_mfma_f32_16x16x32_bf16 v[204:207], a[200:203], a[60:63], v[204:207]\n"
    "v_fma_f32 v246, v246, s76, v166\n"
    "v_fma_f32 v247, v247, s76, v167\n"
    "v_mfma_f32_16x16x32_bf16 v[204:207], a[204:207], a[224:227], v[204:207]\n"
    "ds_write_b128 v46, v[124:127] offset:0\n"
    "v_mfma_f32_16x16x32_bf16 v[204:207], a[208:211], a[228:231], v[204:207]\n"
    "v_cvt_pk_bf16_f32 v120, v120, v121\n"
    "v_cvt_pk_bf16_f32 v121, v122, v123\n"
    "v_mfma_f32_16x16x32_bf16 v[128:131], a[128:131], a[32:35], v[128:131]\n"
    "v_mfma_f32_16x16x32_bf16 v[128:131], a[132:135], a[36:39], v[128:131]\n"
    "ds_write_b128 v46, v[244:247] offset:3072\n"
    "v_mfma_f32_16x16x32_bf16 v[128:131], a[136:139], a[40:43], v[128:131]\n"
    "v_fma_f32 v204, v204, s76, v124\n"
    "v_fma_f32 v205, v205, s76, v125\n"
    "v_mfma_f32_16x16x32_bf16 v[128:131], a[140:143], a[44:47], v[128:131]\n"
    "v_fma_f32 v206, v206, s76, v126\n"
    "v_fma_f32 v207, v207, s76, v127\n"
    "v_mfma_f32_16x16x32_bf16 v[128:131], a[144:147], a[48:51], v[128:131]\n"
    "ds_write_b64 v40, v[240:241] offset:27648\n"
    "v_mfma_f32_16x16x32_bf16 v[128:131], a[148:151], a[52:55], v[128:131]\n"
    "ds_write_b128 v46, v[204:207] offset:2048\n"
    "v_mfma_f32_16x16x32_bf16 v[128:131], a[152:155], a[56:59], v[128:131]\n"
    "ds_write_b64 v40, v[120:121] offset:24576\n"
    "v_mfma_f32_16x16x32_bf16 v[168:171], a[156:159], a[92:95], v[168:171]\n"
    "v_mfma_f32_16x16x32_bf16 v[168:171], a[160:163], a[244:247], v[168:171]\n"
    "v_mfma_f32_16x16x32_bf16 v[168:171], a[164:167], a[0:3], v[168:171]\n"
    "v_mfma_f32_16x16x32_bf16 v[168:171], a[168:171], a[12:15], v[168:171]\n"
    "v_mfma_f32_16x16x32_bf16 v[168:171], a[172:175], a[64:67], v[168:171]\n"
    "v_mfma_f32_16x16x32_bf16 v[168:171], a[176:179], a[232:235], v[168:171]\n"
    "s_waitcnt lgkmcnt(0)\n"
    "s_barrier\n"
    "v_mfma_f32_16x16x32_bf16 v[168:171], a[180:183], a[108:111], v[168:171]\n"
    "ds_read_b128 v[124:127], v47 offset:4096\n"
    "ds_read_b128 v[164:167], v47 offset:5120\n"
    "v_mfma_f32_16x16x32_bf16 v[248:251], a[212:215], a[96:99], v[248:251]\n"
    "ds_read_b128 v[204:207], v47 offset:6144\n"
    "ds_read_b128 v[244:247], v47 offset:7168\n"
    "v_mfma_f32_16x16x32_bf16 v[248:251], a[216:219], a[68:71], v[248:251]\n"
    "ds_read_b128 v[68:71], v41 offset:24576\n"
    "ds_read_b128 v[72:75], v41 offset:20480\n"
    "v_mfma_f32_16x16x32_bf16 v[208:211], a[184:187], a[240:243], v[208:211]\n"
    "v_pk_mul_f32 v[128:129], v[128:129], s[76:77] op_sel_hi:[1,0]\n"
    "v_pk_mul_f32 v[130:131], v[130:131], s[76:77] op_sel_hi:[1,0]\n"
    "v_pk_fma_f32 v[168:169], v[168:169], s[76:77], v[128:129] op_sel_hi:[1,0,1] neg_lo:[1,0,0] neg_hi:[1,0,0]\n"
    "v_pk_fma_f32 v[170:171], v[170:171], s[76:77], v[130:131] op_sel_hi:[1,0,1] neg_lo:[1,0,0] neg_hi:[1,0,0]\n"
    "v_pk_fma_f32 v[128:129], v[128:129], 2.0, v[168:169] op_sel_hi:[1,0,1] neg_lo:[0,0,1] neg_hi:[0,0,1]\n"
    "v_pk_fma_f32 v[130:131], v[130:131], 2.0, v[170:171] op_sel_hi:[1,0,1] neg_lo:[0,0,1] neg_hi:[0,0,1]\n"
    "v_mfma_f32_16x16x32_bf16 v[208:211], a[188:191], a[252:255], v[208:211]\n"
    "v_mfma_f32_16x16x32_bf16 v[208:211], a[192:195], a[8:11], v[208:211]\n"
    "v_fma_f32 v248, v248, s76, v168\n"
    "v_fma_f32 v249, v249, s76, v169\n"
    "v_mfma_f32_16x16x32_bf16 v[208:211], a[196:199], a[60:63], v[208:211]\n"
    "v_fma_f32 v250, v250, s76, v170\n"
    "v_fma_f32 v251, v251, s76, v171\n"
    "v_mfma_f32_16x16x32_bf16 v[208:211], a[200:203], a[224:227], v[208:211]\n"
    "s_waitcnt lgkmcnt(1)\n"
    "s_add_u32 s58, s53, 516096\n"
    "buffer_store_dwordx4 v[68:71], v42, s[24:27], s58 offen sc0 sc1\n"
    "v_pk_fma_f32 v[248:249], v[244:245], s[84:85], v[248:249] op_sel_hi:[1,0,1]\n"
    "v_pk_fma_f32 v[250:251], v[246:247], s[84:85], v[250:251] op_sel_hi:[1,0,1]\n"
    "v_pk_fma_f32 v[168:169], v[164:165], 2.0, v[168:169] op_sel_hi:[1,0,1]\n"
    "v_pk_fma_f32 v[170:171], v[166:167], 2.0, v[170:171] op_sel_hi:[1,0,1]\n"
    "v_pk_fma_f32 v[164:165], v[124:125], 4.0, v[128:129] op_sel_hi:[1,0,1]\n"
    "v_pk_fma_f32 v[166:167], v[126:127], 4.0, v[130:131] op_sel_hi:[1,0,1]\n"
    "v_pk_add_f32 v[128:129], v[128:129], v[124:125]\n"
    "v_pk_add_f32 v[130:131], v[130:131], v[126:127]\n"
    "v_pk_fma_f32 v[248:249], v[204:205], s[82:83], v[248:249] op_sel_hi:[1,0,1]\n"
    "v_pk_fma_f32 v[250:251], v[206:207], s[82:83], v[250:251] op_sel_hi:[1,0,1]\n"
    "v_pk_fma_f32 v[168:169], v[204:205], s[78:79], v[168:169] op_sel_hi:[1,0,1]\n"
    "v_pk_fma_f32 v[170:171], v[206:207], s[78:79], v[170:171] op_sel_hi:[1,0,1]\n"
    "v_pk_fma_f32 v[164:165], v[204:205], s[80:81], v[164:165] op_sel_hi:[1,0,1]\n"
    "v_pk_fma_f32 v[166:167], v[206:207], s[80:81], v[166:167] op_sel_hi:[1,0,1]\n"
    "v_pk_fma_f32 v[128:129], v[204:205], s[76:77], v[128:129] op_sel_hi:[1,0,1]\n"
    "v_pk_fma_f32 v[130:131], v[206:207], s[76:77], v[130:131] op_sel_hi:[1,0,1]\n"
    "v_mfma_f32_16x16x32_bf16 v[208:211], a[204:207], a[228:231], v[208:211]\n"
    "v_mfma_f32_16x16x32_bf16 v[208:211], a[208:211], a[100:103], v[208:211]\n"
    "s_waitcnt lgkmcnt(0)\n"
    "s_add_u32 s58, s53, 430080\n"
    "buffer_store_dwordx4 v[72:75], v42, s[24:27], s58 offen sc0 sc1\n"
    "s_nop 7\n"
    "v_pk_fma_f32 v[128:129], v[208:209], s[76:77], v[128:129] op_sel_hi:[1,0,1]\n"
    "v_pk_fma_f32 v[130:131], v[210:211], s[76:77], v[130:131] op_sel_hi:[1,0,1]\n"
    "v_cvt_pk_bf16_f32 v168, v168, v169\n"
    "v_cvt_pk_bf16_f32 v169, v170, v171\n"
    "v_cvt_pk_bf16_f32 v164, v164, v165\n"
    "v_cvt_pk_bf16_f32 v165, v166, v167\n"
    "v_cvt_pk_bf16_f32 v248, v248, v249\n"
    "v_cvt_pk_bf16_f32 v249, v250, v251\n"
    "v_cvt_pk_bf16_f32 v128, v128, v129\n"
    "v_cvt_pk_bf16_f32 v129, v130, v131\n"
    "ds_write_b64 v40, v[168:169] offset:33792\n"
    "ds_write_b64 v40, v[164:165] offset:34816\n"
    "ds_write_b64 v40, v[248:249] offset:35840\n"
    "ds_write_b64 v40, v[128:129] offset:32768\n"
    "s_waitcnt lgkmcnt(0)\n"
    "s_barrier\n"
    "ds_read_b128 v[96:99], v41 offset:32768\n"
    "ds_read_b128 v[100:103], v41 offset:28672\n"
    "s_add_u32 s58, s53, 688128\n"
    "s_waitcnt lgkmcnt(1)\n"
    "buffer_store_dwordx4 v[96:99], v42, s[24:27], s58 offen sc0 sc1\n"
    "s_add_u32 s58, s53, 602112\n"
    "s_waitcnt lgkmcnt(0)\n"
    "buffer_store_dwordx4 v[100:103], v42, s[24:27], s58 offen sc0 sc1\n"
    "s_endpgm\n"
    "13:\n"
    "v_mfma_f32_16x16x32_bf16 v[96:99], a[128:131], a[0:3], v[96:99]\n"
    "buffer_load_dwordx4 a[212:215], v5, s[28:31], s50 offen offset:1024 sc1\n"
    "ds_read_b128 a[60:63], v18 offset:50256\n"
    "v_mfma_f32_16x16x32_bf16 v[96:99], a[132:135], a[4:7], v[96:99]\n"
    "v_add_u32 v46, s48, v5\n"
    "v_add_u32 v47, s49, v5\n"
    "v_mfma_f32_16x16x32_bf16 v[96:99], a[136:139], a[8:11], v[96:99]\n"
    "s_mov_b32 s76, 0x3f360b61\n"
    "buffer_load_dwordx4 a[216:219], v5, s[28:31], s50 offen offset:2048 sc1\n"
    "s_mov_b32 s78, 0x3eb60b61\n"
    "v_mfma_f32_16x16x32_bf16 v[96:99], a[140:143], a[12:15], v[96:99]\n"
    "s_mov_b32 s80, 0x3e360b61\n"
    "s_mov_b32 s82, 0x3db60b61\n"
    "s_mov_b32 s84, 0x41000000\n"
    "v_mfma_f32_16x16x32_bf16 v[96:99], a[144:147], a[16:19], v[96:99]\n"
    "s_lshl_b32 s72, s43, 5\n"
    "v_xor_b32 v40, s72, v40\n"
    "s_lshl_b32 s72, s42, 10\n"
    "v_mfma_f32_16x16x32_bf16 v[96:99], a[148:151], a[20:23], v[96:99]\n"
    "v_add_u32 v41, s72, v41\n"
    "ds_read_b128 a[64:67], v18 offset:50832\n"
    "v_mfma_f32_16x16x32_bf16 v[96:99], a[152:155], a[24:27], v[96:99]\n"
    "ds_read_b128 a[68:71], v18 offset:51408\n"
    "v_mfma_f32_16x16x32_bf16 v[104:107], a[128:131], a[8:11], v[104:107]\n"
    "ds_read_b128 a[72:75], v18 offset:51984\n"
    "v_mfma_f32_16x16x32_bf16 v[104:107], a[132:135], a[12:15], v[104:107]\n"
    "ds_read_b128 a[76:79], v18 offset:52560\n"
    "v_mfma_f32_16x16x32_bf16 v[104:107], a[136:139], a[16:19], v[104:107]\n"
    "ds_read_b128 a[80:83], v18 offset:53136\n"
    "v_mfma_f32_16x16x32_bf16 v[104:107], a[140:143], a[20:23], v[104:107]\n"
    "ds_read_b128 a[84:87], v18 offset:53712\n"
    "v_mfma_f32_16x16x32_bf16 v[104:107], a[144:147], a[24:27], v[104:107]\n"
    "ds_read_b128 a[88:91], v18 offset:54288\n"
    "v_mfma_f32_16x16x32_bf16 v[104:107], a[148:151], a[28:31], v[104:107]\n"
    "ds_read_b128 a[92:95], v18 offset:54864\n"
    "v_mfma_f32_16x16x32_bf16 v[104:107], a[152:155], a[32:35], v[104:107]\n"
    "ds_read_b128 a[96:99], v19 offset:50176\n"
    "v_mfma_f32_16x16x32_bf16 v[100:103], a[128:131], a[4:7], v[100:103]\n"
    "ds_read_b128 a[100:103], v19 offset:51328\n"
    "v_mfma_f32_16x16x32_bf16 v[100:103], a[132:135], a[8:11], v[100:103]\n"
    "ds_read_b128 a[104:107], v18 offset:50336\n"
    "v_mfma_f32_16x16x32_bf16 v[100:103], a[136:139], a[12:15], v[100:103]\n"
    "ds_read_b128 a[108:111], v18 offset:50912\n"
    "v_mfma_f32_16x16x32_bf16 v[100:103], a[140:143], a[16:19], v[100:103]\n"
    "ds_read_b128 a[112:115], v18 offset:51488\n"
    "v_mfma_f32_16x16x32_bf16 v[100:103], a[144:147], a[20:23], v[100:103]\n"
    "ds_read_b128 a[116:119], v18 offset:52064\n"
    "v_mfma_f32_16x16x32_bf16 v[100:103], a[148:151], a[24:27], v[100:103]\n"
    "ds_read_b128 a[120:123], v18 offset:52640\n"
    "v_mfma_f32_16x16x32_bf16 v[100:103], a[152:155], a[28:31], v[100:103]\n"
    "ds_read_b128 a[124:127], v18 offset:53216\n"
    "v_mfma_f32_16x16x32_bf16 v[112:115], a[128:131], a[16:19], v[112:115]\n"
    "ds_read_b128 a[220:223], v18 offset:53792\n"
    "v_mfma_f32_16x16x32_bf16 v[112:115], a[132:135], a[20:23], v[112:115]\n"
    "ds_read_b128 a[224:227], v19 offset:52480\n"
    "v_mfma_f32_16x16x32_bf16 v[112:115], a[136:139], a[24:27], v[112:115]\n"
    "ds_read_b128 a[228:231], v18 offset:54368\n"
    "v_mfma_f32_16x16x32_bf16 v[112:115], a[140:143], a[28:31], v[112:115]\n"
    "ds_read_b128 a[232:235], v18 offset:54944\n"
    "v_mfma_f32_16x16x32_bf16 v[112:115], a[144:147], a[32:35], v[112:115]\n"
    "ds_read_b128 a[236:239], v19 offset:50752\n"
    "v_mfma_f32_16x16x32_bf16 v[112:115], a[148:151], a[36:39], v[112:115]\n"
    "ds_read_b128 a[240:243], v19 offset:51904\n"
    "v_mfma_f32_16x16x32_bf16 v[112:115], a[152:155], a[40:43], v[112:115]\n"
    "s_waitcnt vmcnt(9) lgkmcnt(9)\n"
    "ds_read_b128 a[244:247], v18 offset:55440\n"
    "v_mfma_f32_16x16x32_bf16 v[136:139], a[156:159], a[60:63], v[136:139]\n"
    "ds_read_b128 a[248:251], v18 offset:56016\n"
    "v_mfma_f32_16x16x32_bf16 v[136:139], a[160:163], a[64:67], v[136:139]\n"
    "ds_read_b128 a[252:255], v19 offset:53632\n"
    "v_mfma_f32_16x16x32_bf16 v[136:139], a[164:167], a[68:71], v[136:139]\n"
    "ds_read_b128 a[0:3], v18 offset:55520\n"
    "v_mfma_f32_16x16x32_bf16 v[136:139], a[168:171], a[72:75], v[136:139]\n"
    "v_mfma_f32_16x16x32_bf16 v[136:139], a[172:175], a[76:79], v[136:139]\n"
    "v_mfma_f32_16x16x32_bf16 v[136:139], a[176:179], a[80:83], v[136:139]\n"
    "v_mfma_f32_16x16x32_bf16 v[136:139], a[180:183], a[84:87], v[136:139]\n"
    "v_mfma_f32_16x16x32_bf16 v[144:147], a[156:159], a[68:71], v[144:147]\n"
    "v_mfma_f32_16x16x32_bf16 v[144:147], a[160:163], a[72:75], v[144:147]\n"
    "v_mfma_f32_16x16x32_bf16 v[144:147], a[164:167], a[76:79], v[144:147]\n"
    "v_mul_f32 v96, s76, v96\n"
    "v_mul_f32 v97, s76, v97\n"
    "v_mfma_f32_16x16x32_bf16 v[144:147], a[168:171], a[80:83], v[144:147]\n"
    "v_mul_f32 v98, s76, v98\n"
    "v_mul_f32 v99, s76, v99\n"
    "v_mfma_f32_16x16x32_bf16 v[144:147], a[172:175], a[84:87], v[144:147]\n"
    "v_fma_f32 v136, -v136, s76, v96\n"
    "v_fma_f32 v137, -v137, s76, v97\n"
    "v_mfma_f32_16x16x32_bf16 v[144:147], a[176:179], a[88:91], v[144:147]\n"
    "v_fma_f32 v138, -v138, s76, v98\n"
    "v_fma_f32 v139, -v139, s76, v99\n"
    "v_mfma_f32_16x16x32_bf16 v[144:147], a[180:183], a[92:95], v[144:147]\n"
    "v_fma_f32 v96, v96, 2.0, -v136\n"
    "v_fma_f32 v97, v97, 2.0, -v137\n"
    "v_mfma_f32_16x16x32_bf16 v[140:143], a[156:159], a[64:67], v[140:143]\n"
    "v_fma_f32 v98, v98, 2.0, -v138\n"
    "v_fma_f32 v99, v99, 2.0, -v139\n"
    "v_mfma_f32_16x16x32_bf16 v[140:143], a[160:163], a[68:71], v[140:143]\n"
    "v_mfma_f32_16x16x32_bf16 v[140:143], a[164:167], a[72:75], v[140:143]\n"
    "ds_write_b128 v46, v[136:139] offset:1024\n"
    "v_mfma_f32_16x16x32_bf16 v[140:143], a[168:171], a[76:79], v[140:143]\n"
    "v_mul_f32 v104, s76, v104\n"
    "v_mul_f32 v105, s76, v105\n"
    "v_mfma_f32_16x16x32_bf16 v[140:143], a[172:175], a[80:83], v[140:143]\n"
    "v_mul_f32 v106, s76, v106\n"
    "v_mul_f32 v107, s76, v107\n"
    "v_mfma_f32_16x16x32_bf16 v[140:143], a[176:179], a[84:87], v[140:143]\n"
    "ds_write_b128 v46, v[96:99] offset:0\n"
    "v_mfma_f32_16x16x32_bf16 v[140:143], a[180:183], a[88:91], v[140:143]\n"
    "s_waitcnt vmcnt(0) lgkmcnt(2)\n"
    "v_fma_f32 v144, -v144, s76, v104\n"
    "v_fma_f32 v145, -v145, s76, v105\n"
    "v_mfma_f32_16x16x32_bf16 v[216:219], a[212:215], a[96:99], v[216:219]\n"
    "v_fma_f32 v146, -v146, s76, v106\n"
    "v_fma_f32 v147, -v147, s76, v107\n"
    "v_mfma_f32_16x16x32_bf16 v[216:219], a[216:219], a[100:103], v[216:219]\n"
    "v_fma_f32 v104, v104, 2.0, -v144\n"
    "v_fma_f32 v105, v105, 2.0, -v145\n"
    "v_mfma_f32_16x16x32_bf16 v[176:179], a[184:187], a[104:107], v[176:179]\n"
    "v_fma_f32 v106, v106, 2.0, -v146\n"
    "v_fma_f32 v107, v107, 2.0, -v147\n"
    "v_mfma_f32_16x16x32_bf16 v[176:179], a[188:191], a[108:111], v[176:179]\n"
    "v_mul_f32 v100, s76, v100\n"
    "v_mul_f32 v101, s76, v101\n"
    "v_mfma_f32_16x16x32_bf16 v[176:179], a[192:195], a[112:115], v[176:179]\n"
    "ds_write_b128 v46, v[144:147] offset:5120\n"
    "v_mfma_f32_16x16x32_bf16 v[176:179], a[196:199], a[116:119], v[176:179]\n"
    "v_fma_f32 v216, v216, s82, v136\n"
    "v_fma_f32 v217, v217, s82, v137\n"
    "v_mfma_f32_16x16x32_bf16 v[176:179], a[200:203], a[120:123], v[176:179]\n"
    "v_fma_f32 v218, v218, s82, v138\n"
    "v_fma_f32 v219, v219, s82, v139\n"
    "v_mfma_f32_16x16x32_bf16 v[176:179], a[204:207], a[124:127], v[176:179]\n"
    "ds_write_b128 v46, v[104:107] offset:4096\n"
    "v_mfma_f32_16x16x32_bf16 v[176:179], a[208:211], a[220:223], v[176:179]\n"
    "v_mul_f32 v102, s76, v102\n"
    "v_mul_f32 v103, s76, v103\n"
    "v_mfma_f32_16x16x32_bf16 v[224:227], a[212:215], a[100:103], v[224:227]\n"
    "v_fma_f32 v140, -v140, s76, v100\n"
    "v_fma_f32 v141, -v141, s76, v101\n"
    "v_mfma_f32_16x16x32_bf16 v[224:227], a[216:219], a[224:227], v[224:227]\n"
    "ds_write_b128 v46, v[216:219] offset:3072\n"
    "v_mfma_f32_16x16x32_bf16 v[184:187], a[184:187], a[112:115], v[184:187]\n"
    "v_fma_f32 v142, -v142, s76, v102\n"
    "v_fma_f32 v143, -v143, s76, v103\n"
    "v_mfma_f32_16x16x32_bf16 v[184:187], a[188:191], a[116:119], v[184:187]\n"
    "v_fma_f32 v100, v100, 2.0, -v140\n"
    "v_fma_f32 v101, v101, 2.0, -v141\n"
    "v_mfma_f32_16x16x32_bf16 v[184:187], a[192:195], a[120:123], v[184:187]\n"
    "v_fma_f32 v102, v102, 2.0, -v142\n"
    "v_fma_f32 v103, v103, 2.0, -v143\n"
    "v_mfma_f32_16x16x32_bf16 v[184:187], a[196:199], a[124:127], v[184:187]\n"
    "ds_write_b128 v46, v[176:179] offset:2048\n"
    "v_mfma_f32_16x16x32_bf16 v[184:187], a[200:203], a[220:223], v[184:187]\n"
    "v_fma_f32 v224, v224, s82, v144\n"
    "v_fma_f32 v225, v225, s82, v145\n"
    "v_mfma_f32_16x16x32_bf16 v[184:187], a[204:207], a[228:231], v[184:187]\n"
    "v_fma_f32 v226, v226, s82, v146\n"
    "v_fma_f32 v227, v227, s82, v147\n"
    "v_mfma_f32_16x16x32_bf16 v[184:187], a[208:211], a[232:235], v[184:187]\n"
    "v_mfma_f32_16x16x32_bf16 v[220:223], a[212:215], a[236:239], v[220:223]\n"
    "ds_write_b128 v46, v[224:227] offset:7168\n"
    "v_mfma_f32_16x16x32_bf16 v[220:223], a[216:219], a[240:243], v[220:223]\n"
    "v_mfma_f32_16x16x32_bf16 v[180:183], a[184:187], a[108:111], v[180:183]\n"
    "v_mfma_f32_16x16x32_bf16 v[180:183], a[188:191], a[112:115], v[180:183]\n"
    "v_mfma_f32_16x16x32_bf16 v[180:183], a[192:195], a[116:119], v[180:183]\n"
    "ds_write_b128 v46, v[184:187] offset:6144\n"
    "v_mfma_f32_16x16x32_bf16 v[180:183], a[196:199], a[120:123], v[180:183]\n"
    "v_mfma_f32_16x16x32_bf16 v[180:183], a[200:203], a[124:127], v[180:183]\n"
    "v_mfma_f32_16x16x32_bf16 v[180:183], a[204:207], a[220:223], v[180:183]\n"
    "v_mfma_f32_16x16x32_bf16 v[180:183], a[208:211], a[228:231], v[180:183]\n"
    "s_waitcnt lgkmcnt(0)\n"
    "s_barrier\n"
    "v_mfma_f32_16x16x32_bf16 v[152:155], a[156:159], a[76:79], v[152:155]\n"
    "ds_read_b128 v[96:99], v47 offset:0\n"
    "ds_read_b128 v[136:139], v47 offset:1024\n"
    "v_mfma_f32_16x16x32_bf16 v[152:155], a[160:163], a[80:83], v[152:155]\n"
    "ds_read_b128 v[176:179], v47 offset:2048\n"
    "ds_read_b128 v[216:219], v47 offset:3072\n"
    "v_mfma_f32_16x16x32_bf16 v[152:155], a[164:167], a[84:87], v[152:155]\n"
    "ds_read_b128 a[4:7], v18 offset:56096\n"
    "v_mfma_f32_16x16x32_bf16 v[152:155], a[168:171], a[88:91], v[152:155]\n"
    "ds_read_b128 a[8:11], v19 offset:53056\n"
    "v_mfma_f32_16x16x32_bf16 v[152:155], a[172:175], a[92:95], v[152:155]\n"
    "ds_read_b128 a[60:63], v18 offset:56592\n"
    "v_mfma_f32_16x16x32_bf16 v[152:155], a[176:179], a[244:247], v[152:155]\n"
    "ds_read_b128 a[64:67], v18 offset:57168\n"
    "v_mfma_f32_16x16x32_bf16 v[152:155], a[180:183], a[248:251], v[152:155]\n"
    "ds_read_b128 a[68:71], v19 offset:54784\n"
    "v_mfma_f32_16x16x32_bf16 v[232:235], a[212:215], a[224:227], v[232:235]\n"
    "s_waitcnt lgkmcnt(5)\n"
    "v_pk_fma_f32 v[216:217], v[220:221], s[76:77], v[216:217] op_sel_hi:[1,0,1]\n"
    "v_pk_fma_f32 v[218:219], v[222:223], s[76:77], v[218:219] op_sel_hi:[1,0,1]\n"
    "v_pk_fma_f32 v[216:217], v[140:141], s[84:85], v[216:217] op_sel_hi:[1,0,1]\n"
    "v_pk_fma_f32 v[218:219], v[142:143], s[84:85], v[218:219] op_sel_hi:[1,0,1]\n"
    "v_pk_fma_f32 v[136:137], v[140:141], 2.0, v[136:137] op_sel_hi:[1,0,1]\n"
    "v_pk_fma_f32 v[138:139], v[142:143], 2.0, v[138:139] op_sel_hi:[1,0,1]\n"
    "v_pk_fma_f32 v[96:97], v[100:101], 4.0, v[96:97] op_sel_hi:[1,0,1]\n"
    "v_pk_fma_f32 v[98:99], v[102:103], 4.0, v[98:99] op_sel_hi:[1,0,1]\n"
    "v_pk_add_f32 v[176:177], v[176:177], v[100:101]\n"
    "v_pk_add_f32 v[178:179], v[178:179], v[102:103]\n"
    "v_pk_fma_f32 v[176:177], v[180:181], s[76:77], v[176:177] op_sel_hi:[1,0,1]\n"
    "v_pk_fma_f32 v[178:179], v[182:183], s[76:77], v[178:179] op_sel_hi:[1,0,1]\n"
    "v_pk_fma_f32 v[136:137], v[180:181], s[78:79], v[136:137] op_sel_hi:[1,0,1]\n"
    "v_pk_fma_f32 v[138:139], v[182:183], s[78:79], v[138:139] op_sel_hi:[1,0,1]\n"
    "v_pk_fma_f32 v[96:97], v[180:181], s[80:81], v[96:97] op_sel_hi:[1,0,1]\n"
    "v_pk_fma_f32 v[98:99], v[182:183], s[80:81], v[98:99] op_sel_hi:[1,0,1]\n"
    "v_pk_fma_f32 v[216:217], v[180:181], s[82:83], v[216:217] op_sel_hi:[1,0,1]\n"
    "v_pk_fma_f32 v[218:219], v[182:183], s[82:83], v[218:219] op_sel_hi:[1,0,1]\n"
    "v_mfma_f32_16x16x32_bf16 v[232:235], a[216:219], a[252:255], v[232:235]\n"
    "v_cvt_pk_bf16_f32 v136, v136, v137\n"
    "v_cvt_pk_bf16_f32 v137, v138, v139\n"
    "v_mfma_f32_16x16x32_bf16 v[192:195], a[184:187], a[120:123], v[192:195]\n"
    "v_mul_f32 v112, s76, v112\n"
    "v_mul_f32 v113, s76, v113\n"
    "v_mfma_f32_16x16x32_bf16 v[192:195], a[188:191], a[124:127], v[192:195]\n"
    "v_mul_f32 v114, s76, v114\n"
    "v_mul_f32 v115, s76, v115\n"
    "v_mfma_f32_16x16x32_bf16 v[192:195], a[192:195], a[220:223], v[192:195]\n"
    "v_fma_f32 v152, -v152, s76, v112\n"
    "v_fma_f32 v153, -v153, s76, v113\n"
    "v_mfma_f32_16x16x32_bf16 v[192:195], a[196:199], a[228:231], v[192:195]\n"
    "v_fma_f32 v154, -v154, s76, v114\n"
    "v_fma_f32 v155, -v155, s76, v115\n"
    "v_mfma_f32_16x16x32_bf16 v[192:195], a[200:203], a[232:235], v[192:195]\n"
    "v_fma_f32 v112, v112, 2.0, -v152\n"
    "v_fma_f32 v113, v113, 2.0, -v153\n"
    "v_mfma_f32_16x16x32_bf16 v[192:195], a[204:207], a[0:3], v[192:195]\n"
    "s_waitcnt lgkmcnt(2)\n"
    "v_fma_f32 v114, v114, 2.0, -v154\n"
    "v_fma_f32 v115, v115, 2.0, -v155\n"
    "v_mfma_f32_16x16x32_bf16 v[192:195], a[208:211], a[4:7], v[192:195]\n"
    "v_fma_f32 v232, v232, s82, v152\n"
    "v_fma_f32 v233, v233, s82, v153\n"
    "v_mfma_f32_16x16x32_bf16 v[108:111], a[128:131], a[12:15], v[108:111]\n"
    "ds_write_b128 v46, v[152:155] offset:9216\n"
    "v_mfma_f32_16x16x32_bf16 v[108:111], a[132:135], a[16:19], v[108:111]\n"
    "v_fma_f32 v234, v234, s82, v154\n"
    "v_fma_f32 v235, v235, s82, v155\n"
    "v_mfma_f32_16x16x32_bf16 v[108:111], a[136:139], a[20:23], v[108:111]\n"
    "v_cvt_pk_bf16_f32 v96, v96, v97\n"
    "v_cvt_pk_bf16_f32 v97, v98, v99\n"
    "v_mfma_f32_16x16x32_bf16 v[108:111], a[140:143], a[24:27], v[108:111]\n"
    "ds_write_b128 v46, v[112:115] offset:8192\n"
    "v_mfma_f32_16x16x32_bf16 v[108:111], a[144:147], a[28:31], v[108:111]\n"
    "v_cvt_pk_bf16_f32 v216, v216, v217\n"
    "v_cvt_pk_bf16_f32 v217, v218, v219\n"
    "v_mfma_f32_16x16x32_bf16 v[108:111], a[148:151], a[32:35], v[108:111]\n"
    "v_cvt_pk_bf16_f32 v176, v176, v177\n"
    "v_cvt_pk_bf16_f32 v177, v178, v179\n"
    "v_mfma_f32_16x16x32_bf16 v[108:111], a[152:155], a[36:39], v[108:111]\n"
    "ds_write_b128 v46, v[232:235] offset:11264\n"
    "v_mfma_f32_16x16x32_bf16 v[148:151], a[156:159], a[72:75], v[148:151]\n"
    "v_mfma_f32_16x16x32_bf16 v[148:151], a[160:163], a[76:79], v[148:151]\n"
    "v_mfma_f32_16x16x32_bf16 v[148:151], a[164:167], a[80:83], v[148:151]\n"
    "ds_write_b64 v40, v[136:137] offset:5120\n"
    "v_mfma_f32_16x16x32_bf16 v[148:151], a[168:171], a[84:87], v[148:151]\n"
    "ds_write_b128 v46, v[192:195] offset:10240\n"
    "v_mfma_f32_16x16x32_bf16 v[148:151], a[172:175], a[88:91], v[148:151]\n"
    "ds_write_b64 v40, v[96:97] offset:6144\n"
    "v_mfma_f32_16x16x32_bf16 v[148:151], a[176:179], a[92:95], v[148:151]\n"
    "v_mfma_f32_16x16x32_bf16 v[148:151], a[180:183], a[244:247], v[148:151]\n"
    "ds_write_b64 v40, v[216:217] offset:7168\n"
    "v_mfma_f32_16x16x32_bf16 v[228:231], a[212:215], a[240:243], v[228:231]\n"
    "v_mfma_f32_16x16x32_bf16 v[228:231], a[216:219], a[8:11], v[228:231]\n"
    "ds_write_b64 v40, v[176:177] offset:4096\n"
    "v_mfma_f32_16x16x32_bf16 v[188:191], a[184:187], a[116:119], v[188:191]\n"
    "v_mul_f32 v108, s76, v108\n"
    "v_mul_f32 v109, s76, v109\n"
    "v_mfma_f32_16x16x32_bf16 v[188:191], a[188:191], a[120:123], v[188:191]\n"
    "v_mul_f32 v110, s76, v110\n"
    "v_mul_f32 v111, s76, v111\n"
    "v_mfma_f32_16x16x32_bf16 v[188:191], a[192:195], a[124:127], v[188:191]\n"
    "v_fma_f32 v148, -v148, s76, v108\n"
    "v_fma_f32 v149, -v149, s76, v109\n"
    "v_mfma_f32_16x16x32_bf16 v[188:191], a[196:199], a[220:223], v[188:191]\n"
    "v_fma_f32 v150, -v150, s76, v110\n"
    "v_fma_f32 v151, -v151, s76, v111\n"
    "v_mfma_f32_16x16x32_bf16 v[188:191], a[200:203], a[228:231], v[188:191]\n"
    "v_fma_f32 v108, v108, 2.0, -v148\n"
    "v_fma_f32 v109, v109, 2.0, -v149\n"
    "v_mfma_f32_16x16x32_bf16 v[188:191], a[204:207], a[232:235], v[188:191]\n"
    "v_fma_f32 v110, v110, 2.0, -v150\n"
    "v_fma_f32 v111, v111, 2.0, -v151\n"
    "v_mfma_f32_16x16x32_bf16 v[188:191], a[208:211], a[0:3], v[188:191]\n"
    "s_waitcnt lgkmcnt(0)\n"
    "s_barrier\n"
    "v_mfma_f32_16x16x32_bf16 v[120:123], a[128:131], a[24:27], v[120:123]\n"
    "ds_read_b128 v[112:115], v47 offset:4096\n"
    "ds_read_b128 v[152:155], v47 offset:5120\n"
    "v_mfma_f32_16x16x32_bf16 v[120:123], a[132:135], a[28:31], v[120:123]\n"
    "ds_read_b128 v[192:195], v47 offset:6144\n"
    "ds_read_b128 v[232:235], v47 offset:7168\n"
    "v_mfma_f32_16x16x32_bf16 v[120:123], a[136:139], a[32:35], v[120:123]\n"
    "ds_read_b128 v[48:51], v41 offset:0\n"
    "ds_read_b128 v[52:55], v41 offset:8192\n"
    "v_mfma_f32_16x16x32_bf16 v[120:123], a[140:143], a[36:39], v[120:123]\n"
    "ds_read_b128 v[56:59], v41 offset:4096\n"
    "v_mfma_f32_16x16x32_bf16 v[120:123], a[144:147], a[40:43], v[120:123]\n"
    "ds_read_b128 a[96:99], v18 offset:56672\n"
    "v_mfma_f32_16x16x32_bf16 v[120:123], a[148:151], a[44:47], v[120:123]\n"
    "ds_read_b128 a[104:107], v18 offset:57248\n"
    "v_mfma_f32_16x16x32_bf16 v[120:123], a[152:155], a[48:51], v[120:123]\n"
    "ds_read_b128 a[100:103], v19 offset:54208\n"
    "v_mfma_f32_16x16x32_bf16 v[160:163], a[156:159], a[84:87], v[160:163]\n"
    "s_waitcnt lgkmcnt(6)\n"
    "v_pk_fma_f32 v[232:233], v[228:229], s[76:77], v[232:233] op_sel_hi:[1,0,1]\n"
    "v_pk_fma_f32 v[234:235], v[230:231], s[76:77], v[234:235] op_sel_hi:[1,0,1]\n"
    "v_pk_fma_f32 v[232:233], v[148:149], s[84:85], v[232:233] op_sel_hi:[1,0,1]\n"
    "v_pk_fma_f32 v[234:235], v[150:151], s[84:85], v[234:235] op_sel_hi:[1,0,1]\n"
    "v_pk_fma_f32 v[152:153], v[148:149], 2.0, v[152:153] op_sel_hi:[1,0,1]\n"
    "v_pk_fma_f32 v[154:155], v[150:151], 2.0, v[154:155] op_sel_hi:[1,0,1]\n"
    "v_pk_fma_f32 v[112:113], v[108:109], 4.0, v[112:113] op_sel_hi:[1,0,1]\n"
    "v_pk_fma_f32 v[114:115], v[110:111], 4.0, v[114:115] op_sel_hi:[1,0,1]\n"
    "v_pk_add_f32 v[192:193], v[192:193], v[108:109]\n"
    "v_pk_add_f32 v[194:195], v[194:195], v[110:111]\n"
    "v_fmac_f32 v192, s76, v188\n"
    "v_fmac_f32 v193, s76, v189\n"
    "v_mfma_f32_16x16x32_bf16 v[160:163], a[160:163], a[88:91], v[160:163]\n"
    "s_waitcnt lgkmcnt(5)\n"
    "s_add_u32 s58, s53, 0\n"
    "buffer_store_dwordx4 v[48:51], v42, s[24:27], s58 offen sc0 sc1\n"
    "v_fmac_f32 v194, s76, v190\n"
    "v_mfma_f32_16x16x32_bf16 v[160:163], a[164:167], a[92:95], v[160:163]\n"
    "v_fmac_f32 v195, s76, v191\n"
    "v_fmac_f32 v152, s78, v188\n"
    "v_mfma_f32_16x16x32_bf16 v[160:163], a[168:171], a[244:247], v[160:163]\n"
    "s_waitcnt lgkmcnt(4)\n"
    "s_add_u32 s58, s53, 172032\n"
    "buffer_store_dwordx4 v[52:55], v42, s[24:27], s58 offen sc0 sc1\n"
    "v_fmac_f32 v153, s78, v189\n"
    "v_mfma_f32_16x16x32_bf16 v[160:163], a[172:175], a[248:251], v[160:163]\n"
    "v_fmac_f32 v154, s78, v190\n"
    "v_fmac_f32 v155, s78, v191\n"
    "v_mfma_f32_16x16x32_bf16 v[160:163], a[176:179], a[60:63], v[160:163]\n"
    "s_waitcnt lgkmcnt(3)\n"
    "s_add_u32 s58, s53, 86016\n"
    "buffer_store_dwordx4 v[56:59], v42, s[24:27], s58 offen sc0 sc1\n"
    "v_fmac_f32 v112, s80, v188\n"
    "v_mfma_f32_16x16x32_bf16 v[160:163], a[180:183], a[64:67], v[160:163]\n"
    "v_fmac_f32 v113, s80, v189\n"
    "v_fmac_f32 v114, s80, v190\n"
    "v_mfma_f32_16x16x32_bf16 v[240:243], a[212:215], a[252:255], v[240:243]\n"
    "v_fmac_f32 v115, s80, v191\n"
    "v_fmac_f32 v232, s82, v188\n"
    "v_mfma_f32_16x16x32_bf16 v[240:243], a[216:219], a[68:71], v[240:243]\n"
    "v_fmac_f32 v233, s82, v189\n"
    "v_fmac_f32 v234, s82, v190\n"
    "v_mfma_f32_16x16x32_bf16 v[200:203], a[184:187], a[220:223], v[200:203]\n"
    "v_fmac_f32 v235, s82, v191\n"
    "v_pk_mul_f32 v[120:121], v[120:121], s[76:77] op_sel_hi:[1,0]\n"
    "v_pk_mul_f32 v[122:123], v[122:123], s[76:77] op_sel_hi:[1,0]\n"
    "v_pk_fma_f32 v[160:161], v[160:161], s[76:77], v[120:121] op_sel_hi:[1,0,1] neg_lo:[1,0,0] neg_hi:[1,0,0]\n"
    "v_pk_fma_f32 v[162:163], v[162:163], s[76:77], v[122:123] op_sel_hi:[1,0,1] neg_lo:[1,0,0] neg_hi:[1,0,0]\n"
    "v_pk_fma_f32 v[120:121], v[120:121], 2.0, v[160:161] op_sel_hi:[1,0,1] neg_lo:[0,0,1] neg_hi:[0,0,1]\n"
    "v_pk_fma_f32 v[122:123], v[122:123], 2.0, v[162:163] op_sel_hi:[1,0,1] neg_lo:[0,0,1] neg_hi:[0,0,1]\n"
    "v_mfma_f32_16x16x32_bf16 v[200:203], a[188:191], a[228:231], v[200:203]\n"
    "v_cvt_pk_bf16_f32 v152, v152, v153\n"
    "v_cvt_pk_bf16_f32 v153, v154, v155\n"
    "v_mfma_f32_16x16x32_bf16 v[200:203], a[192:195], a[232:235], v[200:203]\n"
    "ds_write_b128 v46, v[160:163] offset:1024\n"
    "v_mfma_f32_16x16x32_bf16 v[200:203], a[196:199], a[0:3], v[200:203]\n"
    "v_fma_f32 v240, v240, s82, v160\n"
    "v_fma_f32 v241, v241, s82, v161\n"
    "v_mfma_f32_16x16x32_bf16 v[200:203], a[200:203], a[4:7], v[200:203]\n"
    "s_waitcnt lgkmcnt(1)\n"
    "v_fma_f32 v242, v242, s82, v162\n"
    "v_fma_f32 v243, v243, s82, v163\n"
    "v_mfma_f32_16x16x32_bf16 v[200:203], a[204:207], a[96:99], v[200:203]\n"
    "ds_write_b128 v46, v[120:123] offset:0\n"
    "v_mfma_f32_16x16x32_bf16 v[200:203], a[208:211], a[104:107], v[200:203]\n"
    "v_cvt_pk_bf16_f32 v112, v112, v113\n"
    "v_cvt_pk_bf16_f32 v113, v114, v115\n"
    "v_mfma_f32_16x16x32_bf16 v[116:119], a[128:131], a[20:23], v[116:119]\n"
    "v_cvt_pk_bf16_f32 v232, v232, v233\n"
    "v_cvt_pk_bf16_f32 v233, v234, v235\n"
    "v_mfma_f32_16x16x32_bf16 v[116:119], a[132:135], a[24:27], v[116:119]\n"
    "ds_write_b128 v46, v[240:243] offset:3072\n"
    "v_mfma_f32_16x16x32_bf16 v[116:119], a[136:139], a[28:31], v[116:119]\n"
    "v_cvt_pk_bf16_f32 v192, v192, v193\n"
    "v_cvt_pk_bf16_f32 v193, v194, v195\n"
    "v_mfma_f32_16x16x32_bf16 v[116:119], a[140:143], a[32:35], v[116:119]\n"
    "v_mfma_f32_16x16x32_bf16 v[116:119], a[144:147], a[36:39], v[116:119]\n"
    "ds_write_b64 v40, v[152:153] offset:13312\n"
    "v_mfma_f32_16x16x32_bf16 v[116:119], a[148:151], a[40:43], v[116:119]\n"
    "ds_write_b128 v46, v[200:203] offset:2048\n"
    "v_mfma_f32_16x16x32_bf16 v[116:119], a[152:155], a[44:47], v[116:119]\n"
    "ds_write_b64 v40, v[112:113] offset:14336\n"
    "v_mfma_f32_16x16x32_bf16 v[156:159], a[156:159], a[80:83], v[156:159]\n"
    "v_mfma_f32_16x16x32_bf16 v[156:159], a[160:163], a[84:87], v[156:159]\n"
    "ds_write_b64 v40, v[232:233] offset:15360\n"
    "v_mfma_f32_16x16x32_bf16 v[156:159], a[164:167], a[88:91], v[156:159]\n"
    "v_mfma_f32_16x16x32_bf16 v[156:159], a[168:171], a[92:95], v[156:159]\n"
    "ds_write_b64 v40, v[192:193] offset:12288\n"
    "v_mfma_f32_16x16x32_bf16 v[156:159], a[172:175], a[244:247], v[156:159]\n"
    "v_mfma_f32_16x16x32_bf16 v[156:159], a[176:179], a[248:251], v[156:159]\n"
    "v_mfma_f32_16x16x32_bf16 v[156:159], a[180:183], a[60:63], v[156:159]\n"
    "v_mfma_f32_16x16x32_bf16 v[236:239], a[212:215], a[8:11], v[236:239]\n"
    "s_waitcnt lgkmcnt(0)\n"
    "s_barrier\n"
    "v_mfma_f32_16x16x32_bf16 v[236:239], a[216:219], a[100:103], v[236:239]\n"
    "ds_read_b128 v[120:123], v47 offset:8192\n"
    "ds_read_b128 v[160:163], v47 offset:9216\n"
    "v_mfma_f32_16x16x32_bf16 v[196:199], a[184:187], a[124:127], v[196:199]\n"
    "ds_read_b128 v[200:203], v47 offset:10240\n"
    "ds_read_b128 v[240:243], v47 offset:11264\n"
    "v_mfma_f32_16x16x32_bf16 v[196:199], a[188:191], a[220:223], v[196:199]\n"
    "ds_read_b128 v[60:63], v41 offset:16384\n"
    "ds_read_b128 v[64:67], v41 offset:12288\n"
    "v_mfma_f32_16x16x32_bf16 v[196:199], a[192:195], a[228:231], v[196:199]\n"
    "ds_read_b128 a[236:239], v18 offset:57744\n"
    "v_mul_f32 v116, s76, v116\n"
    "v_mfma_f32_16x16x32_bf16 v[196:199], a[196:199], a[232:235], v[196:199]\n"
    "ds_read_b128 a[108:111], v18 offset:58320\n"
    "v_mul_f32 v117, s76, v117\n"
    "v_mfma_f32_16x16x32_bf16 v[196:199], a[200:203], a[0:3], v[196:199]\n"
    "ds_read_b128 a[112:115], v19 offset:55936\n"
    "v_mul_f32 v118, s76, v118\n"
    "v_mfma_f32_16x16x32_bf16 v[196:199], a[204:207], a[4:7], v[196:199]\n"
    "ds_read_b128 a[224:227], v18 offset:57824\n"
    "v_mul_f32 v119, s76, v119\n"
    "v_mfma_f32_16x16x32_bf16 v[196:199], a[208:211], a[96:99], v[196:199]\n"
    "s_waitcnt lgkmcnt(5)\n"
    "s_add_u32 s58, s53, 344064\n"
    "buffer_store_dwordx4 v[60:63], v42, s[24:27], s58 offen sc0 sc1\n"
    "ds_read_b128 a[12:15], v18 offset:58400\n"
    "v_mfma_f32_16x16x32_bf16 v[128:131], a[128:131], a[32:35], v[128:131]\n"
    "ds_read_b128 a[16:19], v19 offset:55360\n"
    "v_fma_f32 v156, -v156, s76, v116\n"
    "v_mfma_f32_16x16x32_bf16 v[128:131], a[132:135], a[36:39], v[128:131]\n"
    "s_waitcnt lgkmcnt(6)\n"
    "s_add_u32 s58, s53, 258048\n"
    "buffer_store_dwordx4 v[64:67], v42, s[24:27], s58 offen sc0 sc1\n"
    "v_fma_f32 v157, -v157, s76, v117\n"
    "v_mfma_f32_16x16x32_bf16 v[128:131], a[136:139], a[40:43], v[128:131]\n"
    "v_fma_f32 v158, -v158, s76, v118\n"
    "v_fma_f32 v159, -v159, s76, v119\n"
    "v_mfma_f32_16x16x32_bf16 v[128:131], a[140:143], a[44:47], v[128:131]\n"
    "v_fma_f32 v116, v116, 2.0, -v156\n"
    "v_fma_f32 v117, v117, 2.0, -v157\n"
    "v_mfma_f32_16x16x32_bf16 v[128:131], a[144:147], a[48:51], v[128:131]\n"
    "v_fma_f32 v118, v118, 2.0, -v158\n"
    "v_fma_f32 v119, v119, 2.0, -v159\n"
    "v_pk_fma_f32 v[240:241], v[236:237], s[76:77], v[240:241] op_sel_hi:[1,0,1]\n"
    "v_pk_fma_f32 v[242:243], v[238:239], s[76:77], v[242:243] op_sel_hi:[1,0,1]\n"
    "v_pk_fma_f32 v[240:241], v[156:157], s[84:85], v[240:241] op_sel_hi:[1,0,1]\n"
    "v_pk_fma_f32 v[242:243], v[158:159], s[84:85], v[242:243] op_sel_hi:[1,0,1]\n"
    "v_pk_fma_f32 v[160:161], v[156:157], 2.0, v[160:161] op_sel_hi:[1,0,1]\n"
    "v_pk_fma_f32 v[162:163], v[158:159], 2.0, v[162:163] op_sel_hi:[1,0,1]\n"
    "v_pk_fma_f32 v[120:121], v[116:117], 4.0, v[120:121] op_sel_hi:[1,0,1]\n"
    "v_pk_fma_f32 v[122:123], v[118:119], 4.0, v[122:123] op_sel_hi:[1,0,1]\n"
    "v_pk_add_f32 v[200:201], v[200:201], v[116:117]\n"
    "v_pk_add_f32 v[202:203], v[202:203], v[118:119]\n"
    "v_mfma_f32_16x16x32_bf16 v[128:131], a[148:151], a[52:55], v[128:131]\n"
    "v_fmac_f32 v200, s76, v196\n"
    "v_fmac_f32 v201, s76, v197\n"
    "v_mfma_f32_16x16x32_bf16 v[128:131], a[152:155], a[56:59], v[128:131]\n"
    "v_fmac_f32 v202, s76, v198\n"
    "v_fmac_f32 v203, s76, v199\n"
    "v_mfma_f32_16x16x32_bf16 v[168:171], a[156:159], a[92:95], v[168:171]\n"
    "v_fmac_f32 v160, s78, v196\n"
    "v_fmac_f32 v161, s78, v197\n"
    "v_mfma_f32_16x16x32_bf16 v[168:171], a[160:163], a[244:247], v[168:171]\n"
    "v_fmac_f32 v162, s78, v198\n"
    "v_fmac_f32 v163, s78, v199\n"
    "v_mfma_f32_16x16x32_bf16 v[168:171], a[164:167], a[248:251], v[168:171]\n"
    "v_fmac_f32 v120, s80, v196\n"
    "v_fmac_f32 v121, s80, v197\n"
    "v_mfma_f32_16x16x32_bf16 v[168:171], a[168:171], a[60:63], v[168:171]\n"
    "v_fmac_f32 v122, s80, v198\n"
    "v_fmac_f32 v123, s80, v199\n"
    "v_mfma_f32_16x16x32_bf16 v[168:171], a[172:175], a[64:67], v[168:171]\n"
    "s_waitcnt lgkmcnt(0)\n"
    "v_fmac_f32 v240, s82, v196\n"
    "v_fmac_f32 v241, s82, v197\n"
    "v_mfma_f32_16x16x32_bf16 v[168:171], a[176:179], a[236:239], v[168:171]\n"
    "v_fmac_f32 v242, s82, v198\n"
    "v_fmac_f32 v243, s82, v199\n"
    "v_mfma_f32_16x16x32_bf16 v[168:171], a[180:183], a[108:111], v[168:171]\n"
    "v_cvt_pk_bf16_f32 v160, v160, v161\n"
    "v_cvt_pk_bf16_f32 v161, v162, v163\n"
    "v_mfma_f32_16x16x32_bf16 v[248:251], a[212:215], a[68:71], v[248:251]\n"
    "v_cvt_pk_bf16_f32 v120, v120, v121\n"
    "v_cvt_pk_bf16_f32 v121, v122, v123\n"
    "v_mfma_f32_16x16x32_bf16 v[248:251], a[216:219], a[112:115], v[248:251]\n"
    "ds_write_b64 v40, v[160:161] offset:21504\n"
    "v_mfma_f32_16x16x32_bf16 v[208:211], a[184:187], a[232:235], v[208:211]\n"
    "v_pk_mul_f32 v[128:129], v[128:129], s[76:77] op_sel_hi:[1,0]\n"
    "v_pk_mul_f32 v[130:131], v[130:131], s[76:77] op_sel_hi:[1,0]\n"
    "v_pk_fma_f32 v[168:169], v[168:169], s[76:77], v[128:129] op_sel_hi:[1,0,1] neg_lo:[1,0,0] neg_hi:[1,0,0]\n"
    "v_pk_fma_f32 v[170:171], v[170:171], s[76:77], v[130:131] op_sel_hi:[1,0,1] neg_lo:[1,0,0] neg_hi:[1,0,0]\n"
    "v_pk_fma_f32 v[128:129], v[128:129], 2.0, v[168:169] op_sel_hi:[1,0,1] neg_lo:[0,0,1] neg_hi:[0,0,1]\n"
    "v_pk_fma_f32 v[130:131], v[130:131], 2.0, v[170:171] op_sel_hi:[1,0,1] neg_lo:[0,0,1] neg_hi:[0,0,1]\n"
    "v_cvt_pk_bf16_f32 v240, v240, v241\n"
    "v_cvt_pk_bf16_f32 v241, v242, v243\n"
    "v_mfma_f32_16x16x32_bf16 v[208:211], a[188:191], a[0:3], v[208:211]\n"
    "ds_write_b64 v40, v[120:121] offset:22528\n"
    "v_mfma_f32_16x16x32_bf16 v[208:211], a[192:195], a[4:7], v[208:211]\n"
    "ds_write_b128 v46, v[168:171] offset:5120\n"
    "v_mfma_f32_16x16x32_bf16 v[208:211], a[196:199], a[96:99], v[208:211]\n"
    "v_fma_f32 v248, v248, s82, v168\n"
    "v_fma_f32 v249, v249, s82, v169\n"
    "v_mfma_f32_16x16x32_bf16 v[208:211], a[200:203], a[104:107], v[208:211]\n"
    "v_fma_f32 v250, v250, s82, v170\n"
    "v_fma_f32 v251, v251, s82, v171\n"
    "v_mfma_f32_16x16x32_bf16 v[208:211], a[204:207], a[224:227], v[208:211]\n"
    "ds_write_b128 v46, v[128:131] offset:4096\n"
    "v_mfma_f32_16x16x32_bf16 v[208:211], a[208:211], a[12:15], v[208:211]\n"
    "v_cvt_pk_bf16_f32 v200, v200, v201\n"
    "v_cvt_pk_bf16_f32 v201, v202, v203\n"
    "v_mfma_f32_16x16x32_bf16 v[124:127], a[128:131], a[28:31], v[124:127]\n"
    "v_mfma_f32_16x16x32_bf16 v[124:127], a[132:135], a[32:35], v[124:127]\n"
    "ds_write_b128 v46, v[248:251] offset:7168\n"
    "v_mfma_f32_16x16x32_bf16 v[124:127], a[136:139], a[36:39], v[124:127]\n"
    "v_mfma_f32_16x16x32_bf16 v[124:127], a[140:143], a[40:43], v[124:127]\n"
    "v_mfma_f32_16x16x32_bf16 v[124:127], a[144:147], a[44:47], v[124:127]\n"
    "ds_write_b64 v40, v[240:241] offset:23552\n"
    "v_mfma_f32_16x16x32_bf16 v[124:127], a[148:151], a[48:51], v[124:127]\n"
    "ds_write_b128 v46, v[208:211] offset:6144\n"
    "v_mfma_f32_16x16x32_bf16 v[124:127], a[152:155], a[52:55], v[124:127]\n"
    "ds_write_b64 v40, v[200:201] offset:20480\n"
    "v_mfma_f32_16x16x32_bf16 v[164:167], a[156:159], a[88:91], v[164:167]\n"
    "v_mfma_f32_16x16x32_bf16 v[164:167], a[160:163], a[92:95], v[164:167]\n"
    "v_mfma_f32_16x16x32_bf16 v[164:167], a[164:167], a[244:247], v[164:167]\n"
    "v_mfma_f32_16x16x32_bf16 v[164:167], a[168:171], a[248:251], v[164:167]\n"
    "v_mfma_f32_16x16x32_bf16 v[164:167], a[172:175], a[60:63], v[164:167]\n"
    "v_mfma_f32_16x16x32_bf16 v[164:167], a[176:179], a[64:67], v[164:167]\n"
    "s_waitcnt lgkmcnt(0)\n"
    "s_barrier\n"
    "v_mfma_f32_16x16x32_bf16 v[164:167], a[180:183], a[236:239], v[164:167]\n"
    "ds_read_b128 v[128:131], v47 offset:0\n"
    "ds_read_b128 v[168:171], v47 offset:1024\n"
    "v_mfma_f32_16x16x32_bf16 v[244:247], a[212:215], a[100:103], v[244:247]\n"
    "ds_read_b128 v[208:211], v47 offset:2048\n"
    "ds_read_b128 v[248:251], v47 offset:3072\n"
    "v_mfma_f32_16x16x32_bf16 v[244:247], a[216:219], a[16:19], v[244:247]\n"
    "ds_read_b128 v[68:71], v41 offset:24576\n"
    "ds_read_b128 v[72:75], v41 offset:20480\n"
    "v_mfma_f32_16x16x32_bf16 v[204:207], a[184:187], a[228:231], v[204:207]\n"
    "v_pk_mul_f32 v[124:125], v[124:125], s[76:77] op_sel_hi:[1,0]\n"
    "v_pk_mul_f32 v[126:127], v[126:127], s[76:77] op_sel_hi:[1,0]\n"
    "v_pk_fma_f32 v[164:165], v[164:165], s[76:77], v[124:125] op_sel_hi:[1,0,1] neg_lo:[1,0,0] neg_hi:[1,0,0]\n"
    "v_pk_fma_f32 v[166:167], v[166:167], s[76:77], v[126:127] op_sel_hi:[1,0,1] neg_lo:[1,0,0] neg_hi:[1,0,0]\n"
    "v_pk_fma_f32 v[124:125], v[124:125], 2.0, v[164:165] op_sel_hi:[1,0,1] neg_lo:[0,0,1] neg_hi:[0,0,1]\n"
    "v_pk_fma_f32 v[126:127], v[126:127], 2.0, v[166:167] op_sel_hi:[1,0,1] neg_lo:[0,0,1] neg_hi:[0,0,1]\n"
    "v_mfma_f32_16x16x32_bf16 v[204:207], a[188:191], a[232:235], v[204:207]\n"
    "v_mfma_f32_16x16x32_bf16 v[204:207], a[192:195], a[0:3], v[204:207]\n"
    "v_mfma_f32_16x16x32_bf16 v[204:207], a[196:199], a[4:7], v[204:207]\n"
    "v_mfma_f32_16x16x32_bf16 v[204:207], a[200:203], a[96:99], v[204:207]\n"
    "s_waitcnt lgkmcnt(1)\n"
    "s_add_u32 s58, s53, 516096\n"
    "buffer_store_dwordx4 v[68:71], v42, s[24:27], s58 offen sc0 sc1\n"
    "v_pk_fma_f32 v[248:249], v[244:245], s[76:77], v[248:249] op_sel_hi:[1,0,1]\n"
    "v_pk_fma_f32 v[250:251], v[246:247], s[76:77], v[250:251] op_sel_hi:[1,0,1]\n"
    "v_pk_fma_f32 v[248:249], v[164:165], s[84:85], v[248:249] op_sel_hi:[1,0,1]\n"
    "v_pk_fma_f32 v[250:251], v[166:167], s[84:85], v[250:251] op_sel_hi:[1,0,1]\n"
    "v_pk_fma_f32 v[168:169], v[164:165], 2.0, v[168:169] op_sel_hi:[1,0,1]\n"
    "v_pk_fma_f32 v[170:171], v[166:167], 2.0, v[170:171] op_sel_hi:[1,0,1]\n"
    "v_pk_fma_f32 v[128:129], v[124:125], 4.0, v[128:129] op_sel_hi:[1,0,1]\n"
    "v_pk_fma_f32 v[130:131], v[126:127], 4.0, v[130:131] op_sel_hi:[1,0,1]\n"
    "v_pk_add_f32 v[208:209], v[208:209], v[124:125]\n"
    "v_pk_add_f32 v[210:211], v[210:211], v[126:127]\n"
    "v_mfma_f32_16x16x32_bf16 v[204:207], a[204:207], a[104:107], v[204:207]\n"
    "v_mfma_f32_16x16x32_bf16 v[204:207], a[208:211], a[224:227], v[204:207]\n"
    "s_waitcnt lgkmcnt(0)\n"
    "s_add_u32 s58, s53, 430080\n"
    "buffer_store_dwordx4 v[72:75], v42, s[24:27], s58 offen sc0 sc1\n"
    "s_nop 7\n"
    "v_pk_fma_f32 v[208:209], v[204:205], s[76:77], v[208:209] op_sel_hi:[1,0,1]\n"
    "v_pk_fma_f32 v[210:211], v[206:207], s[76:77], v[210:211] op_sel_hi:[1,0,1]\n"
    "v_pk_fma_f32 v[168:169], v[204:205], s[78:79], v[168:169] op_sel_hi:[1,0,1]\n"
    "v_pk_fma_f32 v[170:171], v[206:207], s[78:79], v[170:171] op_sel_hi:[1,0,1]\n"
    "v_pk_fma_f32 v[128:129], v[204:205], s[80:81], v[128:129] op_sel_hi:[1,0,1]\n"
    "v_pk_fma_f32 v[130:131], v[206:207], s[80:81], v[130:131] op_sel_hi:[1,0,1]\n"
    "v_pk_fma_f32 v[248:249], v[204:205], s[82:83], v[248:249] op_sel_hi:[1,0,1]\n"
    "v_pk_fma_f32 v[250:251], v[206:207], s[82:83], v[250:251] op_sel_hi:[1,0,1]\n"
    "v_cvt_pk_bf16_f32 v168, v168, v169\n"
    "v_cvt_pk_bf16_f32 v169, v170, v171\n"
    "v_cvt_pk_bf16_f32 v128, v128, v129\n"
    "v_cvt_pk_bf16_f32 v129, v130, v131\n"
    "v_cvt_pk_bf16_f32 v248, v248, v249\n"
    "v_cvt_pk_bf16_f32 v249, v250, v251\n"
    "v_cvt_pk_bf16_f32 v208, v208, v209\n"
    "v_cvt_pk_bf16_f32 v209, v210, v211\n"
    "ds_write_b64 v40, v[168:169] offset:29696\n"
    "ds_write_b64 v40, v[128:129] offset:30720\n"
    "ds_write_b64 v40, v[248:249] offset:31744\n"
    "ds_write_b64 v40, v[208:209] offset:28672\n"
    "s_waitcnt lgkmcnt(0)\n"
    "s_barrier\n"
    "ds_read_b128 v[96:99], v41 offset:32768\n"
    "ds_read_b128 v[100:103], v41 offset:28672\n"
    "s_add_u32 s58, s53, 688128\n"
    "s_waitcnt lgkmcnt(1)\n"
    "buffer_store_dwordx4 v[96:99], v42, s[24:27], s58 offen sc0 sc1\n"
    "s_add_u32 s58, s53, 602112\n"
    "s_waitcnt lgkmcnt(0)\n"
    "buffer_store_dwordx4 v[100:103], v42, s[24:27], s58 offen sc0 sc1\n"
    "s_endpgm\n"
    "15:\n"
    "s_mov_b32 s80, 0x40000\n"
    "18:\n"
    "buffer_load_dword v36, off, s[28:31], s74 offset:2048 sc1\n"
    "s_waitcnt vmcnt(0)\n"
    "v_cmp_eq_u32 vcc, v36, v38\n"
    "s_cbranch_vccz 16b\n"
    "s_sleep 4\n"
    "s_sub_u32 s80, s80, 1\n"
    "s_cmp_lg_u32 s80, 0\n"
    "s_cbranch_scc1 18b\n"
    "s_branch 16b\n"
    "151:\n"
    "s_mov_b32 s80, 0x40000\n"
    "181:\n"
    "s_and_b32 s81, s80, 7\n"
    "s_cbranch_scc1 19f\n"
    "s_mov_b64 exec, 1\n"
    "buffer_store_dword v39, v3, s[28:31], s61 offen sc0 sc1\n"
    "s_mov_b64 exec, -1\n"
    "19:\n"
    "buffer_load_dword v22, v17, s[28:31], s74 offen sc1\n"
    "s_waitcnt vmcnt(0)\n"
    "v_cmp_eq_u32 vcc, 0, v22\n"
    "s_cbranch_vccz 161b\n"
    "s_sleep 4\n"
    "s_sub_u32 s80, s80, 1\n"
    "s_cmp_lg_u32 s80, 0\n"
    "s_cbranch_scc1 181b\n"
    "s_branch 161b\n"
    "152:\n"
    "s_mov_b32 s80, 0x40000\n"
    "182:\n"
    "buffer_load_dwordx2 v[22:23], v27, s[28:31], s74 offen offset:128 sc1\n"
    "s_waitcnt vmcnt(0)\n"
    "v_min_u32 v22, v22, v23\n"
    "v_cmp_eq_u32 vcc, 0, v22\n"
    "s_cbranch_vccz 162b\n"
    "s_sleep 4\n"
    "s_sub_u32 s80, s80, 1\n"
    "s_cmp_lg_u32 s80, 0\n"
    "s_cbranch_scc1 182b\n"
    "s_branch 162b\n"
    "91:\n"
    :
    : [tid] "v"(threadIdx.x), [t] "s"(t), [ka] "s"(__builtin_amdgcn_kernarg_segment_ptr()), [tab] "s"(lane_tab), [lds] "s"((unsigned)(size_t)lds)
    : "v3", "v4", "v5", "v6", "v7", "v8", "v9", "v10", "v11", "v12", "v13", "v14", "v15", "v16", "v17", "v18", "v19", "v20", "v21", "v22", "v23", "v24", "v25", "v26", "v27", "v28", "v29", "v30", "v31", "v32", "v33", "v34", "v35", "v36", "v37", "v38", "v39", "v40", "v41", "v42", "v43", "v44", "v45", "v46", "v47", "v48", "v49", "v50", "v51", "v52", "v53", "v54", "v55", "v56", "v57", "v58", "v59", "v60", "v61", "v62", "v63", "v64", "v65", "v66", "v67", "v68", "v69", "v70", "v71", "v72", "v73", "v74", "v75", "v76", "v77", "v78", "v79", "v80", "v81", "v82", "v83", "v84", "v85", "v86", "v87", "v88", "v89", "v90", "v91", "v92", "v93", "v94", "v95", "v96", "v97", "v98", "v99", "v100", "v101", "v102", "v103", "v104", "v105", "v106", "v107", "v108", "v109", "v110", "v111", "v112", "v113", "v114", "v115", "v116", "v117", "v118", "v119", "v120", "v121", "v122", "v123", "v124", "v125", "v126", "v127", "v128", "v129", "v130", "v131", "v132", "v133", "v134", "v135", "v136", "v137", "v138", "v139", "v140", "v141", "v142", "v143", "v144", "v145", "v146", "v147", "v148", "v149", "v150", "v151", "v152", "v153", "v154", "v155", "v156", "v157", "v158", "v159", "v160", "v161", "v162", "v163", "v164", "v165", "v166", "v167", "v168", "v169", "v170", "v171", "v172", "v173", "v174", "v175", "v176", "v177", "v178", "v179", "v180", "v181", "v182", "v183", "v184", "v185", "v186", "v187", "v188", "v189", "v190", "v191", "v192", "v193", "v194", "v195", "v196", "v197", "v198", "v199", "v200", "v201", "v202", "v203", "v204", "v205", "v206", "v207", "v208", "v209", "v210", "v211", "v212", "v213", "v214", "v215", "v216", "v217", "v218", "v219", "v220", "v221", "v222", "v223", "v224", "v225", "v226", "v227", "v228", "v229", "v230", "v231", "v232", "v233", "v234", "v235", "v236", "v237", "v238", "v239", "v240", "v241", "v242", "v243", "v244", "v245", "v246", "v247", "v248", "v249", "v250", "v251", "v252", "v253", "v254", "v255", "a0", "a1", "a2", "a3", "a4", "a5", "a6", "a7", "a8", "a9", "a10", "a11", "a12", "a13", "a14", "a15", "a16", "a17", "a18", "a19", "a20", "a21", "a22", "a23", "a24", "a25", "a26", "a27", "a28", "a29", "a30", "a31", "a32", "a33", "a34", "a35", "a36", "a37", "a38", "a39", "a40", "a41", "a42", "a43", "a44", "a45", "a46", "a47", "a48", "a49", "a50", "a51", "a52", "a53", "a54", "a55", "a56", "a57", "a58", "a59", "a60", "a61", "a62", "a63", "a64", "a65", "a66", "a67", "a68", "a69", "a70", "a71", "a72", "a73", "a74", "a75", "a76", "a77", "a78", "a79", "a80", "a81", "a82", "a83", "a84", "a85", "a86", "a87", "a88", "a89", "a90", "a91", "a92", "a93", "a94", "a95", "a96", "a97", "a98", "a99", "a100", "a101", "a102", "a103", "a104", "a105", "a106", "a107", "a108", "a109", "a110", "a111", "a112", "a113", "a114", "a115", "a116", "a117", "a118", "a119", "a120", "a121", "a122", "a123", "a124", "a125", "a126", "a127", "a128", "a129", "a130", "a131", "a132", "a133", "a134", "a135", "a136", "a137", "a138", "a139", "a140", "a141", "a142", "a143", "a144", "a145", "a146", "a147", "a148", "a149", "a150", "a151", "a152", "a153", "a154", "a155", "a156", "a157", "a158", "a159", "a160", "a161", "a162", "a163", "a164", "a165", "a166", "a167", "a168", "a169", "a170", "a171", "a172", "a173", "a174", "a175", "a176", "a177", "a178", "a179", "a180", "a181", "a182", "a183", "a184", "a185", "a186", "a187", "a188", "a189", "a190", "a191", "a192", "a193", "a194", "a195", "a196", "a197", "a198", "a199", "a200", "a201", "a202", "a203", "a204", "a205", "a206", "a207", "a208", "a209", "a210", "a211", "a212", "a213", "a214", "a215", "a216", "a217", "a218", "a219", "a220", "a221", "a222", "a223", "a224", "a225", "a226", "a227", "a228", "a229", "a230", "a231", "a232", "a233", "a234", "a235", "a236", "a237", "a238", "a239", "a240", "a241", "a242", "a243", "a244", "a245", "a246", "a247", "a248", "a249", "a250", "a251", "a252", "a253", "a254", "a255", "s12", "s13", "s14", "s15", "s16", "s17", "s18", "s19", "s20", "s21", "s22", "s23", "s24", "s25", "s26", "s27", "s28", "s29", "s30", "s31", "s36", "s37", "s38", "s39", "s40", "s41", "s42", "s43", "s44", "s45", "s46", "s47", "s48", "s49", "s50", "s51", "s52", "s53", "s54", "s55", "s56", "s57", "s58", "s59", "s60", "s61", "s62", "s63", "s64", "s65", "s66", "s67", "s68", "s69", "s70", "s71", "s72", "s73", "s74", "s75", "s76", "s77", "s78", "s79", "s80", "s81", "s82", "s83", "s84", "s85", "s86", "s87", "s88", "s89", "s90", "s91", "s92", "s93", "s94", "s95", "s96", "s97", "s98", "s99", "scc", "vcc", "memory");
}
extern "C" void kernel_launch(void* const* d_in, const int* in_sizes, int n_in, void* d_out, int out_size, void* d_ws,
                              size_t ws_size, hipStream_t stream) {
  conv_kernel<<<256, 256, 0, stream>>>((const char*)d_in[0], (const char*)d_in[1], (char*)d_out, (char*)d_ws);
}
```
